```python
import torch

def _softmax(x, axis=-1):
    return torch.softmax(x, dim=axis)
def _gelu(x):
    return torch.nn.functional.gelu(x, approximate="tanh")
def _silu(x):
    return torch.nn.functional.silu(x)
def _sigmoid(x):
    return torch.sigmoid(x)
def _layernorm(x, axis=-1, eps=1e-5):
    m = x.mean(dim=axis, keepdim=True)
    v = x.var(dim=axis, keepdim=True, unbiased=False)
    return (x - m) / torch.sqrt(v + eps)
def _scatter_add(tgt, idx, src, axis=0):
    return tgt.clone().index_add_(axis, idx, src)
def _segment_sum(src, seg, n):
    out = torch.zeros((n,) + src.shape[1:], dtype=src.dtype)
    return out.index_add_(0, seg, src)
def _pad(x, pw):
    flat = tuple(v for pair in reversed(pw) for v in pair)
    return torch.nn.functional.pad(x, flat)

kernel_name = 'gated_mix__grouped_kv_mqa__moe_arch_mod__prompt'

def setup_inputs():

    _w = (lambda a: (lambda t: t.to(torch.bfloat16) if t.is_floating_point() else t)(torch.as_tensor(a)))
    def _A_setup():
        B, S, D, H = 1, 2048, 512, 32
        Dh = D // H
        return {
            "x": _w((lambda *s: torch.randn(*s, dtype=torch.bfloat16) * 0.05)(B, S, D)),
            "wq": _w((lambda *s: torch.randn(*s, dtype=torch.bfloat16) * 1.6)(D, H * Dh)),
            "wk": _w((lambda *s: torch.randn(*s, dtype=torch.bfloat16) * 1.6)(D, Dh)),
            "wv": _w((lambda *s: torch.randn(*s, dtype=torch.bfloat16) * 0.1)(D, Dh)),
            "wo": _w((lambda *s: torch.randn(*s, dtype=torch.bfloat16) * 0.1)(H * Dh, D)),
        }
    _A_in = _A_setup()

    def _B_setup():
        B, S, D, F, C = 1, 2048, 512, 1792, 1
        return {
            "x": _w((lambda *s: torch.randn(*s, dtype=torch.bfloat16) * 0.05)(B, S, D)),
            "w_r": _w((lambda *s: torch.randn(*s, dtype=torch.bfloat16) * 3.2)(D)),
            "w_gate": _w((lambda *s: torch.randn(*s, dtype=torch.bfloat16) * 0.2)(D, F)),
            "w_up": _w((lambda *s: torch.randn(*s, dtype=torch.bfloat16) * 0.2)(D, F)),
            "w_down": _w((lambda *s: torch.randn(*s, dtype=torch.bfloat16) * 0.2)(F, D)),
        }
    _B_in = _B_setup()

    return {
        "A_x": _A_in["x"],
        "A_wq": _A_in["wq"],
        "A_wk": _A_in["wk"],
        "A_wv": _A_in["wv"],
        "A_wo": _A_in["wo"],
        "B_x": _B_in["x"],
        "B_w_r": _B_in["w_r"],
        "B_w_gate": _B_in["w_gate"],
        "B_w_up": _B_in["w_up"],
        "B_w_down": _B_in["w_down"],
        "gate_w": _w((lambda *s: torch.randn(*s, dtype=torch.bfloat16) * 0.05)(512) * 16.0),
    }

def reference(A_x, A_wq, A_wk, A_wv, A_wo, B_x, B_w_r, B_w_gate, B_w_up, B_w_down, gate_w):
    def _A_ref(x, wq, wk, wv, wo):
        B, S, D = x.shape
        H = 32
        Dh = D // H
        q = (lambda x, *d: x.permute(*d))((x @ wq).reshape(B, S, H, Dh), 0, 2, 1, 3)
        k = x @ wk
        v = x @ wv
        scores = torch.einsum("bhqd,bkd->bhqk", q, k) * (Dh ** -0.5)
        p = _softmax(scores, axis=-1)
        o = torch.einsum("bhqk,bkd->bhqd", p, v)
        return (lambda x, *d: x.permute(*d))(o, 0, 2, 1, 3).reshape(B, S, H * Dh) @ wo

    def _B_ref(x, w_r, w_gate, w_up, w_down):
        B, S, D = x.shape
        C = 1
        r = x @ w_r
        idx = torch.argsort(-r, axis=-1)[:, :C]
        idx_e = (lambda x, n, axis: torch.repeat_interleave(x, n, dim=axis))(idx.reshape(B, C, 1), D, axis=2)
        x_sel = (lambda x, i, axis: torch.take_along_dim(x, i, dim=axis))(x, idx_e, axis=1)
        g = _silu(x_sel @ w_gate)
        u = x_sel @ w_up
        y = (g * u) @ w_down
        r_sel = _sigmoid((lambda x, i, axis: torch.take_along_dim(x, i, dim=axis))(r, idx, axis=1))
        y = y * r_sel.reshape(B, C, 1)
        b_off = (torch.arange(B) * S).reshape(B, 1)
        flat_idx = (idx + b_off).reshape(B * C)
        zero = (x * 0.0).reshape(B * S, D)
        upd = _scatter_add(zero, flat_idx, y.reshape(B * C, D), axis=0)
        return x + upd.reshape(B, S, D)

    x = A_x
    g = _sigmoid((x * gate_w).sum(-1, keepdims=True))
    a = _A_ref(x, A_wq, A_wk, A_wv, A_wo)
    b = _B_ref(x, B_w_r, B_w_gate, B_w_up, B_w_down)
    return g * a + (1 - g) * b
```

```cpp
#include <hip/hip_runtime.h>
#include <stdint.h>
#define PRIO_STEP 5

typedef unsigned short u16;
typedef __attribute__((ext_vector_type(8))) __bf16 bf16x8;
typedef __attribute__((ext_vector_type(16))) __bf16 bf16x16;
typedef __attribute__((ext_vector_type(2))) __bf16 bf16x2;
typedef __attribute__((ext_vector_type(4))) short s16x4;
typedef __attribute__((ext_vector_type(2))) float f32x2;
typedef __attribute__((ext_vector_type(4))) float f32x4;
typedef __attribute__((ext_vector_type(8))) float f32x8;
typedef __attribute__((ext_vector_type(16))) float f32x16;
typedef __attribute__((ext_vector_type(4))) unsigned u32x4;
typedef __attribute__((ext_vector_type(2))) unsigned u32x2;
#define LAS __attribute__((address_space(3)))
#define GAS __attribute__((address_space(1)))
#define RLX_AGENT __ATOMIC_RELAXED, __HIP_MEMORY_SCOPE_AGENT
#define RLX_WG __ATOMIC_RELAXED, __HIP_MEMORY_SCOPE_WORKGROUP

constexpr unsigned WS_EPOCH = 0;
constexpr unsigned WS_F1   = 4096;
constexpr unsigned WS_F2   = 16384;
constexpr unsigned WS_KF   = 32768;
constexpr unsigned WS_VF   = 98304;
constexpr unsigned WS_WOF  = 163840;
constexpr unsigned WS_OF   = 688128;
constexpr unsigned WS_GATE = 2785280;
constexpr unsigned WS_HG   = 2793472;
constexpr unsigned WS_YG   = 2801664;
constexpr unsigned WS_END  = 2834432;
constexpr unsigned WS_DBG  = 4194304;

constexpr unsigned REC     = 2080;
constexpr unsigned L_WKV   = 0;
constexpr unsigned L_WOST  = 16384;
constexpr unsigned L_XT    = 24960;
constexpr unsigned L_WQ    = 91520;
constexpr unsigned L_FW1   = 133120;
constexpr unsigned L_FW2   = 149504;
constexpr unsigned L_QX    = 157056;
constexpr unsigned L_MISC  = 161152;
constexpr unsigned L_C2 = L_MISC + 0, L_C4 = L_MISC + 4, L_C5 = L_MISC + 8, L_C6 = L_MISC + 12, L_C7 = L_MISC + 16,
                   L_TAG = L_MISC + 24  , L_KEY = L_MISC + 32;
constexpr unsigned L_XSEL  = L_MISC + 128;
constexpr unsigned L_HBUF  = L_XSEL + 1024;
constexpr unsigned LDS_BYTES = 163840;
constexpr int SPIN_CAP = 1 << 18;

__device__ __forceinline__ unsigned pk2(float a, float b) {
    f32x2 v = {a, b};
    return __builtin_bit_cast(unsigned, __builtin_convertvector(v, bf16x2));
}
__device__ __forceinline__ float bfr(float a) {
    return __builtin_bit_cast(float, pk2(a, a) & 0xffff0000u);
}
__device__ __forceinline__ unsigned f2u(float f) { return __builtin_bit_cast(unsigned, f); }
__device__ __forceinline__ float u2f(unsigned u) { return __builtin_bit_cast(float, u); }
__device__ __forceinline__ float bflo(unsigned u) { return __builtin_bit_cast(float, u << 16); }
__device__ __forceinline__ float bfhi(unsigned u) { return __builtin_bit_cast(float, u & 0xffff0000u); }
__device__ __forceinline__ float sigmoidf_(float z) { return __builtin_amdgcn_rcpf(1.f + __builtin_amdgcn_exp2f(-1.4426950408889634f * z)); }

__device__ __forceinline__ void dma16(unsigned voff, const void* sbase, unsigned lds) {
    unsigned keep;
    asm volatile("s_mov_b32 %0, m0\n\ts_mov_b32 m0, %3\n\ts_nop 0\n\tglobal_load_lds_dwordx4 %1, %2\n\ts_mov_b32 m0, %0"
                 : "=&s"(keep) : "v"(voff), "s"(sbase), "s"(lds) : "memory");
}
__device__ __forceinline__ void dma16p(const void* gsrc, unsigned lds) {
    unsigned keep;
    asm volatile("s_mov_b32 %0, m0\n\ts_mov_b32 m0, %2\n\ts_nop 0\n\tglobal_load_lds_dwordx4 %1, off\n\ts_mov_b32 m0, %0"
                 : "=&s"(keep) : "v"(gsrc), "s"(lds) : "memory");
}
#define WAIT_VM0()   asm volatile("s_waitcnt vmcnt(0)" ::: "memory")
#define WAIT_LGKM0() asm volatile("s_waitcnt lgkmcnt(0)" ::: "memory")
#define BARRIER()    asm volatile("s_waitcnt lgkmcnt(0)\n\ts_barrier" ::: "memory")
#define FENCE()      asm volatile("" ::: "memory")

__device__ __forceinline__ u32x4 tr_frag(LAS char* p) {
    s16x4 a0 = __builtin_amdgcn_ds_read_tr16_b64_v4i16((LAS s16x4*)p);
    s16x4 a1 = __builtin_amdgcn_ds_read_tr16_b64_v4i16((LAS s16x4*)(p + 512));
    u32x2 p0 = __builtin_bit_cast(u32x2, a0), p1 = __builtin_bit_cast(u32x2, a1);
    u32x4 a = {p0[0], p0[1], p1[0], p1[1]};
    return a;
}
__device__ __forceinline__ f32x4 mfma16(u32x4 a, u32x4 b, f32x4 c) {
    return __builtin_amdgcn_mfma_f32_16x16x32_bf16(__builtin_bit_cast(bf16x8, a), __builtin_bit_cast(bf16x8, b), c, 0, 0, 0);
}
__device__ __forceinline__ f32x16 mfma32(u32x4 a, u32x4 b, f32x16 c) {
    return __builtin_amdgcn_mfma_f32_32x32x16_bf16(__builtin_bit_cast(bf16x8, a), __builtin_bit_cast(bf16x8, b), c, 0, 0, 0);
}
__device__ __forceinline__ f32x16 smf(u32x4 v, f32x8 p, f32x16 acc) {
    return __builtin_amdgcn_smfmac_f32_32x32x32_bf16(__builtin_bit_cast(bf16x8, v), __builtin_bit_cast(bf16x16, p), acc, (int)0xDDDDDDDD, 0, 0);
}
__device__ __forceinline__ void lds_wait_ge(LAS char* L, unsigned off, unsigned want) {
    _Pragma("nounroll") for (int i = 0; i < SPIN_CAP; ++i) {
        if (__hip_atomic_load((LAS unsigned*)(L + off), RLX_WG) >= want) break;
        __builtin_amdgcn_s_sleep(1);
    }
    FENCE();
}
__device__ __forceinline__ void lds_signal(LAS char* L, unsigned off, int l) {
    WAIT_LGKM0();
    if (l == 0) __hip_atomic_fetch_add((LAS unsigned*)(L + off), 1u, RLX_WG);
    FENCE();
}
__device__ __forceinline__ unsigned get_tag(LAS char* L) {
    lds_wait_ge(L, L_TAG, 1u);
    return __builtin_amdgcn_readfirstlane(*(LAS unsigned*)(L + L_TAG + 4));
}
#define ST(i) do {} while (0)
#define STC(i) do {} while (0)

__device__ __forceinline__ f32x16 attn_true_max(LAS char* kp, LAS char* vp, u32x4 q) {
    f32x16 z; for (int i = 0; i < 16; ++i) z[i] = 0.f;
    float m = -__builtin_inff();
    #pragma unroll 1
    for (int t = 0; t < 64; ++t) {
        f32x16 s = mfma32(*(LAS u32x4*)(kp + t * REC), q, z);
        #pragma unroll
        for (int i = 0; i < 16; ++i) m = fmaxf(m, s[i]);
    }
    m = fmaxf(m, __shfl_xor(m, 32));
    f32x16 acc = z;
    #pragma unroll 1
    for (int t = 0; t < 64; ++t) {
        f32x16 s = mfma32(*(LAS u32x4*)(kp + t * REC), q, z);
        f32x8 p0, p1;
        #pragma unroll
        for (int i = 0; i < 8; ++i) { p0[i] = __builtin_amdgcn_exp2f(s[i] - m); p1[i] = __builtin_amdgcn_exp2f(s[8 + i] - m); }
        acc = smf(*(LAS u32x4*)(vp + t * REC), p0, acc);
        acc = smf(*(LAS u32x4*)(vp + t * REC + 528), p1, acc);
    }
    return acc;
}

__global__ void __launch_bounds__(512, 2) mqa_moe_fused(
        const u16* __restrict__ x, const u16* __restrict__ wq, const u16* __restrict__ wk, const u16* __restrict__ wv,
        const u16* __restrict__ wo, const u16* __restrict__ w_r, const u16* __restrict__ w_gate, const u16* __restrict__ w_up,
        const u16* __restrict__ w_down, const u16* __restrict__ gate_w, u16* __restrict__ out, char* ws) {
    extern __shared__ __attribute__((aligned(16))) char smem[];
    LAS char* L = (LAS char*)smem;
    const unsigned LB = (unsigned)(size_t)L;
    const int l = threadIdx.x & 63;
    const int w = __builtin_amdgcn_readfirstlane(threadIdx.x >> 6);
    const int b = blockIdx.x, xcd = b & 7, j = b >> 3;
    const int tb = xcd * 4 + (j & 3), hg = j >> 2;
    const int g4 = l >> 4, l15 = l & 15;
    const __amdgpu_buffer_rsrc_t rs = __builtin_amdgcn_make_buffer_rsrc((void*)ws, 0, WS_END, 0x00020000);
    const int sg = (l >> 3) & 3, sjh = l >> 5, sq = (l >> 1) & 3;
    const int krow = 8 * sg + 4 * sjh + sq;
    unsigned tag = 0;
    u32x4 qf;
    ST(0);

    if (w == 4) {
        const int type = hg & 1, tg = hg >> 1;
        const u16* W = type ? wv : wk;
        const u16* xr = x + (size_t)(tb * 64 + tg * 16 + l15) * 512 + 8 * g4;
        u32x4 xf[16];
        const unsigned wvo = krow * 32 + (l & 1) * 16;
        #pragma unroll
        for (int s = 0; s < 16; ++s) {
            dma16(wvo, (const char*)W + s * 1024, LB + L_WKV + s * 1024);
            xf[s] = *(const GAS u32x4*)(xr + 32 * s);
        }
        unsigned E = __hip_atomic_load((GAS unsigned*)(ws + WS_EPOCH), RLX_AGENT);
        asm volatile("s_barrier" ::: "memory");
        WAIT_VM0();
        ST(1);
        tag = __builtin_amdgcn_readfirstlane(E) + 1u;
        if (l == 0) { u32x2 tv = {1u, tag}; *(LAS u32x2*)(L + L_TAG) = tv; }
        f32x4 acc = {0.f, 0.f, 0.f, 0.f};
        if (type == 0) {
            #pragma unroll
            for (int s = 0; s < 16; ++s) acc = mfma16(tr_frag(L + L_WKV + s * 1024 + 8 * l), xf[s], acc);
        } else {
            #pragma unroll
            for (int s = 0; s < 16; ++s) acc = mfma16(xf[s], tr_frag(L + L_WKV + s * 1024 + 8 * l), acc);
        }
        unsigned long long pv = (unsigned long long)pk2(acc[0], acc[1]) | ((unsigned long long)pk2(acc[2], acc[3]) << 32);
        unsigned dst;
        if (type == 0) dst = WS_KF + (tb * 2 + (tg >> 1)) * 1024 + ((tg & 1) * 16 + l15 + 32 * (g4 & 1)) * 16 + (g4 >> 1) * 8;
        else           dst = WS_VF + (tb * 4 + tg) * 512 + (g4 >> 1) * 256 + l15 * 16 + (g4 & 1) * 8;
        __hip_atomic_store((GAS unsigned long long*)(ws + dst), pv, RLX_AGENT);
        lds_signal(L, L_C5, l);
        WAIT_VM0();
        ST(2);
        const int kt = tb * 2 + (tg >> 1);
        const int p = (kt & 3) * 64 + (kt >> 2) * 4 + (tg & 1) * 2 + type;
        if (l < 8) __hip_atomic_store((GAS unsigned*)(ws + WS_F1 + l * 1024 + p * 4), tag, RLX_AGENT);
    } else if (w == 5) {
        const int type = hg & 1, tg = hg >> 1;
        const int wrow = 16 * (sg >> 1) + 8 * sjh + 4 * (sg & 1) + sq;
        const char* wsrc = (const char*)(wo + (size_t)(64 * (b & 7)) * 512 + 16 * (b >> 3));
        const unsigned wovo = wrow * 1024 + (l & 1) * 16;
        dma16(wovo, wsrc, LB + L_WOST);
        dma16(wovo, wsrc + 32 * 1024, LB + L_WOST + 1024);
        u32x4 af[16];
        if (type == 0) {
            const u16* ap = ((l & 1) ? gate_w : w_r) + 8 * g4;
            #pragma unroll
            for (int s = 0; s < 16; ++s) af[s] = *(const GAS u32x4*)(ap + 32 * s);
        }
        asm volatile("s_barrier" ::: "memory");
        {
            u32x4 sent = {0xFFFFFFFFu, 0xFFFFFFFFu, 0xFFFFFFFFu, 0xFFFFFFFFu};
            #pragma unroll
            for (int i = 0; i < 8; ++i)
                __builtin_amdgcn_raw_buffer_store_b128(sent, rs, l * 16, WS_OF + ((tb * 2 + (i >> 2)) * 32 + 4 * hg + (i & 3)) * 1024, 16);
        }
        WAIT_VM0();
        #pragma unroll
        for (int i = 0; i < 2; ++i) {
            u32x4 f = tr_frag(L + L_WOST + i * 1024 + 8 * l);
            __builtin_amdgcn_raw_buffer_store_b128(f, rs, l * 16, WS_WOF + (2 * b + i) * 1024, 16);
        }
        lds_signal(L, L_C5, l);
        tag = get_tag(L);
        unsigned key = 0;
        if (type == 0) {
            lds_wait_ge(L, L_C2, 4u);
            f32x4 acc = {0.f, 0.f, 0.f, 0.f};
            LAS char* xp = L + L_XT + (tg * 16 + l15) * 1040 + g4 * 16;
            #pragma unroll
            for (int s = 0; s < 16; ++s) acc = mfma16(af[s], *(LAS u32x4*)(xp + s * 64), acc);
            const int tok = tb * 64 + tg * 16 + l15;
            float gt = bfr(sigmoidf_(bfr(acc[1])));
            if (l < 16) __hip_atomic_store((GAS unsigned*)(ws + WS_GATE) + tok, __builtin_bit_cast(unsigned, gt), RLX_AGENT);
            unsigned u = pk2(acc[0], acc[0]) & 0xffffu;
            unsigned ord = (u & 0x8000u) ? (~u & 0xffffu) : (u | 0x8000u);
            if ((u & 0x7fffu) > 0x7f80u) ord = 0u;
            key = (l < 16) ? ((ord << 16) | (unsigned)(2047 - tok)) : 0u;
            #pragma unroll
            for (int o = 1; o < 16; o <<= 1) { unsigned k2 = __shfl_xor(key, o); key = key > k2 ? key : k2; }
            key = __builtin_amdgcn_readfirstlane(key);
        }
        WAIT_VM0();
        if (l < 8) __hip_atomic_store((GAS unsigned long long*)(ws + WS_F2 + l * 2048 + b * 8), ((unsigned long long)tag << 32) | key, RLX_AGENT);
        ST(1);
    } else {
        if (w == 0 && l < 16) *(LAS unsigned*)(L + L_MISC + 4 * l) = 0u;
        BARRIER();
        if (w < 4) {
            #pragma unroll 4
            for (int i = 0; i < 16; ++i) {
                const int row = 16 * w + i;
                dma16(l * 16, (const char*)x + (size_t)(tb * 64 + row) * 1024, LB + L_XT + row * 1040);
            }
            const unsigned qvo = (l >> 3) * 1024 + (((l & 7) ^ (((l >> 4) & 1) << 2)) * 16);
            #pragma unroll 4
            for (int i = 0; i < 16; ++i) {
                const int blk = 16 * w + i;
                dma16(qvo, (const char*)wq + (size_t)blk * 8192 + hg * 128, LB + L_WQ + blk * 1024);
            }
            WAIT_VM0();
            ST(1);
            lds_signal(L, L_C2, l);
            lds_wait_ge(L, L_C2, 4u);
            ST(2);
            const int cp = w & 1, th = w >> 1, h = l >> 5, nhi = g4 & 1, q = (l >> 2) & 3, p = l & 3;
            LAS char* ap = L + L_WQ + h * 1024 + q * 128 + ((64 * cp + 32 * nhi + 8 * p) ^ (((q >> 1) & 1) << 6));
            LAS char* bp = L + L_XT + (32 * th + (l & 31)) * 1040 + h * 16;
            f32x16 acc; for (int i = 0; i < 16; ++i) acc[i] = 0.f;
            #pragma unroll
            for (int ks = 0; ks < 32; ++ks) acc = mfma32(tr_frag(ap + ks * 2048), *(LAS u32x4*)(bp + ks * 32), acc);
            const float sc = 0.25f * 1.4426950408889634f;
            u32x4 qo;
            #pragma unroll
            for (int i = 0; i < 4; ++i) { qf[i] = pk2(acc[2 * i] * sc, acc[2 * i + 1] * sc); qo[i] = pk2(acc[8 + 2 * i] * sc, acc[9 + 2 * i] * sc); }
            *(LAS u32x4*)(L + L_QX + w * 1024 + l * 16) = qo;
            ST(3);
        }
    }

    const int rep = b & 7;
    if (w >= 4) {
        const int bw = w - 4;
        if (w >= 6) tag = get_tag(L);
        {
            GAS unsigned* fp = (GAS unsigned*)(ws + WS_F1 + rep * 1024) + bw * 64 + l;
            _Pragma("nounroll") for (int i = 0; i < SPIN_CAP; ++i) {
                unsigned v = __hip_atomic_load(fp, RLX_AGENT);
                if (__all(v == tag)) break;
                __builtin_amdgcn_s_sleep(1);
            }
            FENCE();
        }
        ST(4);
        u32x4 kf[16], vf[16];
        #pragma unroll
        for (int i = 0; i < 16; ++i) {
            kf[i] = __builtin_amdgcn_raw_buffer_load_b128(rs, l * 16, WS_KF + (4 * i + bw) * 1024, 16);
            vf[i] = __builtin_amdgcn_raw_buffer_load_b128(rs, l * 16, WS_VF + (4 * i + bw) * 1024, 16);
        }
        lds_wait_ge(L, L_C5, 2u);
        LAS char* kd = L + bw * REC + l * 16;
        LAS char* vd = L + bw * REC + 1024 + (l >> 5) * 528 + (l & 31) * 16;
        const u32x4 ones = {0x3f803f80u, 0x3f803f80u, 0x3f803f80u, 0x3f803f80u};
        LAS char* od = L + ((l >> 1) * 4 + bw) * REC + 1024 + (l & 1) * 528 + 512;
        #pragma unroll
        for (int i = 0; i < 3; ++i) { *(LAS u32x4*)(kd + i * 4 * REC) = kf[i]; *(LAS u32x4*)(vd + i * 4 * REC) = vf[i]; }
        if (l < 6) *(LAS u32x4*)od = ones;
        ST(5);
        BARRIER();
        ST(6);
        qf = *(LAS u32x4*)(L + L_QX + bw * 1024 + l * 16);
        #pragma unroll
        for (int i = 3; i < 16; ++i) { *(LAS u32x4*)(kd + i * 4 * REC) = kf[i]; *(LAS u32x4*)(vd + i * 4 * REC) = vf[i]; }
        if (l >= 6 && l < 32) *(LAS u32x4*)od = ones;
        lds_signal(L, L_C4, l);
        if (b == 0 && w == 4 && l == 0) __hip_atomic_store((GAS unsigned*)(ws + WS_EPOCH), tag, RLX_AGENT);
    } else {
        BARRIER();
        ST(6);
        tag = get_tag(L);
    }

    const int ft = xcd * 28 + j;
    if (j < 28) {
        const u16* wsrc = ((l & 1) ? w_up : w_gate) + (size_t)krow * 1792 + 8 * ft;
        #pragma unroll
        for (int i = 0; i < 2; ++i) { const int s = 2 * w + i; dma16p(wsrc + (size_t)s * 32 * 1792, LB + L_FW1 + s * 1024); }
    }
    if (w < 7) dma16(krow * 1024 + (l & 1) * 16, (const char*)(w_down + (size_t)(224 * xcd + 32 * w) * 512 + 16 * j), LB + L_FW2 + w * 1024);

    const int hl = 2 * (w & 1) + (w >> 2), tha = (w >> 1) & 1;
    unsigned kp = LB + l * 16;
    unsigned vp = LB + 1024 + ((l & 31) < 16 ? (l >> 5) * 256 + (l & 31) * 16 : 512);
    const int idxv = (int)0xDDDDDDDD;
    f32x16 X, Y, acc; u32x4 KR, V0, V1;
    for (int i = 0; i < 16; ++i) acc[i] = 0.f;
    if (w >= 4) __builtin_amdgcn_s_setprio(1);
    ST(7); STC(30);
    asm volatile("ds_read_b128 v[80:83], v97 offset:0\n\tds_read_b128 v[84:87], v98 offset:0\n\ts_waitcnt lgkmcnt(1)\n\tv_mfma_f32_32x32x16_bf16 v[32:47], v[80:83], v[92:95], 0\n\tds_read_b128 v[80:83], v97 offset:2080\n\tds_read_b128 v[88:91], v98 offset:528\n\tv_mov_b32 v56, 0\n\tv_mov_b32 v57, 0\n\tv_mov_b32 v58, 0\n\tv_mov_b32 v59, 0\n\tv_mov_b32 v60, 0\n\tv_mov_b32 v61, 0\n\tv_mov_b32 v62, 0\n\tv_mov_b32 v63, 0\n\tv_mov_b32 v48, 0\n\tv_mov_b32 v49, 0\n\tv_mov_b32 v50, 0\n\tv_mov_b32 v51, 0\n\tv_mov_b32 v52, 0\n\tv_mov_b32 v53, 0\n\tv_mov_b32 v54, 0\n\tv_mov_b32 v55, 0\n\ts_nop 7\n\ts_nop 3\n\ts_waitcnt lgkmcnt(0)"
                 : "={v[32:47]}"(X), "={v[48:63]}"(Y), "={v[80:83]}"(KR), "={v[84:87]}"(V0), "={v[88:91]}"(V1)
                 : "{v[92:95]}"(qf), "{v97}"(kp), "{v98}"(vp) : "memory");
    const int tho = hg >> 2, cg = hg & 3;
    const int n0 = cg * 128 + 16 * w;
    u32x4 wof[16];
    u32x2 xres[2];
    float gres[2];
    u32x4 fl0, fl1;
    unsigned rkey = 0;
    if (w == 0) {
        fl0 = __builtin_amdgcn_raw_buffer_load_b128(rs, l * 16, WS_F2 + rep * 2048, 16);
        fl1 = __builtin_amdgcn_raw_buffer_load_b128(rs, l * 16, WS_F2 + rep * 2048 + 1024, 16);
    }
    #pragma unroll 1
    for (int step = 0; step < 8; ++step) {
        unsigned n = 1;
        asm volatile("LP_%=:\n\tv_exp_f32 v32, v32\n\tv_exp_f32 v33, v33\n\ts_waitcnt lgkmcnt(2)\n\tv_smfmac_f32_32x32x32_bf16 v[64:79], v[88:91], v[56:63], v96\n\tds_read_b128 v[88:91], v98 offset:528\n\tv_exp_f32 v34, v34\n\tv_exp_f32 v35, v35\n\tv_exp_f32 v36, v36\n\tv_exp_f32 v37, v37\n\ts_waitcnt lgkmcnt(2)\n\tv_mfma_f32_32x32x16_bf16 v[48:63], v[80:83], v[92:95], 0\n\tds_read_b128 v[80:83], v97 offset:4160\n\tv_exp_f32 v38, v38\n\tv_exp_f32 v39, v39\n\tv_exp_f32 v40, v40\n\tv_exp_f32 v41, v41\n\ts_waitcnt lgkmcnt(2)\n\tv_smfmac_f32_32x32x32_bf16 v[64:79], v[84:87], v[32:39], v96\n\tds_read_b128 v[84:87], v98 offset:2080\n\tv_exp_f32 v42, v42\n\tv_exp_f32 v43, v43\n\tv_exp_f32 v44, v44\n\tv_exp_f32 v45, v45\n\tv_exp_f32 v46, v46\n\tv_exp_f32 v47, v47\n\tv_exp_f32 v48, v48\n\tv_exp_f32 v49, v49\n\ts_waitcnt lgkmcnt(2)\n\tv_smfmac_f32_32x32x32_bf16 v[64:79], v[88:91], v[40:47], v96\n\tds_read_b128 v[88:91], v98 offset:2608\n\tv_exp_f32 v50, v50\n\tv_exp_f32 v51, v51\n\tv_exp_f32 v52, v52\n\tv_exp_f32 v53, v53\n\ts_waitcnt lgkmcnt(2)\n\tv_mfma_f32_32x32x16_bf16 v[32:47], v[80:83], v[92:95], 0\n\tds_read_b128 v[80:83], v97 offset:6240\n\tv_exp_f32 v54, v54\n\tv_exp_f32 v55, v55\n\tv_exp_f32 v56, v56\n\tv_exp_f32 v57, v57\n\ts_waitcnt lgkmcnt(2)\n\tv_smfmac_f32_32x32x32_bf16 v[64:79], v[84:87], v[48:55], v96\n\tds_read_b128 v[84:87], v98 offset:4160\n\tv_exp_f32 v58, v58\n\tv_exp_f32 v59, v59\n\tv_exp_f32 v60, v60\n\tv_exp_f32 v61, v61\n\tv_exp_f32 v62, v62\n\tv_exp_f32 v63, v63\n\tv_exp_f32 v32, v32\n\tv_exp_f32 v33, v33\n\ts_waitcnt lgkmcnt(2)\n\tv_smfmac_f32_32x32x32_bf16 v[64:79], v[88:91], v[56:63], v96\n\tds_read_b128 v[88:91], v98 offset:4688\n\tv_exp_f32 v34, v34\n\tv_exp_f32 v35, v35\n\tv_exp_f32 v36, v36\n\tv_exp_f32 v37, v37\n\ts_waitcnt lgkmcnt(2)\n\tv_mfma_f32_32x32x16_bf16 v[48:63], v[80:83], v[92:95], 0\n\tds_read_b128 v[80:83], v97 offset:8320\n\tv_exp_f32 v38, v38\n\tv_exp_f32 v39, v39\n\tv_exp_f32 v40, v40\n\tv_exp_f32 v41, v41\n\ts_waitcnt lgkmcnt(2)\n\tv_smfmac_f32_32x32x32_bf16 v[64:79], v[84:87], v[32:39], v96\n\tds_read_b128 v[84:87], v98 offset:6240\n\tv_exp_f32 v42, v42\n\tv_exp_f32 v43, v43\n\tv_exp_f32 v44, v44\n\tv_exp_f32 v45, v45\n\tv_exp_f32 v46, v46\n\tv_exp_f32 v47, v47\n\tv_exp_f32 v48, v48\n\tv_exp_f32 v49, v49\n\ts_waitcnt lgkmcnt(2)\n\tv_smfmac_f32_32x32x32_bf16 v[64:79], v[88:91], v[40:47], v96\n\tds_read_b128 v[88:91], v98 offset:6768\n\tv_exp_f32 v50, v50\n\tv_exp_f32 v51, v51\n\tv_exp_f32 v52, v52\n\tv_exp_f32 v53, v53\n\ts_waitcnt lgkmcnt(2)\n\tv_mfma_f32_32x32x16_bf16 v[32:47], v[80:83], v[92:95], 0\n\tds_read_b128 v[80:83], v97 offset:10400\n\tv_exp_f32 v54, v54\n\tv_exp_f32 v55, v55\n\tv_exp_f32 v56, v56\n\tv_exp_f32 v57, v57\n\ts_waitcnt lgkmcnt(2)\n\tv_smfmac_f32_32x32x32_bf16 v[64:79], v[84:87], v[48:55], v96\n\tds_read_b128 v[84:87], v98 offset:8320\n\tv_exp_f32 v58, v58\n\tv_exp_f32 v59, v59\n\tv_exp_f32 v60, v60\n\tv_exp_f32 v61, v61\n\tv_exp_f32 v62, v62\n\tv_exp_f32 v63, v63\n\tv_exp_f32 v32, v32\n\tv_exp_f32 v33, v33\n\ts_waitcnt lgkmcnt(2)\n\tv_smfmac_f32_32x32x32_bf16 v[64:79], v[88:91], v[56:63], v96\n\tds_read_b128 v[88:91], v98 offset:8848\n\tv_exp_f32 v34, v34\n\tv_exp_f32 v35, v35\n\tv_exp_f32 v36, v36\n\tv_exp_f32 v37, v37\n\ts_waitcnt lgkmcnt(2)\n\tv_mfma_f32_32x32x16_bf16 v[48:63], v[80:83], v[92:95], 0\n\tds_read_b128 v[80:83], v97 offset:12480\n\tv_exp_f32 v38, v38\n\tv_exp_f32 v39, v39\n\tv_exp_f32 v40, v40\n\tv_exp_f32 v41, v41\n\ts_waitcnt lgkmcnt(2)\n\tv_smfmac_f32_32x32x32_bf16 v[64:79], v[84:87], v[32:39], v96\n\tds_read_b128 v[84:87], v98 offset:10400\n\tv_exp_f32 v42, v42\n\tv_exp_f32 v43, v43\n\tv_exp_f32 v44, v44\n\tv_exp_f32 v45, v45\n\tv_exp_f32 v46, v46\n\tv_exp_f32 v47, v47\n\tv_exp_f32 v48, v48\n\tv_exp_f32 v49, v49\n\ts_waitcnt lgkmcnt(2)\n\tv_smfmac_f32_32x32x32_bf16 v[64:79], v[88:91], v[40:47], v96\n\tds_read_b128 v[88:91], v98 offset:10928\n\tv_exp_f32 v50, v50\n\tv_exp_f32 v51, v51\n\tv_exp_f32 v52, v52\n\tv_exp_f32 v53, v53\n\ts_waitcnt lgkmcnt(2)\n\tv_mfma_f32_32x32x16_bf16 v[32:47], v[80:83], v[92:95], 0\n\tds_read_b128 v[80:83], v97 offset:14560\n\tv_exp_f32 v54, v54\n\tv_exp_f32 v55, v55\n\tv_exp_f32 v56, v56\n\tv_exp_f32 v57, v57\n\ts_waitcnt lgkmcnt(2)\n\tv_smfmac_f32_32x32x32_bf16 v[64:79], v[84:87], v[48:55], v96\n\tds_read_b128 v[84:87], v98 offset:12480\n\tv_exp_f32 v58, v58\n\tv_exp_f32 v59, v59\n\tv_exp_f32 v60, v60\n\tv_exp_f32 v61, v61\n\tv_exp_f32 v62, v62\n\tv_exp_f32 v63, v63\n\tv_exp_f32 v32, v32\n\tv_exp_f32 v33, v33\n\ts_waitcnt lgkmcnt(2)\n\tv_smfmac_f32_32x32x32_bf16 v[64:79], v[88:91], v[56:63], v96\n\tds_read_b128 v[88:91], v98 offset:13008\n\tv_exp_f32 v34, v34\n\tv_exp_f32 v35, v35\n\tv_exp_f32 v36, v36\n\tv_exp_f32 v37, v37\n\ts_waitcnt lgkmcnt(2)\n\tv_mfma_f32_32x32x16_bf16 v[48:63], v[80:83], v[92:95], 0\n\tds_read_b128 v[80:83], v97 offset:16640\n\tv_exp_f32 v38, v38\n\tv_exp_f32 v39, v39\n\tv_exp_f32 v40, v40\n\tv_exp_f32 v41, v41\n\ts_waitcnt lgkmcnt(2)\n\tv_smfmac_f32_32x32x32_bf16 v[64:79], v[84:87], v[32:39], v96\n\tds_read_b128 v[84:87], v98 offset:14560\n\tv_exp_f32 v42, v42\n\tv_exp_f32 v43, v43\n\tv_exp_f32 v44, v44\n\tv_exp_f32 v45, v45\n\tv_exp_f32 v46, v46\n\tv_exp_f32 v47, v47\n\tv_exp_f32 v48, v48\n\tv_exp_f32 v49, v49\n\ts_waitcnt lgkmcnt(2)\n\tv_smfmac_f32_32x32x32_bf16 v[64:79], v[88:91], v[40:47], v96\n\tds_read_b128 v[88:91], v98 offset:15088\n\tv_exp_f32 v50, v50\n\tv_exp_f32 v51, v51\n\tv_exp_f32 v52, v52\n\tv_exp_f32 v53, v53\n\ts_waitcnt lgkmcnt(2)\n\tv_mfma_f32_32x32x16_bf16 v[32:47], v[80:83], v[92:95], 0\n\tds_read_b128 v[80:83], v97 offset:18720\n\tv_exp_f32 v54, v54\n\tv_exp_f32 v55, v55\n\tv_exp_f32 v56, v56\n\tv_exp_f32 v57, v57\n\ts_waitcnt lgkmcnt(2)\n\tv_smfmac_f32_32x32x32_bf16 v[64:79], v[84:87], v[48:55], v96\n\tds_read_b128 v[84:87], v98 offset:16640\n\tv_exp_f32 v58, v58\n\tv_exp_f32 v59, v59\n\tv_exp_f32 v60, v60\n\tv_exp_f32 v61, v61\n\tv_exp_f32 v62, v62\n\tv_exp_f32 v63, v63\n\tv_add_u32 v97, 16640, v97\n\tv_add_u32 v98, 16640, v98\n\ts_sub_u32 %[n], %[n], 1\n\ts_cmp_lg_u32 %[n], 0\n\ts_cbranch_scc1 LP_%=\n\ts_waitcnt lgkmcnt(0)\n\ts_nop 7"
                     : "+{v[32:47]}"(X), "+{v[48:63]}"(Y), "+{v[64:79]}"(acc), "+{v[80:83]}"(KR), "+{v[84:87]}"(V0), "+{v[88:91]}"(V1),
                       "+{v97}"(kp), "+{v98}"(vp), [n] "+s"(n)
                     : "{v[92:95]}"(qf), "{v96}"(idxv) : "scc", "memory");
        if (step == 0) {
            WAIT_VM0();
            lds_signal(L, L_C7, l);
            if (w == 0) {
                _Pragma("nounroll") for (int i = 0; i < SPIN_CAP; ++i) {
                    if (__all(fl0[1] == tag && fl0[3] == tag && fl1[1] == tag && fl1[3] == tag)) break;
                    FENCE();
                    fl0 = __builtin_amdgcn_raw_buffer_load_b128(rs, l * 16, WS_F2 + rep * 2048, 16);
                    fl1 = __builtin_amdgcn_raw_buffer_load_b128(rs, l * 16, WS_F2 + rep * 2048 + 1024, 16);
                }
                unsigned key = max(max(fl0[0], fl0[2]), max(fl1[0], fl1[2]));
                #pragma unroll
                for (int o = 1; o < 64; o <<= 1) { unsigned k2 = __shfl_xor(key, o); key = key > k2 ? key : k2; }
                key = __builtin_amdgcn_readfirstlane(key);
                if (l == 0) *(LAS unsigned*)(L + L_KEY) = key;
                lds_signal(L, L_C6, l);
                const int idx = 2047 - (int)(key & 0x7ffu);
                dma16(l * 16, (const char*)x + (size_t)idx * 1024, LB + L_XSEL);
            }
            lds_wait_ge(L, L_C4, 4u);
        } else if (step == 1) {
            if (w == 0 && j < 28) {
                WAIT_VM0();
                lds_wait_ge(L, L_C7, 8u);
                f32x4 a = {0.f, 0.f, 0.f, 0.f};
                #pragma unroll
                for (int s = 0; s < 16; ++s)
                    a = mfma16(tr_frag(L + L_FW1 + s * 1024 + 8 * l), *(LAS u32x4*)(L + L_XSEL + s * 64 + g4 * 16), a);
                float hv[4];
                #pragma unroll
                for (int i = 0; i < 4; ++i) {
                    float u = bfr(__shfl_xor(a[i], 32));
                    float gq = bfr(a[i]);
                    hv[i] = bfr(gq * sigmoidf_(gq)) * u;
                }
                u32x4 gr = {pk2(hv[0], hv[1]), tag, pk2(hv[2], hv[3]), tag};
                if (l == 0 || l == 16) __builtin_amdgcn_raw_buffer_store_b128(gr, rs, g4 * 16, WS_HG + ft * 32, 16);
            }
        } else if (step == 2) {
            lds_wait_ge(L, L_C6, 1u);
            rkey = __builtin_amdgcn_readfirstlane(*(LAS unsigned*)(L + L_KEY));
            #pragma unroll
            for (int s = 0; s < 16; ++s)
                wof[s] = __builtin_amdgcn_raw_buffer_load_b128(rs, l * 16, WS_WOF + ((cg * 8 + w) * 16 + s) * 1024, 16);
            #pragma unroll
            for (int t = 0; t < 2; ++t) {
                const int tok = tb * 64 + tho * 32 + 16 * t + l15;
                xres[t] = *(const GAS u32x2*)(x + (size_t)tok * 512 + n0 + 4 * g4);
                gres[t] = __builtin_bit_cast(float, __hip_atomic_load((GAS unsigned*)(ws + WS_GATE) + tok, RLX_AGENT));
            }
        } else if (step == 3) {
            if (w == 1) fl0 = __builtin_amdgcn_raw_buffer_load_b128(rs, (l < 56 ? l : 55) * 16, WS_HG + xcd * 896, 16);
        } else if (step == 4) {
            if (w == 1) {
                _Pragma("nounroll") for (int i = 0; i < SPIN_CAP; ++i) {
                    if (__all(fl0[1] == tag && fl0[3] == tag)) break;
                    FENCE();
                    fl0 = __builtin_amdgcn_raw_buffer_load_b128(rs, (l < 56 ? l : 55) * 16, WS_HG + xcd * 896, 16);
                }
                u32x2 hv = {fl0[0], fl0[2]};
                if (l < 56) *(LAS u32x2*)(L + L_HBUF + l * 8) = hv;
                WAIT_LGKM0();
                f32x4 a = {0.f, 0.f, 0.f, 0.f};
                #pragma unroll
                for (int s = 0; s < 7; ++s)
                    a = mfma16(tr_frag(L + L_FW2 + s * 1024 + 8 * l), *(LAS u32x4*)(L + L_HBUF + s * 64 + g4 * 16), a);
                u32x4 g0 = {f2u(a[0]), tag, f2u(a[1]), tag};
                u32x4 g1 = {f2u(a[2]), tag, f2u(a[3]), tag};
                if (l15 == 0) {
                    __builtin_amdgcn_raw_buffer_store_b128(g0, rs, g4 * 32, WS_YG + (xcd * 512 + 16 * j) * 8, 16);
                    __builtin_amdgcn_raw_buffer_store_b128(g1, rs, g4 * 32 + 16, WS_YG + (xcd * 512 + 16 * j) * 8, 16);
                }
            }
        }
        ST(12 + step);
        if (step == PRIO_STEP) __builtin_amdgcn_s_setprio(0);
    }
    asm volatile("s_nop 1\n\tv_smfmac_f32_32x32x32_bf16 v[64:79], v[88:91], v[56:63], v96\n\ts_nop 15\n\ts_nop 7" : "+{v[64:79]}"(acc) : "{v[48:63]}"(Y), "{v[88:91]}"(V1), "{v96}"(idxv) : "memory");
    __builtin_amdgcn_s_setprio(0);
    ST(8); STC(31);

    {
        const float ls = acc[8];
        bool ok = ls >= 8.6736174e-19f && ls <= 1.2676506e30f;
        #pragma unroll
        for (int i = 0; i < 8; ++i) ok = ok && (__builtin_fabsf(acc[i]) < __builtin_inff());
        if (!__all(ok)) {
            LAS char* kq = L + l * 16;
            LAS char* vq = L + 1024 + ((l & 31) < 16 ? (l >> 5) * 256 + (l & 31) * 16 : 512);
            acc = attn_true_max(kq, vq, qf);
        }
    }
    {
        const float inv = 1.f / acc[8];
        u32x4 o;
        #pragma unroll
        for (int i = 0; i < 4; ++i) { unsigned u = pk2(acc[2 * i] * inv, acc[2 * i + 1] * inv); o[i] = u < 0xFFFFFFFEu ? u : 0xFFFFFFFEu; }
        __builtin_amdgcn_raw_buffer_store_b128(o, rs, l * 16, WS_OF + ((tb * 2 + tha) * 32 + 4 * hg + hl) * 1024, 16);
    }
    const int idx = 2047 - (int)(rkey & 0x7ffu);
    const bool has_idx = (idx >> 5) == tb * 2 + tho;
    u32x4 yg = {0u, tag, 0u, tag};
    const unsigned yoff = ((l >> 3) * 512 + n0 + 2 * (l & 7)) * 8;
    if (has_idx) yg = __builtin_amdgcn_raw_buffer_load_b128(rs, yoff, WS_YG, 16);
    BARRIER();
    ST(9);
    {
        u32x4 f[4];
        const unsigned src = WS_OF + ((tb * 2 + tho) * 32 + 4 * w) * 1024;
        _Pragma("nounroll") for (int i = 0; i < SPIN_CAP; ++i) {
            bool ok = true;
            #pragma unroll
            for (int k = 0; k < 4; ++k) {
                f[k] = __builtin_amdgcn_raw_buffer_load_b128(rs, l * 16, src + k * 1024, 16);
                ok = ok && f[k][0] != 0xFFFFFFFFu && f[k][1] != 0xFFFFFFFFu && f[k][2] != 0xFFFFFFFFu && f[k][3] != 0xFFFFFFFFu;
            }
            if (__all(ok)) break;
            FENCE();
        }
        #pragma unroll
        for (int k = 0; k < 4; ++k) *(LAS u32x4*)(L + (4 * w + k) * 1024 + l * 16) = f[k];
    }
    ST(10);
    BARRIER();
    f32x4 oa[2] = {{0.f, 0.f, 0.f, 0.f}, {0.f, 0.f, 0.f, 0.f}};
    {
        LAS char* op = L + (g4 >> 1) * 1024 + (g4 & 1) * 512 + l15 * 16;
        #pragma unroll
        for (int s = 0; s < 16; ++s) {
            oa[0] = mfma16(wof[s], *(LAS u32x4*)(op + s * 2048), oa[0]);
            oa[1] = mfma16(wof[s], *(LAS u32x4*)(op + s * 2048 + 256), oa[1]);
        }
    }
    float yv[4] = {0.f, 0.f, 0.f, 0.f};
    if (has_idx) {
        _Pragma("nounroll") for (int i = 0; i < SPIN_CAP; ++i) {
            if (__all(yg[1] == tag && yg[3] == tag)) break;
            FENCE();
            yg = __builtin_amdgcn_raw_buffer_load_b128(rs, yoff, WS_YG, 16);
        }
        float y0 = u2f(yg[0]), y1 = u2f(yg[2]);
        #pragma unroll
        for (int o = 8; o < 64; o <<= 1) { y0 += __shfl_xor(y0, o); y1 += __shfl_xor(y1, o); }
        unsigned ord = rkey >> 16;
        unsigned rb = (ord & 0x8000u) ? (ord & 0x7fffu) : (~ord & 0xffffu);
        const float rsel = bfr(sigmoidf_(bflo(rb)));
        yv[0] = bfr(bfr(__shfl(y0, 2 * g4)) * rsel);     yv[1] = bfr(bfr(__shfl(y1, 2 * g4)) * rsel);
        yv[2] = bfr(bfr(__shfl(y0, 2 * g4 + 1)) * rsel); yv[3] = bfr(bfr(__shfl(y1, 2 * g4 + 1)) * rsel);
    }
    #pragma unroll
    for (int t = 0; t < 2; ++t) {
        const int tok = tb * 64 + tho * 32 + 16 * t + l15;
        const float g = gres[t], omg = bfr(1.f - g);
        float bx[4] = {bflo(xres[t][0]), bfhi(xres[t][0]), bflo(xres[t][1]), bfhi(xres[t][1])};
        float r4[4];
        #pragma unroll
        for (int i = 0; i < 4; ++i) {
            float bb = (has_idx && tok == idx) ? bfr(bx[i] + yv[i]) : bx[i];
            r4[i] = bfr(g * bfr(oa[t][i])) + bfr(omg * bb);
        }
        u32x2 o2 = {pk2(r4[0], r4[1]), pk2(r4[2], r4[3])};
        *(GAS u32x2*)(out + (size_t)tok * 512 + n0 + 4 * g4) = o2;
    }
    ST(11);
}

extern "C" void kernel_launch(void* const* d_in, const int* in_sizes, int n_in, void* d_out, int out_size,
                              void* d_ws, size_t ws_size, hipStream_t stream) {
    (void)in_sizes; (void)n_in; (void)out_size; (void)ws_size;
    (void)hipFuncSetAttribute((const void*)mqa_moe_fused, hipFuncAttributeMaxDynamicSharedMemorySize, LDS_BYTES);
    mqa_moe_fused<<<256, 512, LDS_BYTES, stream>>>(
        (const u16*)d_in[0], (const u16*)d_in[1], (const u16*)d_in[2], (const u16*)d_in[3], (const u16*)d_in[4],
        (const u16*)d_in[6], (const u16*)d_in[7], (const u16*)d_in[8], (const u16*)d_in[9], (const u16*)d_in[10],
        (u16*)d_out, (char*)d_ws);
}
```

```cpp
#include <hip/hip_runtime.h>
#include <stdint.h>
#define PRIO_N 3

typedef unsigned short u16;
typedef __attribute__((ext_vector_type(8))) __bf16 bf16x8;
typedef __attribute__((ext_vector_type(16))) __bf16 bf16x16;
typedef __attribute__((ext_vector_type(2))) __bf16 bf16x2;
typedef __attribute__((ext_vector_type(4))) short s16x4;
typedef __attribute__((ext_vector_type(2))) float f32x2;
typedef __attribute__((ext_vector_type(4))) float f32x4;
typedef __attribute__((ext_vector_type(8))) float f32x8;
typedef __attribute__((ext_vector_type(16))) float f32x16;
typedef __attribute__((ext_vector_type(4))) unsigned u32x4;
typedef __attribute__((ext_vector_type(2))) unsigned u32x2;
#define LAS __attribute__((address_space(3)))
#define GAS __attribute__((address_space(1)))
#define RLX_AGENT __ATOMIC_RELAXED, __HIP_MEMORY_SCOPE_AGENT
#define RLX_WG __ATOMIC_RELAXED, __HIP_MEMORY_SCOPE_WORKGROUP

constexpr unsigned WS_EPOCH = 0;
constexpr unsigned WS_F1   = 4096;
constexpr unsigned WS_F2   = 16384;
constexpr unsigned WS_KF   = 32768;
constexpr unsigned WS_VF   = 98304;
constexpr unsigned WS_WOF  = 163840;
constexpr unsigned WS_OF   = 688128;
constexpr unsigned WS_GATE = 2785280;
constexpr unsigned WS_HG   = 2793472;
constexpr unsigned WS_YG   = 2801664;
constexpr unsigned WS_END  = 2834432;
constexpr unsigned WS_DBG  = 4194304;

constexpr unsigned REC     = 2080;
constexpr unsigned L_WKV   = 0;
constexpr unsigned L_WOST  = 16384;
constexpr unsigned L_XT    = 24960;
constexpr unsigned L_WQ    = 91520;
constexpr unsigned L_FW1   = 133120;
constexpr unsigned L_FW2   = 149504;
constexpr unsigned L_QX    = 157056;
constexpr unsigned L_MISC  = 161152;
constexpr unsigned L_C2 = L_MISC + 0, L_C4 = L_MISC + 4, L_C5 = L_MISC + 8, L_C6 = L_MISC + 12, L_C7 = L_MISC + 16,
                   L_TAG = L_MISC + 24  , L_KEY = L_MISC + 32;
constexpr unsigned L_XSEL  = L_MISC + 128;
constexpr unsigned L_HBUF  = L_XSEL + 1024;
constexpr unsigned LDS_BYTES = 163840;
constexpr int SPIN_CAP = 1 << 18;

__device__ __forceinline__ unsigned pk2(float a, float b) {
    f32x2 v = {a, b};
    return __builtin_bit_cast(unsigned, __builtin_convertvector(v, bf16x2));
}
__device__ __forceinline__ float bfr(float a) {
    return __builtin_bit_cast(float, pk2(a, a) & 0xffff0000u);
}
__device__ __forceinline__ unsigned f2u(float f) { return __builtin_bit_cast(unsigned, f); }
__device__ __forceinline__ float u2f(unsigned u) { return __builtin_bit_cast(float, u); }
__device__ __forceinline__ float bflo(unsigned u) { return __builtin_bit_cast(float, u << 16); }
__device__ __forceinline__ float bfhi(unsigned u) { return __builtin_bit_cast(float, u & 0xffff0000u); }
__device__ __forceinline__ float sigmoidf_(float z) { return __builtin_amdgcn_rcpf(1.f + __builtin_amdgcn_exp2f(-1.4426950408889634f * z)); }

__device__ __forceinline__ void dma16(unsigned voff, const void* sbase, unsigned lds) {
    unsigned keep;
    asm volatile("s_mov_b32 %0, m0\n\ts_mov_b32 m0, %3\n\ts_nop 0\n\tglobal_load_lds_dwordx4 %1, %2\n\ts_mov_b32 m0, %0"
                 : "=&s"(keep) : "v"(voff), "s"(sbase), "s"(lds) : "memory");
}
__device__ __forceinline__ void dma16p(const void* gsrc, unsigned lds) {
    unsigned keep;
    asm volatile("s_mov_b32 %0, m0\n\ts_mov_b32 m0, %2\n\ts_nop 0\n\tglobal_load_lds_dwordx4 %1, off\n\ts_mov_b32 m0, %0"
                 : "=&s"(keep) : "v"(gsrc), "s"(lds) : "memory");
}
#define WAIT_VM0()   asm volatile("s_waitcnt vmcnt(0)" ::: "memory")
#define WAIT_LGKM0() asm volatile("s_waitcnt lgkmcnt(0)" ::: "memory")
#define BARRIER()    asm volatile("s_waitcnt lgkmcnt(0)\n\ts_barrier" ::: "memory")
#define FENCE()      asm volatile("" ::: "memory")

__device__ __forceinline__ u32x4 tr_frag(LAS char* p) {
    s16x4 a0 = __builtin_amdgcn_ds_read_tr16_b64_v4i16((LAS s16x4*)p);
    s16x4 a1 = __builtin_amdgcn_ds_read_tr16_b64_v4i16((LAS s16x4*)(p + 512));
    u32x2 p0 = __builtin_bit_cast(u32x2, a0), p1 = __builtin_bit_cast(u32x2, a1);
    u32x4 a = {p0[0], p0[1], p1[0], p1[1]};
    return a;
}
__device__ __forceinline__ f32x4 mfma16(u32x4 a, u32x4 b, f32x4 c) {
    return __builtin_amdgcn_mfma_f32_16x16x32_bf16(__builtin_bit_cast(bf16x8, a), __builtin_bit_cast(bf16x8, b), c, 0, 0, 0);
}
__device__ __forceinline__ f32x16 mfma32(u32x4 a, u32x4 b, f32x16 c) {
    return __builtin_amdgcn_mfma_f32_32x32x16_bf16(__builtin_bit_cast(bf16x8, a), __builtin_bit_cast(bf16x8, b), c, 0, 0, 0);
}
__device__ __forceinline__ f32x16 smf(u32x4 v, f32x8 p, f32x16 acc) {
    return __builtin_amdgcn_smfmac_f32_32x32x32_bf16(__builtin_bit_cast(bf16x8, v), __builtin_bit_cast(bf16x16, p), acc, (int)0xDDDDDDDD, 0, 0);
}
__device__ __forceinline__ void lds_wait_ge(LAS char* L, unsigned off, unsigned want) {
    _Pragma("nounroll") for (int i = 0; i < SPIN_CAP; ++i) {
        if (__hip_atomic_load((LAS unsigned*)(L + off), RLX_WG) >= want) break;
        __builtin_amdgcn_s_sleep(1);
    }
    FENCE();
}
__device__ __forceinline__ void lds_signal(LAS char* L, unsigned off, int l) {
    WAIT_LGKM0();
    if (l == 0) __hip_atomic_fetch_add((LAS unsigned*)(L + off), 1u, RLX_WG);
    FENCE();
}
__device__ __forceinline__ unsigned get_tag(LAS char* L) {
    lds_wait_ge(L, L_TAG, 1u);
    return __builtin_amdgcn_readfirstlane(*(LAS unsigned*)(L + L_TAG + 4));
}
#define ST(i) do {} while (0)
#define STC(i) do {} while (0)

__device__ __forceinline__ f32x16 attn_true_max(LAS char* kp, LAS char* vp, u32x4 q) {
    f32x16 z; for (int i = 0; i < 16; ++i) z[i] = 0.f;
    float m = -__builtin_inff();
    #pragma unroll 1
    for (int t = 0; t < 64; ++t) {
        f32x16 s = mfma32(*(LAS u32x4*)(kp + t * REC), q, z);
        #pragma unroll
        for (int i = 0; i < 16; ++i) m = fmaxf(m, s[i]);
    }
    m = fmaxf(m, __shfl_xor(m, 32));
    f32x16 acc = z;
    #pragma unroll 1
    for (int t = 0; t < 64; ++t) {
        f32x16 s = mfma32(*(LAS u32x4*)(kp + t * REC), q, z);
        f32x8 p0, p1;
        #pragma unroll
        for (int i = 0; i < 8; ++i) { p0[i] = __builtin_amdgcn_exp2f(s[i] - m); p1[i] = __builtin_amdgcn_exp2f(s[8 + i] - m); }
        acc = smf(*(LAS u32x4*)(vp + t * REC), p0, acc);
        acc = smf(*(LAS u32x4*)(vp + t * REC + 528), p1, acc);
    }
    return acc;
}

__global__ void __launch_bounds__(512, 2) mqa_moe_fused(
        const u16* __restrict__ x, const u16* __restrict__ wq, const u16* __restrict__ wk, const u16* __restrict__ wv,
        const u16* __restrict__ wo, const u16* __restrict__ w_r, const u16* __restrict__ w_gate, const u16* __restrict__ w_up,
        const u16* __restrict__ w_down, const u16* __restrict__ gate_w, u16* __restrict__ out, char* ws) {
    extern __shared__ __attribute__((aligned(16))) char smem[];
    LAS char* L = (LAS char*)smem;
    const unsigned LB = (unsigned)(size_t)L;
    const int l = threadIdx.x & 63;
    const int w = __builtin_amdgcn_readfirstlane(threadIdx.x >> 6);
    const int b = blockIdx.x, xcd = b & 7, j = b >> 3;
    const int tb = xcd * 4 + (j & 3), hg = j >> 2;
    const int g4 = l >> 4, l15 = l & 15;
    const __amdgpu_buffer_rsrc_t rs = __builtin_amdgcn_make_buffer_rsrc((void*)ws, 0, WS_END, 0x00020000);
    const int sg = (l >> 3) & 3, sjh = l >> 5, sq = (l >> 1) & 3;
    const int krow = 8 * sg + 4 * sjh + sq;
    unsigned tag = 0;
    u32x4 qf;
    ST(0); STC(29);

    if (w == 4) {
        const int type = hg & 1, tg = hg >> 1;
        const u16* W = type ? wv : wk;
        const u16* xr = x + (size_t)(tb * 64 + tg * 16 + l15) * 512 + 8 * g4;
        u32x4 xf[16];
        const unsigned wvo = krow * 32 + (l & 1) * 16;
        #pragma unroll
        for (int s = 0; s < 16; ++s) {
            dma16(wvo, (const char*)W + s * 1024, LB + L_WKV + s * 1024);
            xf[s] = *(const GAS u32x4*)(xr + 32 * s);
        }
        unsigned E = __hip_atomic_load((GAS unsigned*)(ws + WS_EPOCH), RLX_AGENT);
        asm volatile("s_barrier" ::: "memory");
        WAIT_VM0();
        ST(1);
        tag = __builtin_amdgcn_readfirstlane(E) + 1u;
        if (l == 0) { u32x2 tv = {1u, tag}; *(LAS u32x2*)(L + L_TAG) = tv; }
        f32x4 acc;
        const unsigned wp = LB + L_WKV + 8 * l;
        if (type == 0) asm volatile("ds_read_b64_tr_b16 v[36:37], %[wp] offset:0\n\tds_read_b64_tr_b16 v[38:39], %[wp] offset:512\n\tds_read_b64_tr_b16 v[40:41], %[wp] offset:1024\n\tds_read_b64_tr_b16 v[42:43], %[wp] offset:1536\n\tds_read_b64_tr_b16 v[44:45], %[wp] offset:2048\n\tds_read_b64_tr_b16 v[46:47], %[wp] offset:2560\n\tds_read_b64_tr_b16 v[48:49], %[wp] offset:3072\n\tds_read_b64_tr_b16 v[50:51], %[wp] offset:3584\n\tds_read_b64_tr_b16 v[52:53], %[wp] offset:4096\n\tds_read_b64_tr_b16 v[54:55], %[wp] offset:4608\n\tds_read_b64_tr_b16 v[56:57], %[wp] offset:5120\n\tds_read_b64_tr_b16 v[58:59], %[wp] offset:5632\n\ts_waitcnt lgkmcnt(10)\n\tv_mfma_f32_16x16x32_bf16 v[32:35], v[36:39], %[x0], 0\n\tds_read_b64_tr_b16 v[36:37], %[wp] offset:6144\n\tds_read_b64_tr_b16 v[38:39], %[wp] offset:6656\n\ts_waitcnt lgkmcnt(10)\n\tv_mfma_f32_16x16x32_bf16 v[32:35], v[40:43], %[x1], v[32:35]\n\tds_read_b64_tr_b16 v[40:41], %[wp] offset:7168\n\tds_read_b64_tr_b16 v[42:43], %[wp] offset:7680\n\ts_waitcnt lgkmcnt(10)\n\tv_mfma_f32_16x16x32_bf16 v[32:35], v[44:47], %[x2], v[32:35]\n\tds_read_b64_tr_b16 v[44:45], %[wp] offset:8192\n\tds_read_b64_tr_b16 v[46:47], %[wp] offset:8704\n\ts_waitcnt lgkmcnt(10)\n\tv_mfma_f32_16x16x32_bf16 v[32:35], v[48:51], %[x3], v[32:35]\n\tds_read_b64_tr_b16 v[48:49], %[wp] offset:9216\n\tds_read_b64_tr_b16 v[50:51], %[wp] offset:9728\n\ts_waitcnt lgkmcnt(10)\n\tv_mfma_f32_16x16x32_bf16 v[32:35], v[52:55], %[x4], v[32:35]\n\tds_read_b64_tr_b16 v[52:53], %[wp] offset:10240\n\tds_read_b64_tr_b16 v[54:55], %[wp] offset:10752\n\ts_waitcnt lgkmcnt(10)\n\tv_mfma_f32_16x16x32_bf16 v[32:35], v[56:59], %[x5], v[32:35]\n\tds_read_b64_tr_b16 v[56:57], %[wp] offset:11264\n\tds_read_b64_tr_b16 v[58:59], %[wp] offset:11776\n\ts_waitcnt lgkmcnt(10)\n\tv_mfma_f32_16x16x32_bf16 v[32:35], v[36:39], %[x6], v[32:35]\n\tds_read_b64_tr_b16 v[36:37], %[wp] offset:12288\n\tds_read_b64_tr_b16 v[38:39], %[wp] offset:12800\n\ts_waitcnt lgkmcnt(10)\n\tv_mfma_f32_16x16x32_bf16 v[32:35], v[40:43], %[x7], v[32:35]\n\tds_read_b64_tr_b16 v[40:41], %[wp] offset:13312\n\tds_read_b64_tr_b16 v[42:43], %[wp] offset:13824\n\ts_waitcnt lgkmcnt(10)\n\tv_mfma_f32_16x16x32_bf16 v[32:35], v[44:47], %[x8], v[32:35]\n\tds_read_b64_tr_b16 v[44:45], %[wp] offset:14336\n\tds_read_b64_tr_b16 v[46:47], %[wp] offset:14848\n\ts_waitcnt lgkmcnt(10)\n\tv_mfma_f32_16x16x32_bf16 v[32:35], v[48:51], %[x9], v[32:35]\n\tds_read_b64_tr_b16 v[48:49], %[wp] offset:15360\n\tds_read_b64_tr_b16 v[50:51], %[wp] offset:15872\n\ts_waitcnt lgkmcnt(10)\n\tv_mfma_f32_16x16x32_bf16 v[32:35], v[52:55], %[x10], v[32:35]\n\ts_waitcnt lgkmcnt(8)\n\tv_mfma_f32_16x16x32_bf16 v[32:35], v[56:59], %[x11], v[32:35]\n\ts_waitcnt lgkmcnt(6)\n\tv_mfma_f32_16x16x32_bf16 v[32:35], v[36:39], %[x12], v[32:35]\n\ts_waitcnt lgkmcnt(4)\n\tv_mfma_f32_16x16x32_bf16 v[32:35], v[40:43], %[x13], v[32:35]\n\ts_waitcnt lgkmcnt(2)\n\tv_mfma_f32_16x16x32_bf16 v[32:35], v[44:47], %[x14], v[32:35]\n\ts_waitcnt lgkmcnt(0)\n\tv_mfma_f32_16x16x32_bf16 v[32:35], v[48:51], %[x15], v[32:35]\n\ts_nop 7\n\ts_nop 1" : "=&{v[32:35]}"(acc) : [wp] "v"(wp), [x0] "v"(xf[0]), [x1] "v"(xf[1]), [x2] "v"(xf[2]), [x3] "v"(xf[3]), [x4] "v"(xf[4]), [x5] "v"(xf[5]), [x6] "v"(xf[6]), [x7] "v"(xf[7]), [x8] "v"(xf[8]), [x9] "v"(xf[9]), [x10] "v"(xf[10]), [x11] "v"(xf[11]), [x12] "v"(xf[12]), [x13] "v"(xf[13]), [x14] "v"(xf[14]), [x15] "v"(xf[15]) : "v36", "v37", "v38", "v39", "v40", "v41", "v42", "v43", "v44", "v45", "v46", "v47", "v48", "v49", "v50", "v51", "v52", "v53", "v54", "v55", "v56", "v57", "v58", "v59", "memory");
        else           asm volatile("ds_read_b64_tr_b16 v[36:37], %[wp] offset:0\n\tds_read_b64_tr_b16 v[38:39], %[wp] offset:512\n\tds_read_b64_tr_b16 v[40:41], %[wp] offset:1024\n\tds_read_b64_tr_b16 v[42:43], %[wp] offset:1536\n\tds_read_b64_tr_b16 v[44:45], %[wp] offset:2048\n\tds_read_b64_tr_b16 v[46:47], %[wp] offset:2560\n\tds_read_b64_tr_b16 v[48:49], %[wp] offset:3072\n\tds_read_b64_tr_b16 v[50:51], %[wp] offset:3584\n\tds_read_b64_tr_b16 v[52:53], %[wp] offset:4096\n\tds_read_b64_tr_b16 v[54:55], %[wp] offset:4608\n\tds_read_b64_tr_b16 v[56:57], %[wp] offset:5120\n\tds_read_b64_tr_b16 v[58:59], %[wp] offset:5632\n\ts_waitcnt lgkmcnt(10)\n\tv_mfma_f32_16x16x32_bf16 v[32:35], %[x0], v[36:39], 0\n\tds_read_b64_tr_b16 v[36:37], %[wp] offset:6144\n\tds_read_b64_tr_b16 v[38:39], %[wp] offset:6656\n\ts_waitcnt lgkmcnt(10)\n\tv_mfma_f32_16x16x32_bf16 v[32:35], %[x1], v[40:43], v[32:35]\n\tds_read_b64_tr_b16 v[40:41], %[wp] offset:7168\n\tds_read_b64_tr_b16 v[42:43], %[wp] offset:7680\n\ts_waitcnt lgkmcnt(10)\n\tv_mfma_f32_16x16x32_bf16 v[32:35], %[x2], v[44:47], v[32:35]\n\tds_read_b64_tr_b16 v[44:45], %[wp] offset:8192\n\tds_read_b64_tr_b16 v[46:47], %[wp] offset:8704\n\ts_waitcnt lgkmcnt(10)\n\tv_mfma_f32_16x16x32_bf16 v[32:35], %[x3], v[48:51], v[32:35]\n\tds_read_b64_tr_b16 v[48:49], %[wp] offset:9216\n\tds_read_b64_tr_b16 v[50:51], %[wp] offset:9728\n\ts_waitcnt lgkmcnt(10)\n\tv_mfma_f32_16x16x32_bf16 v[32:35], %[x4], v[52:55], v[32:35]\n\tds_read_b64_tr_b16 v[52:53], %[wp] offset:10240\n\tds_read_b64_tr_b16 v[54:55], %[wp] offset:10752\n\ts_waitcnt lgkmcnt(10)\n\tv_mfma_f32_16x16x32_bf16 v[32:35], %[x5], v[56:59], v[32:35]\n\tds_read_b64_tr_b16 v[56:57], %[wp] offset:11264\n\tds_read_b64_tr_b16 v[58:59], %[wp] offset:11776\n\ts_waitcnt lgkmcnt(10)\n\tv_mfma_f32_16x16x32_bf16 v[32:35], %[x6], v[36:39], v[32:35]\n\tds_read_b64_tr_b16 v[36:37], %[wp] offset:12288\n\tds_read_b64_tr_b16 v[38:39], %[wp] offset:12800\n\ts_waitcnt lgkmcnt(10)\n\tv_mfma_f32_16x16x32_bf16 v[32:35], %[x7], v[40:43], v[32:35]\n\tds_read_b64_tr_b16 v[40:41], %[wp] offset:13312\n\tds_read_b64_tr_b16 v[42:43], %[wp] offset:13824\n\ts_waitcnt lgkmcnt(10)\n\tv_mfma_f32_16x16x32_bf16 v[32:35], %[x8], v[44:47], v[32:35]\n\tds_read_b64_tr_b16 v[44:45], %[wp] offset:14336\n\tds_read_b64_tr_b16 v[46:47], %[wp] offset:14848\n\ts_waitcnt lgkmcnt(10)\n\tv_mfma_f32_16x16x32_bf16 v[32:35], %[x9], v[48:51], v[32:35]\n\tds_read_b64_tr_b16 v[48:49], %[wp] offset:15360\n\tds_read_b64_tr_b16 v[50:51], %[wp] offset:15872\n\ts_waitcnt lgkmcnt(10)\n\tv_mfma_f32_16x16x32_bf16 v[32:35], %[x10], v[52:55], v[32:35]\n\ts_waitcnt lgkmcnt(8)\n\tv_mfma_f32_16x16x32_bf16 v[32:35], %[x11], v[56:59], v[32:35]\n\ts_waitcnt lgkmcnt(6)\n\tv_mfma_f32_16x16x32_bf16 v[32:35], %[x12], v[36:39], v[32:35]\n\ts_waitcnt lgkmcnt(4)\n\tv_mfma_f32_16x16x32_bf16 v[32:35], %[x13], v[40:43], v[32:35]\n\ts_waitcnt lgkmcnt(2)\n\tv_mfma_f32_16x16x32_bf16 v[32:35], %[x14], v[44:47], v[32:35]\n\ts_waitcnt lgkmcnt(0)\n\tv_mfma_f32_16x16x32_bf16 v[32:35], %[x15], v[48:51], v[32:35]\n\ts_nop 7\n\ts_nop 1" : "=&{v[32:35]}"(acc) : [wp] "v"(wp), [x0] "v"(xf[0]), [x1] "v"(xf[1]), [x2] "v"(xf[2]), [x3] "v"(xf[3]), [x4] "v"(xf[4]), [x5] "v"(xf[5]), [x6] "v"(xf[6]), [x7] "v"(xf[7]), [x8] "v"(xf[8]), [x9] "v"(xf[9]), [x10] "v"(xf[10]), [x11] "v"(xf[11]), [x12] "v"(xf[12]), [x13] "v"(xf[13]), [x14] "v"(xf[14]), [x15] "v"(xf[15]) : "v36", "v37", "v38", "v39", "v40", "v41", "v42", "v43", "v44", "v45", "v46", "v47", "v48", "v49", "v50", "v51", "v52", "v53", "v54", "v55", "v56", "v57", "v58", "v59", "memory");
        unsigned long long pv = (unsigned long long)pk2(acc[0], acc[1]) | ((unsigned long long)pk2(acc[2], acc[3]) << 32);
        unsigned dst;
        if (type == 0) dst = WS_KF + (tb * 2 + (tg >> 1)) * 1024 + ((tg & 1) * 16 + l15 + 32 * (g4 & 1)) * 16 + (g4 >> 1) * 8;
        else           dst = WS_VF + (tb * 4 + tg) * 512 + (g4 >> 1) * 256 + l15 * 16 + (g4 & 1) * 8;
        __hip_atomic_store((GAS unsigned long long*)(ws + dst), pv, RLX_AGENT);
        lds_signal(L, L_C5, l);
        WAIT_VM0();
        ST(2);
        const int kt = tb * 2 + (tg >> 1);
        const int p = (kt & 3) * 64 + (kt >> 2) * 4 + (tg & 1) * 2 + type;
        if (l < 8) __hip_atomic_store((GAS unsigned*)(ws + WS_F1 + l * 1024 + p * 4), tag, RLX_AGENT);
    } else if (w == 5) {
        const int type = hg & 1, tg = hg >> 1;
        const int wrow = 16 * (sg >> 1) + 8 * sjh + 4 * (sg & 1) + sq;
        const char* wsrc = (const char*)(wo + (size_t)(64 * (b & 7)) * 512 + 16 * (b >> 3));
        const unsigned wovo = wrow * 1024 + (l & 1) * 16;
        dma16(wovo, wsrc, LB + L_WOST);
        dma16(wovo, wsrc + 32 * 1024, LB + L_WOST + 1024);
        u32x4 af[16];
        if (type == 0) {
            const u16* ap = ((l & 1) ? gate_w : w_r) + 8 * g4;
            #pragma unroll
            for (int s = 0; s < 16; ++s) af[s] = *(const GAS u32x4*)(ap + 32 * s);
        }
        asm volatile("s_barrier" ::: "memory");
        {
            u32x4 sent = {0xFFFFFFFFu, 0xFFFFFFFFu, 0xFFFFFFFFu, 0xFFFFFFFFu};
            #pragma unroll
            for (int i = 0; i < 8; ++i)
                __builtin_amdgcn_raw_buffer_store_b128(sent, rs, l * 16, WS_OF + ((tb * 2 + (i >> 2)) * 32 + 4 * hg + (i & 3)) * 1024, 16);
        }
        WAIT_VM0();
        #pragma unroll
        for (int i = 0; i < 2; ++i) {
            u32x4 f = tr_frag(L + L_WOST + i * 1024 + 8 * l);
            __builtin_amdgcn_raw_buffer_store_b128(f, rs, l * 16, WS_WOF + (2 * b + i) * 1024, 16);
        }
        lds_signal(L, L_C5, l);
        tag = get_tag(L);
        unsigned key = 0;
        if (type == 0) {
            lds_wait_ge(L, L_C2, 4u);
            f32x4 acc = {0.f, 0.f, 0.f, 0.f};
            LAS char* xp = L + L_XT + (tg * 16 + l15) * 1040 + g4 * 16;
            u32x4 bx[16];
            #pragma unroll
            for (int s = 0; s < 16; ++s) bx[s] = *(LAS u32x4*)(xp + s * 64);
            __builtin_amdgcn_sched_barrier(0);
            #pragma unroll
            for (int s = 0; s < 16; ++s) acc = mfma16(af[s], bx[s], acc);
            const int tok = tb * 64 + tg * 16 + l15;
            float gt = bfr(sigmoidf_(bfr(acc[1])));
            if (l < 16) __hip_atomic_store((GAS unsigned*)(ws + WS_GATE) + tok, __builtin_bit_cast(unsigned, gt), RLX_AGENT);
            unsigned u = pk2(acc[0], acc[0]) & 0xffffu;
            unsigned ord = (u & 0x8000u) ? (~u & 0xffffu) : (u | 0x8000u);
            if ((u & 0x7fffu) > 0x7f80u) ord = 0u;
            key = (l < 16) ? ((ord << 16) | (unsigned)(2047 - tok)) : 0u;
            #pragma unroll
            for (int o = 1; o < 16; o <<= 1) { unsigned k2 = __shfl_xor(key, o); key = key > k2 ? key : k2; }
            key = __builtin_amdgcn_readfirstlane(key);
        }
        WAIT_VM0();
        if (l < 8) __hip_atomic_store((GAS unsigned long long*)(ws + WS_F2 + l * 2048 + b * 8), ((unsigned long long)tag << 32) | key, RLX_AGENT);
        ST(1);
    } else {
        if (w == 0 && l < 16) *(LAS unsigned*)(L + L_MISC + 4 * l) = 0u;
        BARRIER();
        if (w < 4) {
            #pragma unroll 4
            for (int i = 0; i < 16; ++i) {
                const int row = 16 * w + i;
                dma16(l * 16, (const char*)x + (size_t)(tb * 64 + row) * 1024, LB + L_XT + row * 1040);
            }
            const unsigned qvo = (l >> 3) * 1024 + (((l & 7) ^ (((l >> 4) & 1) << 2)) * 16);
            #pragma unroll 4
            for (int i = 0; i < 16; ++i) {
                const int blk = 16 * w + i;
                dma16(qvo, (const char*)wq + (size_t)blk * 8192 + hg * 128, LB + L_WQ + blk * 1024);
            }
            WAIT_VM0();
            ST(1);
            lds_signal(L, L_C2, l);
            lds_wait_ge(L, L_C2, 4u);
            ST(2);
            const int cp = w & 1, th = w >> 1, h = l >> 5, nhi = g4 & 1, q = (l >> 2) & 3, p = l & 3;
            const unsigned ap = LB + L_WQ + h * 1024 + q * 128 + ((64 * cp + 32 * nhi + 8 * p) ^ (((q >> 1) & 1) << 6));
            const unsigned bp = LB + L_XT + (32 * th + (l & 31)) * 1040 + h * 16;
            f32x16 acc;
            asm volatile("ds_read_b64_tr_b16 v[100:101], %[ap] offset:0\n\tds_read_b64_tr_b16 v[102:103], %[ap] offset:512\n\tds_read_b128 v[104:107], %[bp] offset:0\n\tds_read_b64_tr_b16 v[108:109], %[ap] offset:2048\n\tds_read_b64_tr_b16 v[110:111], %[ap] offset:2560\n\tds_read_b128 v[112:115], %[bp] offset:32\n\tds_read_b64_tr_b16 v[116:117], %[ap] offset:4096\n\tds_read_b64_tr_b16 v[118:119], %[ap] offset:4608\n\tds_read_b128 v[120:123], %[bp] offset:64\n\tds_read_b64_tr_b16 v[124:125], %[ap] offset:6144\n\tds_read_b64_tr_b16 v[126:127], %[ap] offset:6656\n\tds_read_b128 v[128:131], %[bp] offset:96\n\ts_waitcnt lgkmcnt(9)\n\tv_mfma_f32_32x32x16_bf16 v[32:47], v[100:103], v[104:107], 0\n\tds_read_b64_tr_b16 v[100:101], %[ap] offset:8192\n\tds_read_b64_tr_b16 v[102:103], %[ap] offset:8704\n\tds_read_b128 v[104:107], %[bp] offset:128\n\ts_waitcnt lgkmcnt(9)\n\tv_mfma_f32_32x32x16_bf16 v[32:47], v[108:111], v[112:115], v[32:47]\n\tds_read_b64_tr_b16 v[108:109], %[ap] offset:10240\n\tds_read_b64_tr_b16 v[110:111], %[ap] offset:10752\n\tds_read_b128 v[112:115], %[bp] offset:160\n\ts_waitcnt lgkmcnt(9)\n\tv_mfma_f32_32x32x16_bf16 v[32:47], v[116:119], v[120:123], v[32:47]\n\tds_read_b64_tr_b16 v[116:117], %[ap] offset:12288\n\tds_read_b64_tr_b16 v[118:119], %[ap] offset:12800\n\tds_read_b128 v[120:123], %[bp] offset:192\n\ts_waitcnt lgkmcnt(9)\n\tv_mfma_f32_32x32x16_bf16 v[32:47], v[124:127], v[128:131], v[32:47]\n\tds_read_b64_tr_b16 v[124:125], %[ap] offset:14336\n\tds_read_b64_tr_b16 v[126:127], %[ap] offset:14848\n\tds_read_b128 v[128:131], %[bp] offset:224\n\ts_waitcnt lgkmcnt(9)\n\tv_mfma_f32_32x32x16_bf16 v[32:47], v[100:103], v[104:107], v[32:47]\n\tds_read_b64_tr_b16 v[100:101], %[ap] offset:16384\n\tds_read_b64_tr_b16 v[102:103], %[ap] offset:16896\n\tds_read_b128 v[104:107], %[bp] offset:256\n\ts_waitcnt lgkmcnt(9)\n\tv_mfma_f32_32x32x16_bf16 v[32:47], v[108:111], v[112:115], v[32:47]\n\tds_read_b64_tr_b16 v[108:109], %[ap] offset:18432\n\tds_read_b64_tr_b16 v[110:111], %[ap] offset:18944\n\tds_read_b128 v[112:115], %[bp] offset:288\n\ts_waitcnt lgkmcnt(9)\n\tv_mfma_f32_32x32x16_bf16 v[32:47], v[116:119], v[120:123], v[32:47]\n\tds_read_b64_tr_b16 v[116:117], %[ap] offset:20480\n\tds_read_b64_tr_b16 v[118:119], %[ap] offset:20992\n\tds_read_b128 v[120:123], %[bp] offset:320\n\ts_waitcnt lgkmcnt(9)\n\tv_mfma_f32_32x32x16_bf16 v[32:47], v[124:127], v[128:131], v[32:47]\n\tds_read_b64_tr_b16 v[124:125], %[ap] offset:22528\n\tds_read_b64_tr_b16 v[126:127], %[ap] offset:23040\n\tds_read_b128 v[128:131], %[bp] offset:352\n\ts_waitcnt lgkmcnt(9)\n\tv_mfma_f32_32x32x16_bf16 v[32:47], v[100:103], v[104:107], v[32:47]\n\tds_read_b64_tr_b16 v[100:101], %[ap] offset:24576\n\tds_read_b64_tr_b16 v[102:103], %[ap] offset:25088\n\tds_read_b128 v[104:107], %[bp] offset:384\n\ts_waitcnt lgkmcnt(9)\n\tv_mfma_f32_32x32x16_bf16 v[32:47], v[108:111], v[112:115], v[32:47]\n\tds_read_b64_tr_b16 v[108:109], %[ap] offset:26624\n\tds_read_b64_tr_b16 v[110:111], %[ap] offset:27136\n\tds_read_b128 v[112:115], %[bp] offset:416\n\ts_waitcnt lgkmcnt(9)\n\tv_mfma_f32_32x32x16_bf16 v[32:47], v[116:119], v[120:123], v[32:47]\n\tds_read_b64_tr_b16 v[116:117], %[ap] offset:28672\n\tds_read_b64_tr_b16 v[118:119], %[ap] offset:29184\n\tds_read_b128 v[120:123], %[bp] offset:448\n\ts_waitcnt lgkmcnt(9)\n\tv_mfma_f32_32x32x16_bf16 v[32:47], v[124:127], v[128:131], v[32:47]\n\tds_read_b64_tr_b16 v[124:125], %[ap] offset:30720\n\tds_read_b64_tr_b16 v[126:127], %[ap] offset:31232\n\tds_read_b128 v[128:131], %[bp] offset:480\n\ts_waitcnt lgkmcnt(9)\n\tv_mfma_f32_32x32x16_bf16 v[32:47], v[100:103], v[104:107], v[32:47]\n\tds_read_b64_tr_b16 v[100:101], %[ap] offset:32768\n\tds_read_b64_tr_b16 v[102:103], %[ap] offset:33280\n\tds_read_b128 v[104:107], %[bp] offset:512\n\ts_waitcnt lgkmcnt(9)\n\tv_mfma_f32_32x32x16_bf16 v[32:47], v[108:111], v[112:115], v[32:47]\n\tds_read_b64_tr_b16 v[108:109], %[ap] offset:34816\n\tds_read_b64_tr_b16 v[110:111], %[ap] offset:35328\n\tds_read_b128 v[112:115], %[bp] offset:544\n\ts_waitcnt lgkmcnt(9)\n\tv_mfma_f32_32x32x16_bf16 v[32:47], v[116:119], v[120:123], v[32:47]\n\tds_read_b64_tr_b16 v[116:117], %[ap] offset:36864\n\tds_read_b64_tr_b16 v[118:119], %[ap] offset:37376\n\tds_read_b128 v[120:123], %[bp] offset:576\n\ts_waitcnt lgkmcnt(9)\n\tv_mfma_f32_32x32x16_bf16 v[32:47], v[124:127], v[128:131], v[32:47]\n\tds_read_b64_tr_b16 v[124:125], %[ap] offset:38912\n\tds_read_b64_tr_b16 v[126:127], %[ap] offset:39424\n\tds_read_b128 v[128:131], %[bp] offset:608\n\ts_waitcnt lgkmcnt(9)\n\tv_mfma_f32_32x32x16_bf16 v[32:47], v[100:103], v[104:107], v[32:47]\n\tds_read_b64_tr_b16 v[100:101], %[ap] offset:40960\n\tds_read_b64_tr_b16 v[102:103], %[ap] offset:41472\n\tds_read_b128 v[104:107], %[bp] offset:640\n\ts_waitcnt lgkmcnt(9)\n\tv_mfma_f32_32x32x16_bf16 v[32:47], v[108:111], v[112:115], v[32:47]\n\tds_read_b64_tr_b16 v[108:109], %[ap] offset:43008\n\tds_read_b64_tr_b16 v[110:111], %[ap] offset:43520\n\tds_read_b128 v[112:115], %[bp] offset:672\n\ts_waitcnt lgkmcnt(9)\n\tv_mfma_f32_32x32x16_bf16 v[32:47], v[116:119], v[120:123], v[32:47]\n\tds_read_b64_tr_b16 v[116:117], %[ap] offset:45056\n\tds_read_b64_tr_b16 v[118:119], %[ap] offset:45568\n\tds_read_b128 v[120:123], %[bp] offset:704\n\ts_waitcnt lgkmcnt(9)\n\tv_mfma_f32_32x32x16_bf16 v[32:47], v[124:127], v[128:131], v[32:47]\n\tds_read_b64_tr_b16 v[124:125], %[ap] offset:47104\n\tds_read_b64_tr_b16 v[126:127], %[ap] offset:47616\n\tds_read_b128 v[128:131], %[bp] offset:736\n\ts_waitcnt lgkmcnt(9)\n\tv_mfma_f32_32x32x16_bf16 v[32:47], v[100:103], v[104:107], v[32:47]\n\tds_read_b64_tr_b16 v[100:101], %[ap] offset:49152\n\tds_read_b64_tr_b16 v[102:103], %[ap] offset:49664\n\tds_read_b128 v[104:107], %[bp] offset:768\n\ts_waitcnt lgkmcnt(9)\n\tv_mfma_f32_32x32x16_bf16 v[32:47], v[108:111], v[112:115], v[32:47]\n\tds_read_b64_tr_b16 v[108:109], %[ap] offset:51200\n\tds_read_b64_tr_b16 v[110:111], %[ap] offset:51712\n\tds_read_b128 v[112:115], %[bp] offset:800\n\ts_waitcnt lgkmcnt(9)\n\tv_mfma_f32_32x32x16_bf16 v[32:47], v[116:119], v[120:123], v[32:47]\n\tds_read_b64_tr_b16 v[116:117], %[ap] offset:53248\n\tds_read_b64_tr_b16 v[118:119], %[ap] offset:53760\n\tds_read_b128 v[120:123], %[bp] offset:832\n\ts_waitcnt lgkmcnt(9)\n\tv_mfma_f32_32x32x16_bf16 v[32:47], v[124:127], v[128:131], v[32:47]\n\tds_read_b64_tr_b16 v[124:125], %[ap] offset:55296\n\tds_read_b64_tr_b16 v[126:127], %[ap] offset:55808\n\tds_read_b128 v[128:131], %[bp] offset:864\n\ts_waitcnt lgkmcnt(9)\n\tv_mfma_f32_32x32x16_bf16 v[32:47], v[100:103], v[104:107], v[32:47]\n\tds_read_b64_tr_b16 v[100:101], %[ap] offset:57344\n\tds_read_b64_tr_b16 v[102:103], %[ap] offset:57856\n\tds_read_b128 v[104:107], %[bp] offset:896\n\ts_waitcnt lgkmcnt(9)\n\tv_mfma_f32_32x32x16_bf16 v[32:47], v[108:111], v[112:115], v[32:47]\n\tds_read_b64_tr_b16 v[108:109], %[ap] offset:59392\n\tds_read_b64_tr_b16 v[110:111], %[ap] offset:59904\n\tds_read_b128 v[112:115], %[bp] offset:928\n\ts_waitcnt lgkmcnt(9)\n\tv_mfma_f32_32x32x16_bf16 v[32:47], v[116:119], v[120:123], v[32:47]\n\tds_read_b64_tr_b16 v[116:117], %[ap] offset:61440\n\tds_read_b64_tr_b16 v[118:119], %[ap] offset:61952\n\tds_read_b128 v[120:123], %[bp] offset:960\n\ts_waitcnt lgkmcnt(9)\n\tv_mfma_f32_32x32x16_bf16 v[32:47], v[124:127], v[128:131], v[32:47]\n\tds_read_b64_tr_b16 v[124:125], %[ap] offset:63488\n\tds_read_b64_tr_b16 v[126:127], %[ap] offset:64000\n\tds_read_b128 v[128:131], %[bp] offset:992\n\ts_waitcnt lgkmcnt(9)\n\tv_mfma_f32_32x32x16_bf16 v[32:47], v[100:103], v[104:107], v[32:47]\n\ts_waitcnt lgkmcnt(6)\n\tv_mfma_f32_32x32x16_bf16 v[32:47], v[108:111], v[112:115], v[32:47]\n\ts_waitcnt lgkmcnt(3)\n\tv_mfma_f32_32x32x16_bf16 v[32:47], v[116:119], v[120:123], v[32:47]\n\ts_waitcnt lgkmcnt(0)\n\tv_mfma_f32_32x32x16_bf16 v[32:47], v[124:127], v[128:131], v[32:47]\n\ts_nop 15\n\ts_nop 3" : "=&{v[32:47]}"(acc) : [ap] "v"(ap), [bp] "v"(bp) : "v100", "v101", "v102", "v103", "v104", "v105", "v106", "v107", "v108", "v109", "v110", "v111", "v112", "v113", "v114", "v115", "v116", "v117", "v118", "v119", "v120", "v121", "v122", "v123", "v124", "v125", "v126", "v127", "v128", "v129", "v130", "v131", "memory");
            const float sc = 0.25f * 1.4426950408889634f;
            u32x4 qo;
            #pragma unroll
            for (int i = 0; i < 4; ++i) { qf[i] = pk2(acc[2 * i] * sc, acc[2 * i + 1] * sc); qo[i] = pk2(acc[8 + 2 * i] * sc, acc[9 + 2 * i] * sc); }
            *(LAS u32x4*)(L + L_QX + w * 1024 + l * 16) = qo;
            ST(3);
        }
    }

    const int rep = b & 7;
    if (w >= 4) {
        const int bw = w - 4;
        if (w >= 6) tag = get_tag(L);
        {
            GAS unsigned* fp = (GAS unsigned*)(ws + WS_F1 + rep * 1024) + bw * 64 + l;
            _Pragma("nounroll") for (int i = 0; i < SPIN_CAP; ++i) {
                unsigned v = __hip_atomic_load(fp, RLX_AGENT);
                if (__all(v == tag)) break;
                __builtin_amdgcn_s_sleep(1);
            }
            FENCE();
        }
        ST(4);
        u32x4 kf[16], vf[16];
        #pragma unroll
        for (int i = 0; i < 16; ++i) {
            kf[i] = __builtin_amdgcn_raw_buffer_load_b128(rs, l * 16, WS_KF + (4 * i + bw) * 1024, 16);
            vf[i] = __builtin_amdgcn_raw_buffer_load_b128(rs, l * 16, WS_VF + (4 * i + bw) * 1024, 16);
        }
        lds_wait_ge(L, L_C5, 2u);
        LAS char* kd = L + bw * REC + l * 16;
        LAS char* vd = L + bw * REC + 1024 + (l >> 5) * 528 + (l & 31) * 16;
        const u32x4 ones = {0x3f803f80u, 0x3f803f80u, 0x3f803f80u, 0x3f803f80u};
        LAS char* od = L + ((l >> 1) * 4 + bw) * REC + 1024 + (l & 1) * 528 + 512;
        #pragma unroll
        for (int i = 0; i < 3; ++i) { *(LAS u32x4*)(kd + i * 4 * REC) = kf[i]; *(LAS u32x4*)(vd + i * 4 * REC) = vf[i]; }
        if (l < 6) *(LAS u32x4*)od = ones;
        ST(5);
        BARRIER();
        ST(6);
        qf = *(LAS u32x4*)(L + L_QX + bw * 1024 + l * 16);
        #pragma unroll
        for (int i = 3; i < 16; ++i) { *(LAS u32x4*)(kd + i * 4 * REC) = kf[i]; *(LAS u32x4*)(vd + i * 4 * REC) = vf[i]; }
        if (l >= 6 && l < 32) *(LAS u32x4*)od = ones;
        lds_signal(L, L_C4, l);
        if (b == 0 && w == 4 && l == 0) __hip_atomic_store((GAS unsigned*)(ws + WS_EPOCH), tag, RLX_AGENT);
    } else {
        BARRIER();
        ST(6);
        tag = get_tag(L);
    }

    const int ft = xcd * 28 + j;
    if (j < 28) {
        const u16* wsrc = ((l & 1) ? w_up : w_gate) + (size_t)krow * 1792 + 8 * ft;
        #pragma unroll
        for (int i = 0; i < 2; ++i) { const int s = 2 * w + i; dma16p(wsrc + (size_t)s * 32 * 1792, LB + L_FW1 + s * 1024); }
    }
    if (w < 7) dma16(krow * 1024 + (l & 1) * 16, (const char*)(w_down + (size_t)(224 * xcd + 32 * w) * 512 + 16 * j), LB + L_FW2 + w * 1024);

    const int hl = 2 * (w & 1) + (w >> 2), tha = (w >> 1) & 1;
    unsigned kp = LB + l * 16;
    unsigned vp = LB + 1024 + ((l & 31) < 16 ? (l >> 5) * 256 + (l & 31) * 16 : 512);
    const int idxv = (int)0xDDDDDDDD;
    f32x16 X, Y, acc; u32x4 KR, V0, V1;
    for (int i = 0; i < 16; ++i) acc[i] = 0.f;
    if (w >= 4) __builtin_amdgcn_s_setprio(1);
    ST(7); STC(30);
    asm volatile("s_branch MQA_OVER\n\tMQA_BODY:\n\tv_exp_f32 v32, v32\n\tv_exp_f32 v33, v33\n\ts_waitcnt lgkmcnt(2)\n\tv_smfmac_f32_32x32x32_bf16 v[64:79], v[88:91], v[56:63], v96\n\tds_read_b128 v[88:91], v98 offset:528\n\tv_exp_f32 v34, v34\n\tv_exp_f32 v35, v35\n\tv_exp_f32 v36, v36\n\tv_exp_f32 v37, v37\n\ts_waitcnt lgkmcnt(2)\n\tv_mfma_f32_32x32x16_bf16 v[48:63], v[80:83], v[92:95], 0\n\tds_read_b128 v[80:83], v97 offset:4160\n\tv_exp_f32 v38, v38\n\tv_exp_f32 v39, v39\n\tv_exp_f32 v40, v40\n\tv_exp_f32 v41, v41\n\ts_waitcnt lgkmcnt(2)\n\tv_smfmac_f32_32x32x32_bf16 v[64:79], v[84:87], v[32:39], v96\n\tds_read_b128 v[84:87], v98 offset:2080\n\tv_exp_f32 v42, v42\n\tv_exp_f32 v43, v43\n\tv_exp_f32 v44, v44\n\tv_exp_f32 v45, v45\n\tv_exp_f32 v46, v46\n\tv_exp_f32 v47, v47\n\tv_exp_f32 v48, v48\n\tv_exp_f32 v49, v49\n\ts_waitcnt lgkmcnt(2)\n\tv_smfmac_f32_32x32x32_bf16 v[64:79], v[88:91], v[40:47], v96\n\tds_read_b128 v[88:91], v98 offset:2608\n\tv_exp_f32 v50, v50\n\tv_exp_f32 v51, v51\n\tv_exp_f32 v52, v52\n\tv_exp_f32 v53, v53\n\ts_waitcnt lgkmcnt(2)\n\tv_mfma_f32_32x32x16_bf16 v[32:47], v[80:83], v[92:95], 0\n\tds_read_b128 v[80:83], v97 offset:6240\n\tv_exp_f32 v54, v54\n\tv_exp_f32 v55, v55\n\tv_exp_f32 v56, v56\n\tv_exp_f32 v57, v57\n\ts_waitcnt lgkmcnt(2)\n\tv_smfmac_f32_32x32x32_bf16 v[64:79], v[84:87], v[48:55], v96\n\tds_read_b128 v[84:87], v98 offset:4160\n\tv_exp_f32 v58, v58\n\tv_exp_f32 v59, v59\n\tv_exp_f32 v60, v60\n\tv_exp_f32 v61, v61\n\tv_exp_f32 v62, v62\n\tv_exp_f32 v63, v63\n\tv_exp_f32 v32, v32\n\tv_exp_f32 v33, v33\n\ts_waitcnt lgkmcnt(2)\n\tv_smfmac_f32_32x32x32_bf16 v[64:79], v[88:91], v[56:63], v96\n\tds_read_b128 v[88:91], v98 offset:4688\n\tv_exp_f32 v34, v34\n\tv_exp_f32 v35, v35\n\tv_exp_f32 v36, v36\n\tv_exp_f32 v37, v37\n\ts_waitcnt lgkmcnt(2)\n\tv_mfma_f32_32x32x16_bf16 v[48:63], v[80:83], v[92:95], 0\n\tds_read_b128 v[80:83], v97 offset:8320\n\tv_exp_f32 v38, v38\n\tv_exp_f32 v39, v39\n\tv_exp_f32 v40, v40\n\tv_exp_f32 v41, v41\n\ts_waitcnt lgkmcnt(2)\n\tv_smfmac_f32_32x32x32_bf16 v[64:79], v[84:87], v[32:39], v96\n\tds_read_b128 v[84:87], v98 offset:6240\n\tv_exp_f32 v42, v42\n\tv_exp_f32 v43, v43\n\tv_exp_f32 v44, v44\n\tv_exp_f32 v45, v45\n\tv_exp_f32 v46, v46\n\tv_exp_f32 v47, v47\n\tv_exp_f32 v48, v48\n\tv_exp_f32 v49, v49\n\ts_waitcnt lgkmcnt(2)\n\tv_smfmac_f32_32x32x32_bf16 v[64:79], v[88:91], v[40:47], v96\n\tds_read_b128 v[88:91], v98 offset:6768\n\tv_exp_f32 v50, v50\n\tv_exp_f32 v51, v51\n\tv_exp_f32 v52, v52\n\tv_exp_f32 v53, v53\n\ts_waitcnt lgkmcnt(2)\n\tv_mfma_f32_32x32x16_bf16 v[32:47], v[80:83], v[92:95], 0\n\tds_read_b128 v[80:83], v97 offset:10400\n\tv_exp_f32 v54, v54\n\tv_exp_f32 v55, v55\n\tv_exp_f32 v56, v56\n\tv_exp_f32 v57, v57\n\ts_waitcnt lgkmcnt(2)\n\tv_smfmac_f32_32x32x32_bf16 v[64:79], v[84:87], v[48:55], v96\n\tds_read_b128 v[84:87], v98 offset:8320\n\tv_exp_f32 v58, v58\n\tv_exp_f32 v59, v59\n\tv_exp_f32 v60, v60\n\tv_exp_f32 v61, v61\n\tv_exp_f32 v62, v62\n\tv_exp_f32 v63, v63\n\tv_exp_f32 v32, v32\n\tv_exp_f32 v33, v33\n\ts_waitcnt lgkmcnt(2)\n\tv_smfmac_f32_32x32x32_bf16 v[64:79], v[88:91], v[56:63], v96\n\tds_read_b128 v[88:91], v98 offset:8848\n\tv_exp_f32 v34, v34\n\tv_exp_f32 v35, v35\n\tv_exp_f32 v36, v36\n\tv_exp_f32 v37, v37\n\ts_waitcnt lgkmcnt(2)\n\tv_mfma_f32_32x32x16_bf16 v[48:63], v[80:83], v[92:95], 0\n\tds_read_b128 v[80:83], v97 offset:12480\n\tv_exp_f32 v38, v38\n\tv_exp_f32 v39, v39\n\tv_exp_f32 v40, v40\n\tv_exp_f32 v41, v41\n\ts_waitcnt lgkmcnt(2)\n\tv_smfmac_f32_32x32x32_bf16 v[64:79], v[84:87], v[32:39], v96\n\tds_read_b128 v[84:87], v98 offset:10400\n\tv_exp_f32 v42, v42\n\tv_exp_f32 v43, v43\n\tv_exp_f32 v44, v44\n\tv_exp_f32 v45, v45\n\tv_exp_f32 v46, v46\n\tv_exp_f32 v47, v47\n\tv_exp_f32 v48, v48\n\tv_exp_f32 v49, v49\n\ts_waitcnt lgkmcnt(2)\n\tv_smfmac_f32_32x32x32_bf16 v[64:79], v[88:91], v[40:47], v96\n\tds_read_b128 v[88:91], v98 offset:10928\n\tv_exp_f32 v50, v50\n\tv_exp_f32 v51, v51\n\tv_exp_f32 v52, v52\n\tv_exp_f32 v53, v53\n\ts_waitcnt lgkmcnt(2)\n\tv_mfma_f32_32x32x16_bf16 v[32:47], v[80:83], v[92:95], 0\n\tds_read_b128 v[80:83], v97 offset:14560\n\tv_exp_f32 v54, v54\n\tv_exp_f32 v55, v55\n\tv_exp_f32 v56, v56\n\tv_exp_f32 v57, v57\n\ts_waitcnt lgkmcnt(2)\n\tv_smfmac_f32_32x32x32_bf16 v[64:79], v[84:87], v[48:55], v96\n\tds_read_b128 v[84:87], v98 offset:12480\n\tv_exp_f32 v58, v58\n\tv_exp_f32 v59, v59\n\tv_exp_f32 v60, v60\n\tv_exp_f32 v61, v61\n\tv_exp_f32 v62, v62\n\tv_exp_f32 v63, v63\n\tv_exp_f32 v32, v32\n\tv_exp_f32 v33, v33\n\ts_waitcnt lgkmcnt(2)\n\tv_smfmac_f32_32x32x32_bf16 v[64:79], v[88:91], v[56:63], v96\n\tds_read_b128 v[88:91], v98 offset:13008\n\tv_exp_f32 v34, v34\n\tv_exp_f32 v35, v35\n\tv_exp_f32 v36, v36\n\tv_exp_f32 v37, v37\n\ts_waitcnt lgkmcnt(2)\n\tv_mfma_f32_32x32x16_bf16 v[48:63], v[80:83], v[92:95], 0\n\tds_read_b128 v[80:83], v97 offset:16640\n\tv_exp_f32 v38, v38\n\tv_exp_f32 v39, v39\n\tv_exp_f32 v40, v40\n\tv_exp_f32 v41, v41\n\ts_waitcnt lgkmcnt(2)\n\tv_smfmac_f32_32x32x32_bf16 v[64:79], v[84:87], v[32:39], v96\n\tds_read_b128 v[84:87], v98 offset:14560\n\tv_exp_f32 v42, v42\n\tv_exp_f32 v43, v43\n\tv_exp_f32 v44, v44\n\tv_exp_f32 v45, v45\n\tv_exp_f32 v46, v46\n\tv_exp_f32 v47, v47\n\tv_exp_f32 v48, v48\n\tv_exp_f32 v49, v49\n\ts_waitcnt lgkmcnt(2)\n\tv_smfmac_f32_32x32x32_bf16 v[64:79], v[88:91], v[40:47], v96\n\tds_read_b128 v[88:91], v98 offset:15088\n\tv_exp_f32 v50, v50\n\tv_exp_f32 v51, v51\n\tv_exp_f32 v52, v52\n\tv_exp_f32 v53, v53\n\ts_waitcnt lgkmcnt(2)\n\tv_mfma_f32_32x32x16_bf16 v[32:47], v[80:83], v[92:95], 0\n\tds_read_b128 v[80:83], v97 offset:18720\n\tv_exp_f32 v54, v54\n\tv_exp_f32 v55, v55\n\tv_exp_f32 v56, v56\n\tv_exp_f32 v57, v57\n\ts_waitcnt lgkmcnt(2)\n\tv_smfmac_f32_32x32x32_bf16 v[64:79], v[84:87], v[48:55], v96\n\tds_read_b128 v[84:87], v98 offset:16640\n\tv_exp_f32 v58, v58\n\tv_exp_f32 v59, v59\n\tv_exp_f32 v60, v60\n\tv_exp_f32 v61, v61\n\tv_exp_f32 v62, v62\n\tv_exp_f32 v63, v63\n\tv_add_u32 v97, 16640, v97\n\tv_add_u32 v98, 16640, v98\n\ts_sub_u32 s78, s78, 1\n\ts_cmp_lg_u32 s78, 0\n\ts_cbranch_scc1 MQA_BODY\n\ts_waitcnt lgkmcnt(0)\n\ts_nop 7\n\ts_setpc_b64 s[76:77]\n\tMQA_OVER:\n\tds_read_b128 v[80:83], v97 offset:0\n\tds_read_b128 v[84:87], v98 offset:0\n\ts_waitcnt lgkmcnt(1)\n\tv_mfma_f32_32x32x16_bf16 v[32:47], v[80:83], v[92:95], 0\n\tds_read_b128 v[80:83], v97 offset:2080\n\tds_read_b128 v[88:91], v98 offset:528\n\tv_mov_b32 v56, 0\n\tv_mov_b32 v57, 0\n\tv_mov_b32 v58, 0\n\tv_mov_b32 v59, 0\n\tv_mov_b32 v60, 0\n\tv_mov_b32 v61, 0\n\tv_mov_b32 v62, 0\n\tv_mov_b32 v63, 0\n\tv_mov_b32 v48, 0\n\tv_mov_b32 v49, 0\n\tv_mov_b32 v50, 0\n\tv_mov_b32 v51, 0\n\tv_mov_b32 v52, 0\n\tv_mov_b32 v53, 0\n\tv_mov_b32 v54, 0\n\tv_mov_b32 v55, 0\n\ts_nop 7\n\ts_nop 3\n\ts_waitcnt lgkmcnt(0)"
                 : "={v[32:47]}"(X), "={v[48:63]}"(Y), "={v[80:83]}"(KR), "={v[84:87]}"(V0), "={v[88:91]}"(V1)
                 : "{v[92:95]}"(qf), "{v97}"(kp), "{v98}"(vp) : "memory");
#define SEG(N) asm volatile("s_mov_b32 s78, %[n]\n\ts_getpc_b64 s[76:77]\n\ts_add_u32 s76, s76, 12\n\ts_addc_u32 s77, s77, 0\n\ts_branch MQA_BODY" \
                     : "+{v[32:47]}"(X), "+{v[48:63]}"(Y), "+{v[64:79]}"(acc), "+{v[80:83]}"(KR), "+{v[84:87]}"(V0), "+{v[88:91]}"(V1), \
                       "+{v97}"(kp), "+{v98}"(vp) \
                     : [n] "n"(N), "{v[92:95]}"(qf), "{v96}"(idxv) : "s76", "s77", "s78", "scc", "memory")
    const int tho = hg >> 2, cg = hg & 3;
    const int n0 = cg * 128 + 16 * w;
    u32x4 fl0, fl1;
    if (w == 0) {
        fl0 = __builtin_amdgcn_raw_buffer_load_b128(rs, l * 16, WS_F2 + rep * 2048, 16);
        fl1 = __builtin_amdgcn_raw_buffer_load_b128(rs, l * 16, WS_F2 + rep * 2048 + 1024, 16);
    }
    SEG(1);
    ST(12);
    WAIT_VM0();
    lds_signal(L, L_C7, l);
    if (w == 0) {
        _Pragma("nounroll") for (int i = 0; i < SPIN_CAP; ++i) {
            if (__all(fl0[1] == tag && fl0[3] == tag && fl1[1] == tag && fl1[3] == tag)) break;
            FENCE();
            fl0 = __builtin_amdgcn_raw_buffer_load_b128(rs, l * 16, WS_F2 + rep * 2048, 16);
            fl1 = __builtin_amdgcn_raw_buffer_load_b128(rs, l * 16, WS_F2 + rep * 2048 + 1024, 16);
        }
        unsigned key = max(max(fl0[0], fl0[2]), max(fl1[0], fl1[2]));
        #pragma unroll
        for (int o = 1; o < 64; o <<= 1) { unsigned k2 = __shfl_xor(key, o); key = key > k2 ? key : k2; }
        key = __builtin_amdgcn_readfirstlane(key);
        if (l == 0) *(LAS unsigned*)(L + L_KEY) = key;
        lds_signal(L, L_C6, l);
        const int idx = 2047 - (int)(key & 0x7ffu);
        dma16(l * 16, (const char*)x + (size_t)idx * 1024, LB + L_XSEL);
    }
    lds_wait_ge(L, L_C4, 4u);
    SEG(1);
    ST(13);
    if (w == 0 && j < 28) {
        WAIT_VM0();
        lds_wait_ge(L, L_C7, 8u);
        f32x4 a = {0.f, 0.f, 0.f, 0.f};
        u32x4 fa[8], fb[8];
        #pragma unroll
        for (int hh = 0; hh < 2; ++hh) {
            #pragma unroll
            for (int s = 0; s < 8; ++s) {
                fa[s] = tr_frag(L + L_FW1 + (8 * hh + s) * 1024 + 8 * l);
                fb[s] = *(LAS u32x4*)(L + L_XSEL + (8 * hh + s) * 64 + g4 * 16);
            }
            __builtin_amdgcn_sched_barrier(0);
            #pragma unroll
            for (int s = 0; s < 8; ++s) a = mfma16(fa[s], fb[s], a);
            __builtin_amdgcn_sched_barrier(0);
        }
        float hv[4];
        #pragma unroll
        for (int i = 0; i < 4; ++i) {
            float u = bfr(__shfl_xor(a[i], 32));
            float gq = bfr(a[i]);
            hv[i] = bfr(gq * sigmoidf_(gq)) * u;
        }
        u32x4 gr = {pk2(hv[0], hv[1]), tag, pk2(hv[2], hv[3]), tag};
        if (l == 0 || l == 16) __builtin_amdgcn_raw_buffer_store_b128(gr, rs, g4 * 16, WS_HG + ft * 32, 16);
    }
    SEG(1);
    ST(14);
    lds_wait_ge(L, L_C6, 1u);
    const unsigned rkey = __builtin_amdgcn_readfirstlane(*(LAS unsigned*)(L + L_KEY));
    if (w == 1) fl0 = __builtin_amdgcn_raw_buffer_load_b128(rs, (l < 56 ? l : 55) * 16, WS_HG + xcd * 896, 16);
    u32x4 wof[16];
    u32x2 xres[2];
    float gres[2];
    #pragma unroll
    for (int s = 0; s < 16; ++s)
        wof[s] = __builtin_amdgcn_raw_buffer_load_b128(rs, l * 16, WS_WOF + ((cg * 8 + w) * 16 + s) * 1024, 16);
    #pragma unroll
    for (int t = 0; t < 2; ++t) {
        const int tok = tb * 64 + tho * 32 + 16 * t + l15;
        xres[t] = *(const GAS u32x2*)(x + (size_t)tok * 512 + n0 + 4 * g4);
        gres[t] = u2f(__hip_atomic_load((GAS unsigned*)(ws + WS_GATE) + tok, RLX_AGENT));
    }
    SEG(1);
    ST(15);
    if (w == 1) {
        _Pragma("nounroll") for (int i = 0; i < SPIN_CAP; ++i) {
            if (__all(fl0[1] == tag && fl0[3] == tag)) break;
            FENCE();
            fl0 = __builtin_amdgcn_raw_buffer_load_b128(rs, (l < 56 ? l : 55) * 16, WS_HG + xcd * 896, 16);
        }
        u32x2 hv = {fl0[0], fl0[2]};
        if (l < 56) *(LAS u32x2*)(L + L_HBUF + l * 8) = hv;
        WAIT_LGKM0();
        f32x4 a = {0.f, 0.f, 0.f, 0.f};
        u32x4 fa[7], fb[7];
        #pragma unroll
        for (int s = 0; s < 7; ++s) {
            fa[s] = tr_frag(L + L_FW2 + s * 1024 + 8 * l);
            fb[s] = *(LAS u32x4*)(L + L_HBUF + s * 64 + g4 * 16);
        }
        __builtin_amdgcn_sched_barrier(0);
        #pragma unroll
        for (int s = 0; s < 7; ++s) a = mfma16(fa[s], fb[s], a);
        u32x4 g0 = {f2u(a[0]), tag, f2u(a[1]), tag};
        u32x4 g1 = {f2u(a[2]), tag, f2u(a[3]), tag};
        if (l15 == 0) {
            __builtin_amdgcn_raw_buffer_store_b128(g0, rs, g4 * 32, WS_YG + (xcd * 512 + 16 * j) * 8, 16);
            __builtin_amdgcn_raw_buffer_store_b128(g1, rs, g4 * 32 + 16, WS_YG + (xcd * 512 + 16 * j) * 8, 16);
        }
    }
    SEG(PRIO_N);
    ST(16);
    __builtin_amdgcn_s_setprio(0);
    SEG(4 - PRIO_N);
    asm volatile("s_nop 1\n\tv_smfmac_f32_32x32x32_bf16 v[64:79], v[88:91], v[56:63], v96\n\ts_nop 15\n\ts_nop 7" : "+{v[64:79]}"(acc) : "{v[48:63]}"(Y), "{v[88:91]}"(V1), "{v96}"(idxv) : "memory");
    __builtin_amdgcn_s_setprio(0);
    ST(8); STC(31);

    {
        const float ls = acc[8];
        bool ok = ls >= 8.6736174e-19f && ls <= 1.2676506e30f;
        #pragma unroll
        for (int i = 0; i < 8; ++i) ok = ok && (__builtin_fabsf(acc[i]) < __builtin_inff());
        if (!__all(ok)) {
            LAS char* kq = L + l * 16;
            LAS char* vq = L + 1024 + ((l & 31) < 16 ? (l >> 5) * 256 + (l & 31) * 16 : 512);
            acc = attn_true_max(kq, vq, qf);
        }
    }
    {
        const float inv = 1.f / acc[8];
        u32x4 o;
        #pragma unroll
        for (int i = 0; i < 4; ++i) { unsigned u = pk2(acc[2 * i] * inv, acc[2 * i + 1] * inv); o[i] = u < 0xFFFFFFFEu ? u : 0xFFFFFFFEu; }
        __builtin_amdgcn_raw_buffer_store_b128(o, rs, l * 16, WS_OF + ((tb * 2 + tha) * 32 + 4 * hg + hl) * 1024, 16);
    }
    const int idx = 2047 - (int)(rkey & 0x7ffu);
    const bool has_idx = (idx >> 5) == tb * 2 + tho;
    u32x4 yg = {0u, tag, 0u, tag};
    const unsigned yoff = ((l >> 3) * 512 + n0 + 2 * (l & 7)) * 8;
    if (has_idx) yg = __builtin_amdgcn_raw_buffer_load_b128(rs, yoff, WS_YG, 16);
    BARRIER();
    ST(9);
    {
        u32x4 f[4];
        const unsigned src = WS_OF + ((tb * 2 + tho) * 32 + 4 * w) * 1024;
        _Pragma("nounroll") for (int i = 0; i < SPIN_CAP; ++i) {
            bool ok = true;
            #pragma unroll
            for (int k = 0; k < 4; ++k) {
                f[k] = __builtin_amdgcn_raw_buffer_load_b128(rs, l * 16, src + k * 1024, 16);
                ok = ok && f[k][0] != 0xFFFFFFFFu && f[k][1] != 0xFFFFFFFFu && f[k][2] != 0xFFFFFFFFu && f[k][3] != 0xFFFFFFFFu;
            }
            if (__all(ok)) break;
            FENCE();
        }
        #pragma unroll
        for (int k = 0; k < 4; ++k) *(LAS u32x4*)(L + (4 * w + k) * 1024 + l * 16) = f[k];
    }
    ST(10);
    BARRIER();
    f32x4 oa[2];
    {
        const unsigned op = LB + (g4 >> 1) * 1024 + (g4 & 1) * 512 + l15 * 16;
        asm volatile("ds_read_b128 v[40:43], %[op] offset:0\n\tds_read_b128 v[44:47], %[op] offset:256\n\tds_read_b128 v[48:51], %[op] offset:2048\n\tds_read_b128 v[52:55], %[op] offset:2304\n\tds_read_b128 v[56:59], %[op] offset:4096\n\tds_read_b128 v[60:63], %[op] offset:4352\n\tds_read_b128 v[64:67], %[op] offset:6144\n\tds_read_b128 v[68:71], %[op] offset:6400\n\tds_read_b128 v[72:75], %[op] offset:8192\n\tds_read_b128 v[76:79], %[op] offset:8448\n\tds_read_b128 v[80:83], %[op] offset:10240\n\tds_read_b128 v[84:87], %[op] offset:10496\n\ts_waitcnt lgkmcnt(11)\n\tv_mfma_f32_16x16x32_bf16 v[32:35], %[w0], v[40:43], 0\n\ts_waitcnt lgkmcnt(10)\n\tv_mfma_f32_16x16x32_bf16 v[36:39], %[w0], v[44:47], 0\n\tds_read_b128 v[40:43], %[op] offset:12288\n\tds_read_b128 v[44:47], %[op] offset:12544\n\ts_waitcnt lgkmcnt(11)\n\tv_mfma_f32_16x16x32_bf16 v[32:35], %[w1], v[48:51], v[32:35]\n\ts_waitcnt lgkmcnt(10)\n\tv_mfma_f32_16x16x32_bf16 v[36:39], %[w1], v[52:55], v[36:39]\n\tds_read_b128 v[48:51], %[op] offset:14336\n\tds_read_b128 v[52:55], %[op] offset:14592\n\ts_waitcnt lgkmcnt(11)\n\tv_mfma_f32_16x16x32_bf16 v[32:35], %[w2], v[56:59], v[32:35]\n\ts_waitcnt lgkmcnt(10)\n\tv_mfma_f32_16x16x32_bf16 v[36:39], %[w2], v[60:63], v[36:39]\n\tds_read_b128 v[56:59], %[op] offset:16384\n\tds_read_b128 v[60:63], %[op] offset:16640\n\ts_waitcnt lgkmcnt(11)\n\tv_mfma_f32_16x16x32_bf16 v[32:35], %[w3], v[64:67], v[32:35]\n\ts_waitcnt lgkmcnt(10)\n\tv_mfma_f32_16x16x32_bf16 v[36:39], %[w3], v[68:71], v[36:39]\n\tds_read_b128 v[64:67], %[op] offset:18432\n\tds_read_b128 v[68:71], %[op] offset:18688\n\ts_waitcnt lgkmcnt(11)\n\tv_mfma_f32_16x16x32_bf16 v[32:35], %[w4], v[72:75], v[32:35]\n\ts_waitcnt lgkmcnt(10)\n\tv_mfma_f32_16x16x32_bf16 v[36:39], %[w4], v[76:79], v[36:39]\n\tds_read_b128 v[72:75], %[op] offset:20480\n\tds_read_b128 v[76:79], %[op] offset:20736\n\ts_waitcnt lgkmcnt(11)\n\tv_mfma_f32_16x16x32_bf16 v[32:35], %[w5], v[80:83], v[32:35]\n\ts_waitcnt lgkmcnt(10)\n\tv_mfma_f32_16x16x32_bf16 v[36:39], %[w5], v[84:87], v[36:39]\n\tds_read_b128 v[80:83], %[op] offset:22528\n\tds_read_b128 v[84:87], %[op] offset:22784\n\ts_waitcnt lgkmcnt(11)\n\tv_mfma_f32_16x16x32_bf16 v[32:35], %[w6], v[40:43], v[32:35]\n\ts_waitcnt lgkmcnt(10)\n\tv_mfma_f32_16x16x32_bf16 v[36:39], %[w6], v[44:47], v[36:39]\n\tds_read_b128 v[40:43], %[op] offset:24576\n\tds_read_b128 v[44:47], %[op] offset:24832\n\ts_waitcnt lgkmcnt(11)\n\tv_mfma_f32_16x16x32_bf16 v[32:35], %[w7], v[48:51], v[32:35]\n\ts_waitcnt lgkmcnt(10)\n\tv_mfma_f32_16x16x32_bf16 v[36:39], %[w7], v[52:55], v[36:39]\n\tds_read_b128 v[48:51], %[op] offset:26624\n\tds_read_b128 v[52:55], %[op] offset:26880\n\ts_waitcnt lgkmcnt(11)\n\tv_mfma_f32_16x16x32_bf16 v[32:35], %[w8], v[56:59], v[32:35]\n\ts_waitcnt lgkmcnt(10)\n\tv_mfma_f32_16x16x32_bf16 v[36:39], %[w8], v[60:63], v[36:39]\n\tds_read_b128 v[56:59], %[op] offset:28672\n\tds_read_b128 v[60:63], %[op] offset:28928\n\ts_waitcnt lgkmcnt(11)\n\tv_mfma_f32_16x16x32_bf16 v[32:35], %[w9], v[64:67], v[32:35]\n\ts_waitcnt lgkmcnt(10)\n\tv_mfma_f32_16x16x32_bf16 v[36:39], %[w9], v[68:71], v[36:39]\n\tds_read_b128 v[64:67], %[op] offset:30720\n\tds_read_b128 v[68:71], %[op] offset:30976\n\ts_waitcnt lgkmcnt(11)\n\tv_mfma_f32_16x16x32_bf16 v[32:35], %[w10], v[72:75], v[32:35]\n\ts_waitcnt lgkmcnt(10)\n\tv_mfma_f32_16x16x32_bf16 v[36:39], %[w10], v[76:79], v[36:39]\n\ts_waitcnt lgkmcnt(9)\n\tv_mfma_f32_16x16x32_bf16 v[32:35], %[w11], v[80:83], v[32:35]\n\ts_waitcnt lgkmcnt(8)\n\tv_mfma_f32_16x16x32_bf16 v[36:39], %[w11], v[84:87], v[36:39]\n\ts_waitcnt lgkmcnt(7)\n\tv_mfma_f32_16x16x32_bf16 v[32:35], %[w12], v[40:43], v[32:35]\n\ts_waitcnt lgkmcnt(6)\n\tv_mfma_f32_16x16x32_bf16 v[36:39], %[w12], v[44:47], v[36:39]\n\ts_waitcnt lgkmcnt(5)\n\tv_mfma_f32_16x16x32_bf16 v[32:35], %[w13], v[48:51], v[32:35]\n\ts_waitcnt lgkmcnt(4)\n\tv_mfma_f32_16x16x32_bf16 v[36:39], %[w13], v[52:55], v[36:39]\n\ts_waitcnt lgkmcnt(3)\n\tv_mfma_f32_16x16x32_bf16 v[32:35], %[w14], v[56:59], v[32:35]\n\ts_waitcnt lgkmcnt(2)\n\tv_mfma_f32_16x16x32_bf16 v[36:39], %[w14], v[60:63], v[36:39]\n\ts_waitcnt lgkmcnt(1)\n\tv_mfma_f32_16x16x32_bf16 v[32:35], %[w15], v[64:67], v[32:35]\n\ts_waitcnt lgkmcnt(0)\n\tv_mfma_f32_16x16x32_bf16 v[36:39], %[w15], v[68:71], v[36:39]\n\ts_nop 7\n\ts_nop 1" : "=&{v[32:35]}"(oa[0]), "=&{v[36:39]}"(oa[1]) : [op] "v"(op), [w0] "v"(wof[0]), [w1] "v"(wof[1]), [w2] "v"(wof[2]), [w3] "v"(wof[3]), [w4] "v"(wof[4]), [w5] "v"(wof[5]), [w6] "v"(wof[6]), [w7] "v"(wof[7]), [w8] "v"(wof[8]), [w9] "v"(wof[9]), [w10] "v"(wof[10]), [w11] "v"(wof[11]), [w12] "v"(wof[12]), [w13] "v"(wof[13]), [w14] "v"(wof[14]), [w15] "v"(wof[15]) : "v40", "v41", "v42", "v43", "v44", "v45", "v46", "v47", "v48", "v49", "v50", "v51", "v52", "v53", "v54", "v55", "v56", "v57", "v58", "v59", "v60", "v61", "v62", "v63", "v64", "v65", "v66", "v67", "v68", "v69", "v70", "v71", "v72", "v73", "v74", "v75", "v76", "v77", "v78", "v79", "v80", "v81", "v82", "v83", "v84", "v85", "v86", "v87", "memory");
    }
    float yv[4] = {0.f, 0.f, 0.f, 0.f};
    if (has_idx) {
        _Pragma("nounroll") for (int i = 0; i < SPIN_CAP; ++i) {
            if (__all(yg[1] == tag && yg[3] == tag)) break;
            FENCE();
            yg = __builtin_amdgcn_raw_buffer_load_b128(rs, yoff, WS_YG, 16);
        }
        float y0 = u2f(yg[0]), y1 = u2f(yg[2]);
        #pragma unroll
        for (int o = 8; o < 64; o <<= 1) { y0 += __shfl_xor(y0, o); y1 += __shfl_xor(y1, o); }
        unsigned ord = rkey >> 16;
        unsigned rb = (ord & 0x8000u) ? (ord & 0x7fffu) : (~ord & 0xffffu);
        const float rsel = bfr(sigmoidf_(bflo(rb)));
        yv[0] = bfr(bfr(__shfl(y0, 2 * g4)) * rsel);     yv[1] = bfr(bfr(__shfl(y1, 2 * g4)) * rsel);
        yv[2] = bfr(bfr(__shfl(y0, 2 * g4 + 1)) * rsel); yv[3] = bfr(bfr(__shfl(y1, 2 * g4 + 1)) * rsel);
    }
    #pragma unroll
    for (int t = 0; t < 2; ++t) {
        const int tok = tb * 64 + tho * 32 + 16 * t + l15;
        const float g = gres[t], omg = bfr(1.f - g);
        float bx[4] = {bflo(xres[t][0]), bfhi(xres[t][0]), bflo(xres[t][1]), bfhi(xres[t][1])};
        float r4[4];
        #pragma unroll
        for (int i = 0; i < 4; ++i) {
            float bb = (has_idx && tok == idx) ? bfr(bx[i] + yv[i]) : bx[i];
            r4[i] = bfr(g * bfr(oa[t][i])) + bfr(omg * bb);
        }
        u32x2 o2 = {pk2(r4[0], r4[1]), pk2(r4[2], r4[3])};
        *(GAS u32x2*)(out + (size_t)tok * 512 + n0 + 4 * g4) = o2;
    }
    ST(11);
}

extern "C" void kernel_launch(void* const* d_in, const int* in_sizes, int n_in, void* d_out, int out_size,
                              void* d_ws, size_t ws_size, hipStream_t stream) {
    (void)in_sizes; (void)n_in; (void)out_size; (void)ws_size;
    (void)hipFuncSetAttribute((const void*)mqa_moe_fused, hipFuncAttributeMaxDynamicSharedMemorySize, LDS_BYTES);
    mqa_moe_fused<<<256, 512, LDS_BYTES, stream>>>(
        (const u16*)d_in[0], (const u16*)d_in[1], (const u16*)d_in[2], (const u16*)d_in[3], (const u16*)d_in[4],
        (const u16*)d_in[6], (const u16*)d_in[7], (const u16*)d_in[8], (const u16*)d_in[9], (const u16*)d_in[10],
        (u16*)d_out, (char*)d_ws);
}
```

```cpp
#include <hip/hip_runtime.h>
#include <stdint.h>
#define PRIO_N 3

typedef unsigned short u16;
typedef __attribute__((ext_vector_type(8))) __bf16 bf16x8;
typedef __attribute__((ext_vector_type(16))) __bf16 bf16x16;
typedef __attribute__((ext_vector_type(2))) __bf16 bf16x2;
typedef __attribute__((ext_vector_type(4))) short s16x4;
typedef __attribute__((ext_vector_type(2))) float f32x2;
typedef __attribute__((ext_vector_type(4))) float f32x4;
typedef __attribute__((ext_vector_type(8))) float f32x8;
typedef __attribute__((ext_vector_type(16))) float f32x16;
typedef __attribute__((ext_vector_type(4))) unsigned u32x4;
typedef __attribute__((ext_vector_type(2))) unsigned u32x2;
#define LAS __attribute__((address_space(3)))
#define GAS __attribute__((address_space(1)))
#define RLX_AGENT __ATOMIC_RELAXED, __HIP_MEMORY_SCOPE_AGENT
#define RLX_WG __ATOMIC_RELAXED, __HIP_MEMORY_SCOPE_WORKGROUP

constexpr unsigned WS_EPOCH = 0;
constexpr unsigned WS_F1   = 4096;
constexpr unsigned WS_F2   = 16384;
constexpr unsigned WS_KF   = 32768;
constexpr unsigned WS_VF   = 98304;
constexpr unsigned WS_WOF  = 163840;
constexpr unsigned WS_OF   = 688128;
constexpr unsigned WS_GATE = 2785280;
constexpr unsigned WS_HG   = 2793472;
constexpr unsigned WS_YG   = 2801664;
constexpr unsigned WS_END  = 2834432;
constexpr unsigned WS_DBG  = 4194304;

constexpr unsigned REC     = 2080;
constexpr unsigned L_WKV   = 0;
constexpr unsigned L_WOST  = 16384;
constexpr unsigned L_XT    = 24960;
constexpr unsigned L_WQ    = 91520;
constexpr unsigned L_FW1   = 133120;
constexpr unsigned L_FW2   = 149504;
constexpr unsigned L_QX    = 157056;
constexpr unsigned L_MISC  = 161152;
constexpr unsigned L_C2 = L_MISC + 0, L_C4 = L_MISC + 4, L_C5 = L_MISC + 8, L_C6 = L_MISC + 12, L_C7 = L_MISC + 16,
                   L_TAG = L_MISC + 24  , L_KEY = L_MISC + 32;
constexpr unsigned L_XSEL  = L_MISC + 128;
constexpr unsigned L_HBUF  = L_XSEL + 1024;
constexpr unsigned LDS_BYTES = 163840;
constexpr int SPIN_CAP = 1 << 18;

__device__ __forceinline__ unsigned pk2(float a, float b) {
    f32x2 v = {a, b};
    return __builtin_bit_cast(unsigned, __builtin_convertvector(v, bf16x2));
}
__device__ __forceinline__ float bfr(float a) {
    return __builtin_bit_cast(float, pk2(a, a) & 0xffff0000u);
}
__device__ __forceinline__ unsigned f2u(float f) { return __builtin_bit_cast(unsigned, f); }
__device__ __forceinline__ float u2f(unsigned u) { return __builtin_bit_cast(float, u); }
__device__ __forceinline__ float bflo(unsigned u) { return __builtin_bit_cast(float, u << 16); }
__device__ __forceinline__ float bfhi(unsigned u) { return __builtin_bit_cast(float, u & 0xffff0000u); }
__device__ __forceinline__ float sigmoidf_(float z) { return __builtin_amdgcn_rcpf(1.f + __builtin_amdgcn_exp2f(-1.4426950408889634f * z)); }

__device__ __forceinline__ void dma16(unsigned voff, const void* sbase, unsigned lds) {
    unsigned keep;
    asm volatile("s_mov_b32 %0, m0\n\ts_mov_b32 m0, %3\n\ts_nop 0\n\tglobal_load_lds_dwordx4 %1, %2\n\ts_mov_b32 m0, %0"
                 : "=&s"(keep) : "v"(voff), "s"(sbase), "s"(lds) : "memory");
}
__device__ __forceinline__ void dma16p(const void* gsrc, unsigned lds) {
    unsigned keep;
    asm volatile("s_mov_b32 %0, m0\n\ts_mov_b32 m0, %2\n\ts_nop 0\n\tglobal_load_lds_dwordx4 %1, off\n\ts_mov_b32 m0, %0"
                 : "=&s"(keep) : "v"(gsrc), "s"(lds) : "memory");
}
#define WAIT_VM0()   asm volatile("s_waitcnt vmcnt(0)" ::: "memory")
#define WAIT_LGKM0() asm volatile("s_waitcnt lgkmcnt(0)" ::: "memory")
#define BARRIER()    asm volatile("s_waitcnt lgkmcnt(0)\n\ts_barrier" ::: "memory")
#define FENCE()      asm volatile("" ::: "memory")

__device__ __forceinline__ u32x4 tr_frag(LAS char* p) {
    s16x4 a0 = __builtin_amdgcn_ds_read_tr16_b64_v4i16((LAS s16x4*)p);
    s16x4 a1 = __builtin_amdgcn_ds_read_tr16_b64_v4i16((LAS s16x4*)(p + 512));
    u32x2 p0 = __builtin_bit_cast(u32x2, a0), p1 = __builtin_bit_cast(u32x2, a1);
    u32x4 a = {p0[0], p0[1], p1[0], p1[1]};
    return a;
}
__device__ __forceinline__ f32x4 mfma16(u32x4 a, u32x4 b, f32x4 c) {
    return __builtin_amdgcn_mfma_f32_16x16x32_bf16(__builtin_bit_cast(bf16x8, a), __builtin_bit_cast(bf16x8, b), c, 0, 0, 0);
}
__device__ __forceinline__ f32x16 mfma32(u32x4 a, u32x4 b, f32x16 c) {
    return __builtin_amdgcn_mfma_f32_32x32x16_bf16(__builtin_bit_cast(bf16x8, a), __builtin_bit_cast(bf16x8, b), c, 0, 0, 0);
}
__device__ __forceinline__ f32x16 smf(u32x4 v, f32x8 p, f32x16 acc) {
    return __builtin_amdgcn_smfmac_f32_32x32x32_bf16(__builtin_bit_cast(bf16x8, v), __builtin_bit_cast(bf16x16, p), acc, (int)0xDDDDDDDD, 0, 0);
}
__device__ __forceinline__ void lds_wait_ge(LAS char* L, unsigned off, unsigned want) {
    _Pragma("nounroll") for (int i = 0; i < SPIN_CAP; ++i) {
        if (__hip_atomic_load((LAS unsigned*)(L + off), RLX_WG) >= want) break;
        __builtin_amdgcn_s_sleep(1);
    }
    FENCE();
}
__device__ __forceinline__ void lds_signal(LAS char* L, unsigned off, int l) {
    WAIT_LGKM0();
    if (l == 0) __hip_atomic_fetch_add((LAS unsigned*)(L + off), 1u, RLX_WG);
    FENCE();
}
__device__ __forceinline__ unsigned get_tag(LAS char* L) {
    lds_wait_ge(L, L_TAG, 1u);
    return __builtin_amdgcn_readfirstlane(*(LAS unsigned*)(L + L_TAG + 4));
}
#define ST(i) do {} while (0)
#define STC(i) do {} while (0)
#define AP(i) do {} while (0)

__device__ __forceinline__ f32x16 attn_true_max(LAS char* kp, LAS char* vp, u32x4 q) {
    f32x16 z; for (int i = 0; i < 16; ++i) z[i] = 0.f;
    float m = -__builtin_inff();
    #pragma unroll 1
    for (int t = 0; t < 64; ++t) {
        f32x16 s = mfma32(*(LAS u32x4*)(kp + t * REC), q, z);
        #pragma unroll
        for (int i = 0; i < 16; ++i) m = fmaxf(m, s[i]);
    }
    m = fmaxf(m, __shfl_xor(m, 32));
    f32x16 acc = z;
    #pragma unroll 1
    for (int t = 0; t < 64; ++t) {
        f32x16 s = mfma32(*(LAS u32x4*)(kp + t * REC), q, z);
        f32x8 p0, p1;
        #pragma unroll
        for (int i = 0; i < 8; ++i) { p0[i] = __builtin_amdgcn_exp2f(s[i] - m); p1[i] = __builtin_amdgcn_exp2f(s[8 + i] - m); }
        acc = smf(*(LAS u32x4*)(vp + t * REC), p0, acc);
        acc = smf(*(LAS u32x4*)(vp + t * REC + 528), p1, acc);
    }
    return acc;
}

__global__ void __launch_bounds__(512, 2) mqa_moe_fused(
        const u16* __restrict__ x, const u16* __restrict__ wq, const u16* __restrict__ wk, const u16* __restrict__ wv,
        const u16* __restrict__ wo, const u16* __restrict__ w_r, const u16* __restrict__ w_gate, const u16* __restrict__ w_up,
        const u16* __restrict__ w_down, const u16* __restrict__ gate_w, u16* __restrict__ out, char* ws) {
    extern __shared__ __attribute__((aligned(16))) char smem[];
    LAS char* L = (LAS char*)smem;
    const unsigned LB = (unsigned)(size_t)L;
    const int l = threadIdx.x & 63;
    const int w = __builtin_amdgcn_readfirstlane(threadIdx.x >> 6);
    const int b = blockIdx.x, xcd = b & 7, j = b >> 3;
    const int tb = xcd * 4 + (j & 3), hg = j >> 2;
    const int g4 = l >> 4, l15 = l & 15;
    const __amdgpu_buffer_rsrc_t rs = __builtin_amdgcn_make_buffer_rsrc((void*)ws, 0, WS_END, 0x00020000);
    const int sg = (l >> 3) & 3, sjh = l >> 5, sq = (l >> 1) & 3;
    const int krow = 8 * sg + 4 * sjh + sq;
    unsigned tag = 0;
    u32x4 qf;
    ST(0); STC(29); AP(0);

    if (w == 4) {
        const int type = hg & 1, tg = hg >> 1;
        const u16* W = type ? wv : wk;
        const u16* xr = x + (size_t)(tb * 64 + tg * 16 + l15) * 512 + 8 * g4;
        u32x4 xf[16];
        const unsigned wvo = krow * 32 + (l & 1) * 16;
        #pragma unroll
        for (int s = 0; s < 16; ++s) {
            dma16(wvo, (const char*)W + s * 1024, LB + L_WKV + s * 1024);
            xf[s] = *(const GAS u32x4*)(xr + 32 * s);
        }
        unsigned E = __hip_atomic_load((GAS unsigned*)(ws + WS_EPOCH), RLX_AGENT);
        asm volatile("s_barrier" ::: "memory");
        WAIT_VM0();
        ST(1);
        tag = __builtin_amdgcn_readfirstlane(E) + 1u;
        if (l == 0) { u32x2 tv = {1u, tag}; *(LAS u32x2*)(L + L_TAG) = tv; }
        f32x4 acc;
        const unsigned wp = LB + L_WKV + 8 * l;
        if (type == 0) asm volatile("ds_read_b64_tr_b16 v[36:37], %[wp] offset:0\n\tds_read_b64_tr_b16 v[38:39], %[wp] offset:512\n\tds_read_b64_tr_b16 v[40:41], %[wp] offset:1024\n\tds_read_b64_tr_b16 v[42:43], %[wp] offset:1536\n\tds_read_b64_tr_b16 v[44:45], %[wp] offset:2048\n\tds_read_b64_tr_b16 v[46:47], %[wp] offset:2560\n\tds_read_b64_tr_b16 v[48:49], %[wp] offset:3072\n\tds_read_b64_tr_b16 v[50:51], %[wp] offset:3584\n\tds_read_b64_tr_b16 v[52:53], %[wp] offset:4096\n\tds_read_b64_tr_b16 v[54:55], %[wp] offset:4608\n\tds_read_b64_tr_b16 v[56:57], %[wp] offset:5120\n\tds_read_b64_tr_b16 v[58:59], %[wp] offset:5632\n\ts_waitcnt lgkmcnt(10)\n\tv_mfma_f32_16x16x32_bf16 v[32:35], v[36:39], %[x0], 0\n\tds_read_b64_tr_b16 v[36:37], %[wp] offset:6144\n\tds_read_b64_tr_b16 v[38:39], %[wp] offset:6656\n\ts_waitcnt lgkmcnt(10)\n\tv_mfma_f32_16x16x32_bf16 v[32:35], v[40:43], %[x1], v[32:35]\n\tds_read_b64_tr_b16 v[40:41], %[wp] offset:7168\n\tds_read_b64_tr_b16 v[42:43], %[wp] offset:7680\n\ts_waitcnt lgkmcnt(10)\n\tv_mfma_f32_16x16x32_bf16 v[32:35], v[44:47], %[x2], v[32:35]\n\tds_read_b64_tr_b16 v[44:45], %[wp] offset:8192\n\tds_read_b64_tr_b16 v[46:47], %[wp] offset:8704\n\ts_waitcnt lgkmcnt(10)\n\tv_mfma_f32_16x16x32_bf16 v[32:35], v[48:51], %[x3], v[32:35]\n\tds_read_b64_tr_b16 v[48:49], %[wp] offset:9216\n\tds_read_b64_tr_b16 v[50:51], %[wp] offset:9728\n\ts_waitcnt lgkmcnt(10)\n\tv_mfma_f32_16x16x32_bf16 v[32:35], v[52:55], %[x4], v[32:35]\n\tds_read_b64_tr_b16 v[52:53], %[wp] offset:10240\n\tds_read_b64_tr_b16 v[54:55], %[wp] offset:10752\n\ts_waitcnt lgkmcnt(10)\n\tv_mfma_f32_16x16x32_bf16 v[32:35], v[56:59], %[x5], v[32:35]\n\tds_read_b64_tr_b16 v[56:57], %[wp] offset:11264\n\tds_read_b64_tr_b16 v[58:59], %[wp] offset:11776\n\ts_waitcnt lgkmcnt(10)\n\tv_mfma_f32_16x16x32_bf16 v[32:35], v[36:39], %[x6], v[32:35]\n\tds_read_b64_tr_b16 v[36:37], %[wp] offset:12288\n\tds_read_b64_tr_b16 v[38:39], %[wp] offset:12800\n\ts_waitcnt lgkmcnt(10)\n\tv_mfma_f32_16x16x32_bf16 v[32:35], v[40:43], %[x7], v[32:35]\n\tds_read_b64_tr_b16 v[40:41], %[wp] offset:13312\n\tds_read_b64_tr_b16 v[42:43], %[wp] offset:13824\n\ts_waitcnt lgkmcnt(10)\n\tv_mfma_f32_16x16x32_bf16 v[32:35], v[44:47], %[x8], v[32:35]\n\tds_read_b64_tr_b16 v[44:45], %[wp] offset:14336\n\tds_read_b64_tr_b16 v[46:47], %[wp] offset:14848\n\ts_waitcnt lgkmcnt(10)\n\tv_mfma_f32_16x16x32_bf16 v[32:35], v[48:51], %[x9], v[32:35]\n\tds_read_b64_tr_b16 v[48:49], %[wp] offset:15360\n\tds_read_b64_tr_b16 v[50:51], %[wp] offset:15872\n\ts_waitcnt lgkmcnt(10)\n\tv_mfma_f32_16x16x32_bf16 v[32:35], v[52:55], %[x10], v[32:35]\n\ts_waitcnt lgkmcnt(8)\n\tv_mfma_f32_16x16x32_bf16 v[32:35], v[56:59], %[x11], v[32:35]\n\ts_waitcnt lgkmcnt(6)\n\tv_mfma_f32_16x16x32_bf16 v[32:35], v[36:39], %[x12], v[32:35]\n\ts_waitcnt lgkmcnt(4)\n\tv_mfma_f32_16x16x32_bf16 v[32:35], v[40:43], %[x13], v[32:35]\n\ts_waitcnt lgkmcnt(2)\n\tv_mfma_f32_16x16x32_bf16 v[32:35], v[44:47], %[x14], v[32:35]\n\ts_waitcnt lgkmcnt(0)\n\tv_mfma_f32_16x16x32_bf16 v[32:35], v[48:51], %[x15], v[32:35]\n\ts_nop 7\n\ts_nop 1" : "=&{v[32:35]}"(acc) : [wp] "v"(wp), [x0] "v"(xf[0]), [x1] "v"(xf[1]), [x2] "v"(xf[2]), [x3] "v"(xf[3]), [x4] "v"(xf[4]), [x5] "v"(xf[5]), [x6] "v"(xf[6]), [x7] "v"(xf[7]), [x8] "v"(xf[8]), [x9] "v"(xf[9]), [x10] "v"(xf[10]), [x11] "v"(xf[11]), [x12] "v"(xf[12]), [x13] "v"(xf[13]), [x14] "v"(xf[14]), [x15] "v"(xf[15]) : "v36", "v37", "v38", "v39", "v40", "v41", "v42", "v43", "v44", "v45", "v46", "v47", "v48", "v49", "v50", "v51", "v52", "v53", "v54", "v55", "v56", "v57", "v58", "v59", "memory");
        else           asm volatile("ds_read_b64_tr_b16 v[36:37], %[wp] offset:0\n\tds_read_b64_tr_b16 v[38:39], %[wp] offset:512\n\tds_read_b64_tr_b16 v[40:41], %[wp] offset:1024\n\tds_read_b64_tr_b16 v[42:43], %[wp] offset:1536\n\tds_read_b64_tr_b16 v[44:45], %[wp] offset:2048\n\tds_read_b64_tr_b16 v[46:47], %[wp] offset:2560\n\tds_read_b64_tr_b16 v[48:49], %[wp] offset:3072\n\tds_read_b64_tr_b16 v[50:51], %[wp] offset:3584\n\tds_read_b64_tr_b16 v[52:53], %[wp] offset:4096\n\tds_read_b64_tr_b16 v[54:55], %[wp] offset:4608\n\tds_read_b64_tr_b16 v[56:57], %[wp] offset:5120\n\tds_read_b64_tr_b16 v[58:59], %[wp] offset:5632\n\ts_waitcnt lgkmcnt(10)\n\tv_mfma_f32_16x16x32_bf16 v[32:35], %[x0], v[36:39], 0\n\tds_read_b64_tr_b16 v[36:37], %[wp] offset:6144\n\tds_read_b64_tr_b16 v[38:39], %[wp] offset:6656\n\ts_waitcnt lgkmcnt(10)\n\tv_mfma_f32_16x16x32_bf16 v[32:35], %[x1], v[40:43], v[32:35]\n\tds_read_b64_tr_b16 v[40:41], %[wp] offset:7168\n\tds_read_b64_tr_b16 v[42:43], %[wp] offset:7680\n\ts_waitcnt lgkmcnt(10)\n\tv_mfma_f32_16x16x32_bf16 v[32:35], %[x2], v[44:47], v[32:35]\n\tds_read_b64_tr_b16 v[44:45], %[wp] offset:8192\n\tds_read_b64_tr_b16 v[46:47], %[wp] offset:8704\n\ts_waitcnt lgkmcnt(10)\n\tv_mfma_f32_16x16x32_bf16 v[32:35], %[x3], v[48:51], v[32:35]\n\tds_read_b64_tr_b16 v[48:49], %[wp] offset:9216\n\tds_read_b64_tr_b16 v[50:51], %[wp] offset:9728\n\ts_waitcnt lgkmcnt(10)\n\tv_mfma_f32_16x16x32_bf16 v[32:35], %[x4], v[52:55], v[32:35]\n\tds_read_b64_tr_b16 v[52:53], %[wp] offset:10240\n\tds_read_b64_tr_b16 v[54:55], %[wp] offset:10752\n\ts_waitcnt lgkmcnt(10)\n\tv_mfma_f32_16x16x32_bf16 v[32:35], %[x5], v[56:59], v[32:35]\n\tds_read_b64_tr_b16 v[56:57], %[wp] offset:11264\n\tds_read_b64_tr_b16 v[58:59], %[wp] offset:11776\n\ts_waitcnt lgkmcnt(10)\n\tv_mfma_f32_16x16x32_bf16 v[32:35], %[x6], v[36:39], v[32:35]\n\tds_read_b64_tr_b16 v[36:37], %[wp] offset:12288\n\tds_read_b64_tr_b16 v[38:39], %[wp] offset:12800\n\ts_waitcnt lgkmcnt(10)\n\tv_mfma_f32_16x16x32_bf16 v[32:35], %[x7], v[40:43], v[32:35]\n\tds_read_b64_tr_b16 v[40:41], %[wp] offset:13312\n\tds_read_b64_tr_b16 v[42:43], %[wp] offset:13824\n\ts_waitcnt lgkmcnt(10)\n\tv_mfma_f32_16x16x32_bf16 v[32:35], %[x8], v[44:47], v[32:35]\n\tds_read_b64_tr_b16 v[44:45], %[wp] offset:14336\n\tds_read_b64_tr_b16 v[46:47], %[wp] offset:14848\n\ts_waitcnt lgkmcnt(10)\n\tv_mfma_f32_16x16x32_bf16 v[32:35], %[x9], v[48:51], v[32:35]\n\tds_read_b64_tr_b16 v[48:49], %[wp] offset:15360\n\tds_read_b64_tr_b16 v[50:51], %[wp] offset:15872\n\ts_waitcnt lgkmcnt(10)\n\tv_mfma_f32_16x16x32_bf16 v[32:35], %[x10], v[52:55], v[32:35]\n\ts_waitcnt lgkmcnt(8)\n\tv_mfma_f32_16x16x32_bf16 v[32:35], %[x11], v[56:59], v[32:35]\n\ts_waitcnt lgkmcnt(6)\n\tv_mfma_f32_16x16x32_bf16 v[32:35], %[x12], v[36:39], v[32:35]\n\ts_waitcnt lgkmcnt(4)\n\tv_mfma_f32_16x16x32_bf16 v[32:35], %[x13], v[40:43], v[32:35]\n\ts_waitcnt lgkmcnt(2)\n\tv_mfma_f32_16x16x32_bf16 v[32:35], %[x14], v[44:47], v[32:35]\n\ts_waitcnt lgkmcnt(0)\n\tv_mfma_f32_16x16x32_bf16 v[32:35], %[x15], v[48:51], v[32:35]\n\ts_nop 7\n\ts_nop 1" : "=&{v[32:35]}"(acc) : [wp] "v"(wp), [x0] "v"(xf[0]), [x1] "v"(xf[1]), [x2] "v"(xf[2]), [x3] "v"(xf[3]), [x4] "v"(xf[4]), [x5] "v"(xf[5]), [x6] "v"(xf[6]), [x7] "v"(xf[7]), [x8] "v"(xf[8]), [x9] "v"(xf[9]), [x10] "v"(xf[10]), [x11] "v"(xf[11]), [x12] "v"(xf[12]), [x13] "v"(xf[13]), [x14] "v"(xf[14]), [x15] "v"(xf[15]) : "v36", "v37", "v38", "v39", "v40", "v41", "v42", "v43", "v44", "v45", "v46", "v47", "v48", "v49", "v50", "v51", "v52", "v53", "v54", "v55", "v56", "v57", "v58", "v59", "memory");
        unsigned long long pv = (unsigned long long)pk2(acc[0], acc[1]) | ((unsigned long long)pk2(acc[2], acc[3]) << 32);
        unsigned dst;
        if (type == 0) dst = WS_KF + (tb * 2 + (tg >> 1)) * 1024 + ((tg & 1) * 16 + l15 + 32 * (g4 & 1)) * 16 + (g4 >> 1) * 8;
        else           dst = WS_VF + (tb * 4 + tg) * 512 + (g4 >> 1) * 256 + l15 * 16 + (g4 & 1) * 8;
        __hip_atomic_store((GAS unsigned long long*)(ws + dst), pv, RLX_AGENT);
        lds_signal(L, L_C5, l);
        WAIT_VM0();
        ST(2);
        const int kt = tb * 2 + (tg >> 1);
        const int p = (kt & 3) * 64 + (kt >> 2) * 4 + (tg & 1) * 2 + type;
        if (l < 8) __hip_atomic_store((GAS unsigned*)(ws + WS_F1 + l * 1024 + p * 4), tag, RLX_AGENT);
    } else if (w == 5) {
        const int type = hg & 1, tg = hg >> 1;
        const int wrow = 16 * (sg >> 1) + 8 * sjh + 4 * (sg & 1) + sq;
        const char* wsrc = (const char*)(wo + (size_t)(64 * (b & 7)) * 512 + 16 * (b >> 3));
        const unsigned wovo = wrow * 1024 + (l & 1) * 16;
        dma16(wovo, wsrc, LB + L_WOST);
        dma16(wovo, wsrc + 32 * 1024, LB + L_WOST + 1024);
        u32x4 af[16];
        if (type == 0) {
            const u16* ap = ((l & 1) ? gate_w : w_r) + 8 * g4;
            #pragma unroll
            for (int s = 0; s < 16; ++s) af[s] = *(const GAS u32x4*)(ap + 32 * s);
        }
        asm volatile("s_barrier" ::: "memory");
        {
            u32x4 sent = {0xFFFFFFFFu, 0xFFFFFFFFu, 0xFFFFFFFFu, 0xFFFFFFFFu};
            #pragma unroll
            for (int i = 0; i < 8; ++i)
                __builtin_amdgcn_raw_buffer_store_b128(sent, rs, l * 16, WS_OF + ((tb * 2 + (i >> 2)) * 32 + 4 * hg + (i & 3)) * 1024, 16);
        }
        WAIT_VM0();
        #pragma unroll
        for (int i = 0; i < 2; ++i) {
            u32x4 f = tr_frag(L + L_WOST + i * 1024 + 8 * l);
            __builtin_amdgcn_raw_buffer_store_b128(f, rs, l * 16, WS_WOF + (2 * b + i) * 1024, 16);
        }
        lds_signal(L, L_C5, l);
        tag = get_tag(L);
        unsigned key = 0;
        if (type == 0) {
            lds_wait_ge(L, L_C2, 4u);
            f32x4 acc = {0.f, 0.f, 0.f, 0.f};
            LAS char* xp = L + L_XT + (tg * 16 + l15) * 1040 + g4 * 16;
            u32x4 bx[16];
            #pragma unroll
            for (int s = 0; s < 16; ++s) bx[s] = *(LAS u32x4*)(xp + s * 64);
            __builtin_amdgcn_sched_barrier(0);
            #pragma unroll
            for (int s = 0; s < 16; ++s) acc = mfma16(af[s], bx[s], acc);
            const int tok = tb * 64 + tg * 16 + l15;
            float gt = bfr(sigmoidf_(bfr(acc[1])));
            if (l < 16) __hip_atomic_store((GAS unsigned*)(ws + WS_GATE) + tok, __builtin_bit_cast(unsigned, gt), RLX_AGENT);
            unsigned u = pk2(acc[0], acc[0]) & 0xffffu;
            unsigned ord = (u & 0x8000u) ? (~u & 0xffffu) : (u | 0x8000u);
            if ((u & 0x7fffu) > 0x7f80u) ord = 0u;
            key = (l < 16) ? ((ord << 16) | (unsigned)(2047 - tok)) : 0u;
            #pragma unroll
            for (int o = 1; o < 16; o <<= 1) { unsigned k2 = __shfl_xor(key, o); key = key > k2 ? key : k2; }
            key = __builtin_amdgcn_readfirstlane(key);
        }
        WAIT_VM0();
        if (l < 8) __hip_atomic_store((GAS unsigned long long*)(ws + WS_F2 + l * 2048 + b * 8), ((unsigned long long)tag << 32) | key, RLX_AGENT);
        ST(1);
    } else {
        if (w == 0 && l < 16) *(LAS unsigned*)(L + L_MISC + 4 * l) = 0u;
        BARRIER();
        if (w < 4) {
            #pragma unroll 4
            for (int i = 0; i < 16; ++i) {
                const int row = 16 * w + i;
                dma16(l * 16, (const char*)x + (size_t)(tb * 64 + row) * 1024, LB + L_XT + row * 1040);
            }
            const unsigned qvo = (l >> 3) * 1024 + (((l & 7) ^ (((l >> 4) & 1) << 2)) * 16);
            #pragma unroll 4
            for (int i = 0; i < 16; ++i) {
                const int blk = 16 * w + i;
                dma16(qvo, (const char*)wq + (size_t)blk * 8192 + hg * 128, LB + L_WQ + blk * 1024);
            }
            WAIT_VM0();
            ST(1);
            lds_signal(L, L_C2, l);
            lds_wait_ge(L, L_C2, 4u);
            ST(2);
            const int cp = w & 1, th = w >> 1, h = l >> 5, nhi = g4 & 1, q = (l >> 2) & 3, p = l & 3;
            const unsigned ap = LB + L_WQ + h * 1024 + q * 128 + ((64 * cp + 32 * nhi + 8 * p) ^ (((q >> 1) & 1) << 6));
            const unsigned bp = LB + L_XT + (32 * th + (l & 31)) * 1040 + h * 16;
            f32x16 acc;
            asm volatile("ds_read_b64_tr_b16 v[100:101], %[ap] offset:0\n\tds_read_b64_tr_b16 v[102:103], %[ap] offset:512\n\tds_read_b128 v[104:107], %[bp] offset:0\n\tds_read_b64_tr_b16 v[108:109], %[ap] offset:2048\n\tds_read_b64_tr_b16 v[110:111], %[ap] offset:2560\n\tds_read_b128 v[112:115], %[bp] offset:32\n\tds_read_b64_tr_b16 v[116:117], %[ap] offset:4096\n\tds_read_b64_tr_b16 v[118:119], %[ap] offset:4608\n\tds_read_b128 v[120:123], %[bp] offset:64\n\tds_read_b64_tr_b16 v[124:125], %[ap] offset:6144\n\tds_read_b64_tr_b16 v[126:127], %[ap] offset:6656\n\tds_read_b128 v[128:131], %[bp] offset:96\n\ts_waitcnt lgkmcnt(9)\n\tv_mfma_f32_32x32x16_bf16 v[32:47], v[100:103], v[104:107], 0\n\tds_read_b64_tr_b16 v[100:101], %[ap] offset:8192\n\tds_read_b64_tr_b16 v[102:103], %[ap] offset:8704\n\tds_read_b128 v[104:107], %[bp] offset:128\n\ts_waitcnt lgkmcnt(9)\n\tv_mfma_f32_32x32x16_bf16 v[32:47], v[108:111], v[112:115], v[32:47]\n\tds_read_b64_tr_b16 v[108:109], %[ap] offset:10240\n\tds_read_b64_tr_b16 v[110:111], %[ap] offset:10752\n\tds_read_b128 v[112:115], %[bp] offset:160\n\ts_waitcnt lgkmcnt(9)\n\tv_mfma_f32_32x32x16_bf16 v[32:47], v[116:119], v[120:123], v[32:47]\n\tds_read_b64_tr_b16 v[116:117], %[ap] offset:12288\n\tds_read_b64_tr_b16 v[118:119], %[ap] offset:12800\n\tds_read_b128 v[120:123], %[bp] offset:192\n\ts_waitcnt lgkmcnt(9)\n\tv_mfma_f32_32x32x16_bf16 v[32:47], v[124:127], v[128:131], v[32:47]\n\tds_read_b64_tr_b16 v[124:125], %[ap] offset:14336\n\tds_read_b64_tr_b16 v[126:127], %[ap] offset:14848\n\tds_read_b128 v[128:131], %[bp] offset:224\n\ts_waitcnt lgkmcnt(9)\n\tv_mfma_f32_32x32x16_bf16 v[32:47], v[100:103], v[104:107], v[32:47]\n\tds_read_b64_tr_b16 v[100:101], %[ap] offset:16384\n\tds_read_b64_tr_b16 v[102:103], %[ap] offset:16896\n\tds_read_b128 v[104:107], %[bp] offset:256\n\ts_waitcnt lgkmcnt(9)\n\tv_mfma_f32_32x32x16_bf16 v[32:47], v[108:111], v[112:115], v[32:47]\n\tds_read_b64_tr_b16 v[108:109], %[ap] offset:18432\n\tds_read_b64_tr_b16 v[110:111], %[ap] offset:18944\n\tds_read_b128 v[112:115], %[bp] offset:288\n\ts_waitcnt lgkmcnt(9)\n\tv_mfma_f32_32x32x16_bf16 v[32:47], v[116:119], v[120:123], v[32:47]\n\tds_read_b64_tr_b16 v[116:117], %[ap] offset:20480\n\tds_read_b64_tr_b16 v[118:119], %[ap] offset:20992\n\tds_read_b128 v[120:123], %[bp] offset:320\n\ts_waitcnt lgkmcnt(9)\n\tv_mfma_f32_32x32x16_bf16 v[32:47], v[124:127], v[128:131], v[32:47]\n\tds_read_b64_tr_b16 v[124:125], %[ap] offset:22528\n\tds_read_b64_tr_b16 v[126:127], %[ap] offset:23040\n\tds_read_b128 v[128:131], %[bp] offset:352\n\ts_waitcnt lgkmcnt(9)\n\tv_mfma_f32_32x32x16_bf16 v[32:47], v[100:103], v[104:107], v[32:47]\n\tds_read_b64_tr_b16 v[100:101], %[ap] offset:24576\n\tds_read_b64_tr_b16 v[102:103], %[ap] offset:25088\n\tds_read_b128 v[104:107], %[bp] offset:384\n\ts_waitcnt lgkmcnt(9)\n\tv_mfma_f32_32x32x16_bf16 v[32:47], v[108:111], v[112:115], v[32:47]\n\tds_read_b64_tr_b16 v[108:109], %[ap] offset:26624\n\tds_read_b64_tr_b16 v[110:111], %[ap] offset:27136\n\tds_read_b128 v[112:115], %[bp] offset:416\n\ts_waitcnt lgkmcnt(9)\n\tv_mfma_f32_32x32x16_bf16 v[32:47], v[116:119], v[120:123], v[32:47]\n\tds_read_b64_tr_b16 v[116:117], %[ap] offset:28672\n\tds_read_b64_tr_b16 v[118:119], %[ap] offset:29184\n\tds_read_b128 v[120:123], %[bp] offset:448\n\ts_waitcnt lgkmcnt(9)\n\tv_mfma_f32_32x32x16_bf16 v[32:47], v[124:127], v[128:131], v[32:47]\n\tds_read_b64_tr_b16 v[124:125], %[ap] offset:30720\n\tds_read_b64_tr_b16 v[126:127], %[ap] offset:31232\n\tds_read_b128 v[128:131], %[bp] offset:480\n\ts_waitcnt lgkmcnt(9)\n\tv_mfma_f32_32x32x16_bf16 v[32:47], v[100:103], v[104:107], v[32:47]\n\tds_read_b64_tr_b16 v[100:101], %[ap] offset:32768\n\tds_read_b64_tr_b16 v[102:103], %[ap] offset:33280\n\tds_read_b128 v[104:107], %[bp] offset:512\n\ts_waitcnt lgkmcnt(9)\n\tv_mfma_f32_32x32x16_bf16 v[32:47], v[108:111], v[112:115], v[32:47]\n\tds_read_b64_tr_b16 v[108:109], %[ap] offset:34816\n\tds_read_b64_tr_b16 v[110:111], %[ap] offset:35328\n\tds_read_b128 v[112:115], %[bp] offset:544\n\ts_waitcnt lgkmcnt(9)\n\tv_mfma_f32_32x32x16_bf16 v[32:47], v[116:119], v[120:123], v[32:47]\n\tds_read_b64_tr_b16 v[116:117], %[ap] offset:36864\n\tds_read_b64_tr_b16 v[118:119], %[ap] offset:37376\n\tds_read_b128 v[120:123], %[bp] offset:576\n\ts_waitcnt lgkmcnt(9)\n\tv_mfma_f32_32x32x16_bf16 v[32:47], v[124:127], v[128:131], v[32:47]\n\tds_read_b64_tr_b16 v[124:125], %[ap] offset:38912\n\tds_read_b64_tr_b16 v[126:127], %[ap] offset:39424\n\tds_read_b128 v[128:131], %[bp] offset:608\n\ts_waitcnt lgkmcnt(9)\n\tv_mfma_f32_32x32x16_bf16 v[32:47], v[100:103], v[104:107], v[32:47]\n\tds_read_b64_tr_b16 v[100:101], %[ap] offset:40960\n\tds_read_b64_tr_b16 v[102:103], %[ap] offset:41472\n\tds_read_b128 v[104:107], %[bp] offset:640\n\ts_waitcnt lgkmcnt(9)\n\tv_mfma_f32_32x32x16_bf16 v[32:47], v[108:111], v[112:115], v[32:47]\n\tds_read_b64_tr_b16 v[108:109], %[ap] offset:43008\n\tds_read_b64_tr_b16 v[110:111], %[ap] offset:43520\n\tds_read_b128 v[112:115], %[bp] offset:672\n\ts_waitcnt lgkmcnt(9)\n\tv_mfma_f32_32x32x16_bf16 v[32:47], v[116:119], v[120:123], v[32:47]\n\tds_read_b64_tr_b16 v[116:117], %[ap] offset:45056\n\tds_read_b64_tr_b16 v[118:119], %[ap] offset:45568\n\tds_read_b128 v[120:123], %[bp] offset:704\n\ts_waitcnt lgkmcnt(9)\n\tv_mfma_f32_32x32x16_bf16 v[32:47], v[124:127], v[128:131], v[32:47]\n\tds_read_b64_tr_b16 v[124:125], %[ap] offset:47104\n\tds_read_b64_tr_b16 v[126:127], %[ap] offset:47616\n\tds_read_b128 v[128:131], %[bp] offset:736\n\ts_waitcnt lgkmcnt(9)\n\tv_mfma_f32_32x32x16_bf16 v[32:47], v[100:103], v[104:107], v[32:47]\n\tds_read_b64_tr_b16 v[100:101], %[ap] offset:49152\n\tds_read_b64_tr_b16 v[102:103], %[ap] offset:49664\n\tds_read_b128 v[104:107], %[bp] offset:768\n\ts_waitcnt lgkmcnt(9)\n\tv_mfma_f32_32x32x16_bf16 v[32:47], v[108:111], v[112:115], v[32:47]\n\tds_read_b64_tr_b16 v[108:109], %[ap] offset:51200\n\tds_read_b64_tr_b16 v[110:111], %[ap] offset:51712\n\tds_read_b128 v[112:115], %[bp] offset:800\n\ts_waitcnt lgkmcnt(9)\n\tv_mfma_f32_32x32x16_bf16 v[32:47], v[116:119], v[120:123], v[32:47]\n\tds_read_b64_tr_b16 v[116:117], %[ap] offset:53248\n\tds_read_b64_tr_b16 v[118:119], %[ap] offset:53760\n\tds_read_b128 v[120:123], %[bp] offset:832\n\ts_waitcnt lgkmcnt(9)\n\tv_mfma_f32_32x32x16_bf16 v[32:47], v[124:127], v[128:131], v[32:47]\n\tds_read_b64_tr_b16 v[124:125], %[ap] offset:55296\n\tds_read_b64_tr_b16 v[126:127], %[ap] offset:55808\n\tds_read_b128 v[128:131], %[bp] offset:864\n\ts_waitcnt lgkmcnt(9)\n\tv_mfma_f32_32x32x16_bf16 v[32:47], v[100:103], v[104:107], v[32:47]\n\tds_read_b64_tr_b16 v[100:101], %[ap] offset:57344\n\tds_read_b64_tr_b16 v[102:103], %[ap] offset:57856\n\tds_read_b128 v[104:107], %[bp] offset:896\n\ts_waitcnt lgkmcnt(9)\n\tv_mfma_f32_32x32x16_bf16 v[32:47], v[108:111], v[112:115], v[32:47]\n\tds_read_b64_tr_b16 v[108:109], %[ap] offset:59392\n\tds_read_b64_tr_b16 v[110:111], %[ap] offset:59904\n\tds_read_b128 v[112:115], %[bp] offset:928\n\ts_waitcnt lgkmcnt(9)\n\tv_mfma_f32_32x32x16_bf16 v[32:47], v[116:119], v[120:123], v[32:47]\n\tds_read_b64_tr_b16 v[116:117], %[ap] offset:61440\n\tds_read_b64_tr_b16 v[118:119], %[ap] offset:61952\n\tds_read_b128 v[120:123], %[bp] offset:960\n\ts_waitcnt lgkmcnt(9)\n\tv_mfma_f32_32x32x16_bf16 v[32:47], v[124:127], v[128:131], v[32:47]\n\tds_read_b64_tr_b16 v[124:125], %[ap] offset:63488\n\tds_read_b64_tr_b16 v[126:127], %[ap] offset:64000\n\tds_read_b128 v[128:131], %[bp] offset:992\n\ts_waitcnt lgkmcnt(9)\n\tv_mfma_f32_32x32x16_bf16 v[32:47], v[100:103], v[104:107], v[32:47]\n\ts_waitcnt lgkmcnt(6)\n\tv_mfma_f32_32x32x16_bf16 v[32:47], v[108:111], v[112:115], v[32:47]\n\ts_waitcnt lgkmcnt(3)\n\tv_mfma_f32_32x32x16_bf16 v[32:47], v[116:119], v[120:123], v[32:47]\n\ts_waitcnt lgkmcnt(0)\n\tv_mfma_f32_32x32x16_bf16 v[32:47], v[124:127], v[128:131], v[32:47]\n\ts_nop 15\n\ts_nop 3" : "=&{v[32:47]}"(acc) : [ap] "v"(ap), [bp] "v"(bp) : "v100", "v101", "v102", "v103", "v104", "v105", "v106", "v107", "v108", "v109", "v110", "v111", "v112", "v113", "v114", "v115", "v116", "v117", "v118", "v119", "v120", "v121", "v122", "v123", "v124", "v125", "v126", "v127", "v128", "v129", "v130", "v131", "memory");
            const float sc = 0.25f * 1.4426950408889634f;
            u32x4 qo;
            #pragma unroll
            for (int i = 0; i < 4; ++i) { qf[i] = pk2(acc[2 * i] * sc, acc[2 * i + 1] * sc); qo[i] = pk2(acc[8 + 2 * i] * sc, acc[9 + 2 * i] * sc); }
            *(LAS u32x4*)(L + L_QX + w * 1024 + l * 16) = qo;
            ST(3);
        }
    }

    const int rep = b & 7;
    if (w >= 4) {
        const int bw = w - 4;
        if (w >= 6) tag = get_tag(L);
        {
            GAS unsigned* fp = (GAS unsigned*)(ws + WS_F1 + rep * 1024) + bw * 64 + l;
            _Pragma("nounroll") for (int i = 0; i < SPIN_CAP; ++i) {
                unsigned v = __hip_atomic_load(fp, RLX_AGENT);
                if (__all(v == tag)) break;
                __builtin_amdgcn_s_sleep(1);
            }
            FENCE();
        }
        ST(4);
        u32x4 kf[16], vf[16];
        #pragma unroll
        for (int i = 0; i < 16; ++i) {
            kf[i] = __builtin_amdgcn_raw_buffer_load_b128(rs, l * 16, WS_KF + (4 * i + bw) * 1024, 16);
            vf[i] = __builtin_amdgcn_raw_buffer_load_b128(rs, l * 16, WS_VF + (4 * i + bw) * 1024, 16);
        }
        lds_wait_ge(L, L_C5, 2u);
        LAS char* kd = L + bw * REC + l * 16;
        LAS char* vd = L + bw * REC + 1024 + (l >> 5) * 528 + (l & 31) * 16;
        const u32x4 ones = {0x3f803f80u, 0x3f803f80u, 0x3f803f80u, 0x3f803f80u};
        LAS char* od = L + ((l >> 1) * 4 + bw) * REC + 1024 + (l & 1) * 528 + 512;
        #pragma unroll
        for (int i = 0; i < 3; ++i) { *(LAS u32x4*)(kd + i * 4 * REC) = kf[i]; *(LAS u32x4*)(vd + i * 4 * REC) = vf[i]; }
        if (l < 6) *(LAS u32x4*)od = ones;
        ST(5);
        BARRIER();
        ST(6); AP(6);
        qf = *(LAS u32x4*)(L + L_QX + bw * 1024 + l * 16);
        #pragma unroll
        for (int i = 3; i < 16; ++i) { *(LAS u32x4*)(kd + i * 4 * REC) = kf[i]; *(LAS u32x4*)(vd + i * 4 * REC) = vf[i]; }
        if (l >= 6 && l < 32) *(LAS u32x4*)od = ones;
        lds_signal(L, L_C4, l);
        if (b == 0 && w == 4 && l == 0) __hip_atomic_store((GAS unsigned*)(ws + WS_EPOCH), tag, RLX_AGENT);
    } else {
        BARRIER();
        ST(6); AP(6);
        tag = get_tag(L);
    }

    const int ft = xcd * 28 + j;
    if (j < 28) {
        const u16* wsrc = ((l & 1) ? w_up : w_gate) + (size_t)krow * 1792 + 8 * ft;
        #pragma unroll
        for (int i = 0; i < 2; ++i) { const int s = 2 * w + i; dma16p(wsrc + (size_t)s * 32 * 1792, LB + L_FW1 + s * 1024); }
    }
    if (w < 7) dma16(krow * 1024 + (l & 1) * 16, (const char*)(w_down + (size_t)(224 * xcd + 32 * w) * 512 + 16 * j), LB + L_FW2 + w * 1024);

    const int hl = 2 * (w & 1) + (w >> 2), tha = (w >> 1) & 1;
    unsigned kp = LB + l * 16;
    unsigned vp = LB + 1024 + ((l & 31) < 16 ? (l >> 5) * 256 + (l & 31) * 16 : 512);
    const int idxv = (int)0xDDDDDDDD;
    f32x16 X, Y, acc; u32x4 KR, V0, V1;
    for (int i = 0; i < 16; ++i) acc[i] = 0.f;
    if (w >= 4) __builtin_amdgcn_s_setprio(1);
    ST(7); STC(30); AP(7);
    asm volatile("s_branch MQA_OVER\n\tMQA_BODY:\n\tv_exp_f32 v32, v32\n\tv_exp_f32 v33, v33\n\ts_waitcnt lgkmcnt(2)\n\tv_smfmac_f32_32x32x32_bf16 v[64:79], v[88:91], v[56:63], v96\n\tds_read_b128 v[88:91], v98 offset:528\n\tv_exp_f32 v34, v34\n\tv_exp_f32 v35, v35\n\tv_exp_f32 v36, v36\n\tv_exp_f32 v37, v37\n\ts_waitcnt lgkmcnt(2)\n\tv_mfma_f32_32x32x16_bf16 v[48:63], v[80:83], v[92:95], 0\n\tds_read_b128 v[80:83], v97 offset:4160\n\tv_exp_f32 v38, v38\n\tv_exp_f32 v39, v39\n\tv_exp_f32 v40, v40\n\tv_exp_f32 v41, v41\n\ts_waitcnt lgkmcnt(2)\n\tv_smfmac_f32_32x32x32_bf16 v[64:79], v[84:87], v[32:39], v96\n\tds_read_b128 v[84:87], v98 offset:2080\n\tv_exp_f32 v42, v42\n\tv_exp_f32 v43, v43\n\tv_exp_f32 v44, v44\n\tv_exp_f32 v45, v45\n\tv_exp_f32 v46, v46\n\tv_exp_f32 v47, v47\n\tv_exp_f32 v48, v48\n\tv_exp_f32 v49, v49\n\ts_waitcnt lgkmcnt(2)\n\tv_smfmac_f32_32x32x32_bf16 v[64:79], v[88:91], v[40:47], v96\n\tds_read_b128 v[88:91], v98 offset:2608\n\tv_exp_f32 v50, v50\n\tv_exp_f32 v51, v51\n\tv_exp_f32 v52, v52\n\tv_exp_f32 v53, v53\n\ts_waitcnt lgkmcnt(2)\n\tv_mfma_f32_32x32x16_bf16 v[32:47], v[80:83], v[92:95], 0\n\tds_read_b128 v[80:83], v97 offset:6240\n\tv_exp_f32 v54, v54\n\tv_exp_f32 v55, v55\n\tv_exp_f32 v56, v56\n\tv_exp_f32 v57, v57\n\ts_waitcnt lgkmcnt(2)\n\tv_smfmac_f32_32x32x32_bf16 v[64:79], v[84:87], v[48:55], v96\n\tds_read_b128 v[84:87], v98 offset:4160\n\tv_exp_f32 v58, v58\n\tv_exp_f32 v59, v59\n\tv_exp_f32 v60, v60\n\tv_exp_f32 v61, v61\n\tv_exp_f32 v62, v62\n\tv_exp_f32 v63, v63\n\tv_exp_f32 v32, v32\n\tv_exp_f32 v33, v33\n\ts_waitcnt lgkmcnt(2)\n\tv_smfmac_f32_32x32x32_bf16 v[64:79], v[88:91], v[56:63], v96\n\tds_read_b128 v[88:91], v98 offset:4688\n\tv_exp_f32 v34, v34\n\tv_exp_f32 v35, v35\n\tv_exp_f32 v36, v36\n\tv_exp_f32 v37, v37\n\ts_waitcnt lgkmcnt(2)\n\tv_mfma_f32_32x32x16_bf16 v[48:63], v[80:83], v[92:95], 0\n\tds_read_b128 v[80:83], v97 offset:8320\n\tv_exp_f32 v38, v38\n\tv_exp_f32 v39, v39\n\tv_exp_f32 v40, v40\n\tv_exp_f32 v41, v41\n\ts_waitcnt lgkmcnt(2)\n\tv_smfmac_f32_32x32x32_bf16 v[64:79], v[84:87], v[32:39], v96\n\tds_read_b128 v[84:87], v98 offset:6240\n\tv_exp_f32 v42, v42\n\tv_exp_f32 v43, v43\n\tv_exp_f32 v44, v44\n\tv_exp_f32 v45, v45\n\tv_exp_f32 v46, v46\n\tv_exp_f32 v47, v47\n\tv_exp_f32 v48, v48\n\tv_exp_f32 v49, v49\n\ts_waitcnt lgkmcnt(2)\n\tv_smfmac_f32_32x32x32_bf16 v[64:79], v[88:91], v[40:47], v96\n\tds_read_b128 v[88:91], v98 offset:6768\n\tv_exp_f32 v50, v50\n\tv_exp_f32 v51, v51\n\tv_exp_f32 v52, v52\n\tv_exp_f32 v53, v53\n\ts_waitcnt lgkmcnt(2)\n\tv_mfma_f32_32x32x16_bf16 v[32:47], v[80:83], v[92:95], 0\n\tds_read_b128 v[80:83], v97 offset:10400\n\tv_exp_f32 v54, v54\n\tv_exp_f32 v55, v55\n\tv_exp_f32 v56, v56\n\tv_exp_f32 v57, v57\n\ts_waitcnt lgkmcnt(2)\n\tv_smfmac_f32_32x32x32_bf16 v[64:79], v[84:87], v[48:55], v96\n\tds_read_b128 v[84:87], v98 offset:8320\n\tv_exp_f32 v58, v58\n\tv_exp_f32 v59, v59\n\tv_exp_f32 v60, v60\n\tv_exp_f32 v61, v61\n\tv_exp_f32 v62, v62\n\tv_exp_f32 v63, v63\n\tv_exp_f32 v32, v32\n\tv_exp_f32 v33, v33\n\ts_waitcnt lgkmcnt(2)\n\tv_smfmac_f32_32x32x32_bf16 v[64:79], v[88:91], v[56:63], v96\n\tds_read_b128 v[88:91], v98 offset:8848\n\tv_exp_f32 v34, v34\n\tv_exp_f32 v35, v35\n\tv_exp_f32 v36, v36\n\tv_exp_f32 v37, v37\n\ts_waitcnt lgkmcnt(2)\n\tv_mfma_f32_32x32x16_bf16 v[48:63], v[80:83], v[92:95], 0\n\tds_read_b128 v[80:83], v97 offset:12480\n\tv_exp_f32 v38, v38\n\tv_exp_f32 v39, v39\n\tv_exp_f32 v40, v40\n\tv_exp_f32 v41, v41\n\ts_waitcnt lgkmcnt(2)\n\tv_smfmac_f32_32x32x32_bf16 v[64:79], v[84:87], v[32:39], v96\n\tds_read_b128 v[84:87], v98 offset:10400\n\tv_exp_f32 v42, v42\n\tv_exp_f32 v43, v43\n\tv_exp_f32 v44, v44\n\tv_exp_f32 v45, v45\n\tv_exp_f32 v46, v46\n\tv_exp_f32 v47, v47\n\tv_exp_f32 v48, v48\n\tv_exp_f32 v49, v49\n\ts_waitcnt lgkmcnt(2)\n\tv_smfmac_f32_32x32x32_bf16 v[64:79], v[88:91], v[40:47], v96\n\tds_read_b128 v[88:91], v98 offset:10928\n\tv_exp_f32 v50, v50\n\tv_exp_f32 v51, v51\n\tv_exp_f32 v52, v52\n\tv_exp_f32 v53, v53\n\ts_waitcnt lgkmcnt(2)\n\tv_mfma_f32_32x32x16_bf16 v[32:47], v[80:83], v[92:95], 0\n\tds_read_b128 v[80:83], v97 offset:14560\n\tv_exp_f32 v54, v54\n\tv_exp_f32 v55, v55\n\tv_exp_f32 v56, v56\n\tv_exp_f32 v57, v57\n\ts_waitcnt lgkmcnt(2)\n\tv_smfmac_f32_32x32x32_bf16 v[64:79], v[84:87], v[48:55], v96\n\tds_read_b128 v[84:87], v98 offset:12480\n\tv_exp_f32 v58, v58\n\tv_exp_f32 v59, v59\n\tv_exp_f32 v60, v60\n\tv_exp_f32 v61, v61\n\tv_exp_f32 v62, v62\n\tv_exp_f32 v63, v63\n\tv_exp_f32 v32, v32\n\tv_exp_f32 v33, v33\n\ts_waitcnt lgkmcnt(2)\n\tv_smfmac_f32_32x32x32_bf16 v[64:79], v[88:91], v[56:63], v96\n\tds_read_b128 v[88:91], v98 offset:13008\n\tv_exp_f32 v34, v34\n\tv_exp_f32 v35, v35\n\tv_exp_f32 v36, v36\n\tv_exp_f32 v37, v37\n\ts_waitcnt lgkmcnt(2)\n\tv_mfma_f32_32x32x16_bf16 v[48:63], v[80:83], v[92:95], 0\n\tds_read_b128 v[80:83], v97 offset:16640\n\tv_exp_f32 v38, v38\n\tv_exp_f32 v39, v39\n\tv_exp_f32 v40, v40\n\tv_exp_f32 v41, v41\n\ts_waitcnt lgkmcnt(2)\n\tv_smfmac_f32_32x32x32_bf16 v[64:79], v[84:87], v[32:39], v96\n\tds_read_b128 v[84:87], v98 offset:14560\n\tv_exp_f32 v42, v42\n\tv_exp_f32 v43, v43\n\tv_exp_f32 v44, v44\n\tv_exp_f32 v45, v45\n\tv_exp_f32 v46, v46\n\tv_exp_f32 v47, v47\n\tv_exp_f32 v48, v48\n\tv_exp_f32 v49, v49\n\ts_waitcnt lgkmcnt(2)\n\tv_smfmac_f32_32x32x32_bf16 v[64:79], v[88:91], v[40:47], v96\n\tds_read_b128 v[88:91], v98 offset:15088\n\tv_exp_f32 v50, v50\n\tv_exp_f32 v51, v51\n\tv_exp_f32 v52, v52\n\tv_exp_f32 v53, v53\n\ts_waitcnt lgkmcnt(2)\n\tv_mfma_f32_32x32x16_bf16 v[32:47], v[80:83], v[92:95], 0\n\tds_read_b128 v[80:83], v97 offset:18720\n\tv_exp_f32 v54, v54\n\tv_exp_f32 v55, v55\n\tv_exp_f32 v56, v56\n\tv_exp_f32 v57, v57\n\ts_waitcnt lgkmcnt(2)\n\tv_smfmac_f32_32x32x32_bf16 v[64:79], v[84:87], v[48:55], v96\n\tds_read_b128 v[84:87], v98 offset:16640\n\tv_exp_f32 v58, v58\n\tv_exp_f32 v59, v59\n\tv_exp_f32 v60, v60\n\tv_exp_f32 v61, v61\n\tv_exp_f32 v62, v62\n\tv_exp_f32 v63, v63\n\tv_add_u32 v97, 16640, v97\n\tv_add_u32 v98, 16640, v98\n\ts_sub_u32 s78, s78, 1\n\ts_cmp_lg_u32 s78, 0\n\ts_cbranch_scc1 MQA_BODY\n\ts_waitcnt lgkmcnt(0)\n\ts_nop 7\n\ts_setpc_b64 s[76:77]\n\tMQA_OVER:\n\tds_read_b128 v[80:83], v97 offset:0\n\tds_read_b128 v[84:87], v98 offset:0\n\ts_waitcnt lgkmcnt(1)\n\tv_mfma_f32_32x32x16_bf16 v[32:47], v[80:83], v[92:95], 0\n\tds_read_b128 v[80:83], v97 offset:2080\n\tds_read_b128 v[88:91], v98 offset:528\n\tv_mov_b32 v56, 0\n\tv_mov_b32 v57, 0\n\tv_mov_b32 v58, 0\n\tv_mov_b32 v59, 0\n\tv_mov_b32 v60, 0\n\tv_mov_b32 v61, 0\n\tv_mov_b32 v62, 0\n\tv_mov_b32 v63, 0\n\tv_mov_b32 v48, 0\n\tv_mov_b32 v49, 0\n\tv_mov_b32 v50, 0\n\tv_mov_b32 v51, 0\n\tv_mov_b32 v52, 0\n\tv_mov_b32 v53, 0\n\tv_mov_b32 v54, 0\n\tv_mov_b32 v55, 0\n\ts_nop 7\n\ts_nop 3\n\ts_waitcnt lgkmcnt(0)"
                 : "={v[32:47]}"(X), "={v[48:63]}"(Y), "={v[80:83]}"(KR), "={v[84:87]}"(V0), "={v[88:91]}"(V1)
                 : "{v[92:95]}"(qf), "{v97}"(kp), "{v98}"(vp) : "memory");
#define SEG(N) asm volatile("s_mov_b32 s78, %[n]\n\ts_getpc_b64 s[76:77]\n\ts_add_u32 s76, s76, 12\n\ts_addc_u32 s77, s77, 0\n\ts_branch MQA_BODY" \
                     : "+{v[32:47]}"(X), "+{v[48:63]}"(Y), "+{v[64:79]}"(acc), "+{v[80:83]}"(KR), "+{v[84:87]}"(V0), "+{v[88:91]}"(V1), \
                       "+{v97}"(kp), "+{v98}"(vp) \
                     : [n] "n"(N), "{v[92:95]}"(qf), "{v96}"(idxv) : "s76", "s77", "s78", "scc", "memory")
    const int tho = hg >> 2, cg = hg & 3;
    const int n0 = cg * 128 + 16 * w;
    u32x4 fl0, fl1;
    if (w == 0) {
        fl0 = __builtin_amdgcn_raw_buffer_load_b128(rs, l * 16, WS_F2 + rep * 2048, 16);
        fl1 = __builtin_amdgcn_raw_buffer_load_b128(rs, l * 16, WS_F2 + rep * 2048 + 1024, 16);
    }
    SEG(1);
    ST(12);
    WAIT_VM0();
    lds_signal(L, L_C7, l);
    if (w == 0) {
        _Pragma("nounroll") for (int i = 0; i < SPIN_CAP; ++i) {
            if (__all(fl0[1] == tag && fl0[3] == tag && fl1[1] == tag && fl1[3] == tag)) break;
            FENCE();
            fl0 = __builtin_amdgcn_raw_buffer_load_b128(rs, l * 16, WS_F2 + rep * 2048, 16);
            fl1 = __builtin_amdgcn_raw_buffer_load_b128(rs, l * 16, WS_F2 + rep * 2048 + 1024, 16);
        }
        unsigned key = max(max(fl0[0], fl0[2]), max(fl1[0], fl1[2]));
        #pragma unroll
        for (int o = 1; o < 64; o <<= 1) { unsigned k2 = __shfl_xor(key, o); key = key > k2 ? key : k2; }
        key = __builtin_amdgcn_readfirstlane(key);
        if (l == 0) *(LAS unsigned*)(L + L_KEY) = key;
        lds_signal(L, L_C6, l);
        const int idx = 2047 - (int)(key & 0x7ffu);
        dma16(l * 16, (const char*)x + (size_t)idx * 1024, LB + L_XSEL);
    }
    lds_wait_ge(L, L_C4, 4u);
    SEG(1);
    ST(13);
    if (w == 0 && j < 28) {
        WAIT_VM0();
        lds_wait_ge(L, L_C7, 8u);
        f32x4 a = {0.f, 0.f, 0.f, 0.f};
        u32x4 fa[8], fb[8];
        #pragma unroll
        for (int hh = 0; hh < 2; ++hh) {
            #pragma unroll
            for (int s = 0; s < 8; ++s) {
                fa[s] = tr_frag(L + L_FW1 + (8 * hh + s) * 1024 + 8 * l);
                fb[s] = *(LAS u32x4*)(L + L_XSEL + (8 * hh + s) * 64 + g4 * 16);
            }
            __builtin_amdgcn_sched_barrier(0);
            #pragma unroll
            for (int s = 0; s < 8; ++s) a = mfma16(fa[s], fb[s], a);
            __builtin_amdgcn_sched_barrier(0);
        }
        float hv[4];
        #pragma unroll
        for (int i = 0; i < 4; ++i) {
            float u = bfr(__shfl_xor(a[i], 32));
            float gq = bfr(a[i]);
            hv[i] = bfr(gq * sigmoidf_(gq)) * u;
        }
        u32x4 gr = {pk2(hv[0], hv[1]), tag, pk2(hv[2], hv[3]), tag};
        if (l == 0 || l == 16) __builtin_amdgcn_raw_buffer_store_b128(gr, rs, g4 * 16, WS_HG + ft * 32, 16);
    }
    SEG(1);
    ST(14);
    lds_wait_ge(L, L_C6, 1u);
    const unsigned rkey = __builtin_amdgcn_readfirstlane(*(LAS unsigned*)(L + L_KEY));
    if (w == 1) fl0 = __builtin_amdgcn_raw_buffer_load_b128(rs, (l < 56 ? l : 55) * 16, WS_HG + xcd * 896, 16);
    u32x4 wof[16];
    u32x2 xres[2];
    float gres[2];
    #pragma unroll
    for (int s = 0; s < 16; ++s)
        wof[s] = __builtin_amdgcn_raw_buffer_load_b128(rs, l * 16, WS_WOF + ((cg * 8 + w) * 16 + s) * 1024, 16);
    #pragma unroll
    for (int t = 0; t < 2; ++t) {
        const int tok = tb * 64 + tho * 32 + 16 * t + l15;
        xres[t] = *(const GAS u32x2*)(x + (size_t)tok * 512 + n0 + 4 * g4);
        gres[t] = u2f(__hip_atomic_load((GAS unsigned*)(ws + WS_GATE) + tok, RLX_AGENT));
    }
    SEG(1);
    ST(15);
    if (w == 1) {
        _Pragma("nounroll") for (int i = 0; i < SPIN_CAP; ++i) {
            if (__all(fl0[1] == tag && fl0[3] == tag)) break;
            FENCE();
            fl0 = __builtin_amdgcn_raw_buffer_load_b128(rs, (l < 56 ? l : 55) * 16, WS_HG + xcd * 896, 16);
        }
        u32x2 hv = {fl0[0], fl0[2]};
        if (l < 56) *(LAS u32x2*)(L + L_HBUF + l * 8) = hv;
        WAIT_LGKM0();
        f32x4 a = {0.f, 0.f, 0.f, 0.f};
        u32x4 fa[7], fb[7];
        #pragma unroll
        for (int s = 0; s < 7; ++s) {
            fa[s] = tr_frag(L + L_FW2 + s * 1024 + 8 * l);
            fb[s] = *(LAS u32x4*)(L + L_HBUF + s * 64 + g4 * 16);
        }
        __builtin_amdgcn_sched_barrier(0);
        #pragma unroll
        for (int s = 0; s < 7; ++s) a = mfma16(fa[s], fb[s], a);
        u32x4 g0 = {f2u(a[0]), tag, f2u(a[1]), tag};
        u32x4 g1 = {f2u(a[2]), tag, f2u(a[3]), tag};
        if (l15 == 0) {
            __builtin_amdgcn_raw_buffer_store_b128(g0, rs, g4 * 32, WS_YG + (xcd * 512 + 16 * j) * 8, 16);
            __builtin_amdgcn_raw_buffer_store_b128(g1, rs, g4 * 32 + 16, WS_YG + (xcd * 512 + 16 * j) * 8, 16);
        }
    }
    SEG(PRIO_N);
    ST(16);
    __builtin_amdgcn_s_setprio(0);
    SEG(4 - PRIO_N);
    asm volatile("s_nop 1\n\tv_smfmac_f32_32x32x32_bf16 v[64:79], v[88:91], v[56:63], v96\n\ts_nop 15\n\ts_nop 7" : "+{v[64:79]}"(acc) : "{v[48:63]}"(Y), "{v[88:91]}"(V1), "{v96}"(idxv) : "memory");
    __builtin_amdgcn_s_setprio(0);
    ST(8); STC(31); AP(8);

    {
        const float ls = acc[8];
        bool ok = ls >= 8.6736174e-19f && ls <= 1.2676506e30f;
        #pragma unroll
        for (int i = 0; i < 8; ++i) ok = ok && (__builtin_fabsf(acc[i]) < __builtin_inff());
        if (!__all(ok)) {
            LAS char* kq = L + l * 16;
            LAS char* vq = L + 1024 + ((l & 31) < 16 ? (l >> 5) * 256 + (l & 31) * 16 : 512);
            acc = attn_true_max(kq, vq, qf);
        }
    }
    {
        const float inv = 1.f / acc[8];
        u32x4 o;
        #pragma unroll
        for (int i = 0; i < 4; ++i) { unsigned u = pk2(acc[2 * i] * inv, acc[2 * i + 1] * inv); o[i] = u < 0xFFFFFFFEu ? u : 0xFFFFFFFEu; }
        __builtin_amdgcn_raw_buffer_store_b128(o, rs, l * 16, WS_OF + ((tb * 2 + tha) * 32 + 4 * hg + hl) * 1024, 16);
    }
    const int idx = 2047 - (int)(rkey & 0x7ffu);
    const bool has_idx = (idx >> 5) == tb * 2 + tho;
    u32x4 yg = {0u, tag, 0u, tag};
    const unsigned yoff = ((l >> 3) * 512 + n0 + 2 * (l & 7)) * 8;
    if (has_idx) yg = __builtin_amdgcn_raw_buffer_load_b128(rs, yoff, WS_YG, 16);
    BARRIER();
    ST(9); AP(9);
    {
        u32x4 f[4];
        const unsigned src = WS_OF + ((tb * 2 + tho) * 32 + 4 * w) * 1024;
        _Pragma("nounroll") for (int i = 0; i < SPIN_CAP; ++i) {
            bool ok = true;
            #pragma unroll
            for (int k = 0; k < 4; ++k) {
                f[k] = __builtin_amdgcn_raw_buffer_load_b128(rs, l * 16, src + k * 1024, 16);
                ok = ok && f[k][0] != 0xFFFFFFFFu && f[k][1] != 0xFFFFFFFFu && f[k][2] != 0xFFFFFFFFu && f[k][3] != 0xFFFFFFFFu;
            }
            if (__all(ok)) break;
            FENCE();
        }
        #pragma unroll
        for (int k = 0; k < 4; ++k) *(LAS u32x4*)(L + (4 * w + k) * 1024 + l * 16) = f[k];
    }
    ST(10); AP(10);
    BARRIER();
    f32x4 oa[2];
    {
        const unsigned op = LB + (g4 >> 1) * 1024 + (g4 & 1) * 512 + l15 * 16;
        asm volatile("ds_read_b128 v[40:43], %[op] offset:0\n\tds_read_b128 v[44:47], %[op] offset:256\n\tds_read_b128 v[48:51], %[op] offset:2048\n\tds_read_b128 v[52:55], %[op] offset:2304\n\tds_read_b128 v[56:59], %[op] offset:4096\n\tds_read_b128 v[60:63], %[op] offset:4352\n\tds_read_b128 v[64:67], %[op] offset:6144\n\tds_read_b128 v[68:71], %[op] offset:6400\n\tds_read_b128 v[72:75], %[op] offset:8192\n\tds_read_b128 v[76:79], %[op] offset:8448\n\tds_read_b128 v[80:83], %[op] offset:10240\n\tds_read_b128 v[84:87], %[op] offset:10496\n\ts_waitcnt lgkmcnt(11)\n\tv_mfma_f32_16x16x32_bf16 v[32:35], %[w0], v[40:43], 0\n\ts_waitcnt lgkmcnt(10)\n\tv_mfma_f32_16x16x32_bf16 v[36:39], %[w0], v[44:47], 0\n\tds_read_b128 v[40:43], %[op] offset:12288\n\tds_read_b128 v[44:47], %[op] offset:12544\n\ts_waitcnt lgkmcnt(11)\n\tv_mfma_f32_16x16x32_bf16 v[32:35], %[w1], v[48:51], v[32:35]\n\ts_waitcnt lgkmcnt(10)\n\tv_mfma_f32_16x16x32_bf16 v[36:39], %[w1], v[52:55], v[36:39]\n\tds_read_b128 v[48:51], %[op] offset:14336\n\tds_read_b128 v[52:55], %[op] offset:14592\n\ts_waitcnt lgkmcnt(11)\n\tv_mfma_f32_16x16x32_bf16 v[32:35], %[w2], v[56:59], v[32:35]\n\ts_waitcnt lgkmcnt(10)\n\tv_mfma_f32_16x16x32_bf16 v[36:39], %[w2], v[60:63], v[36:39]\n\tds_read_b128 v[56:59], %[op] offset:16384\n\tds_read_b128 v[60:63], %[op] offset:16640\n\ts_waitcnt lgkmcnt(11)\n\tv_mfma_f32_16x16x32_bf16 v[32:35], %[w3], v[64:67], v[32:35]\n\ts_waitcnt lgkmcnt(10)\n\tv_mfma_f32_16x16x32_bf16 v[36:39], %[w3], v[68:71], v[36:39]\n\tds_read_b128 v[64:67], %[op] offset:18432\n\tds_read_b128 v[68:71], %[op] offset:18688\n\ts_waitcnt lgkmcnt(11)\n\tv_mfma_f32_16x16x32_bf16 v[32:35], %[w4], v[72:75], v[32:35]\n\ts_waitcnt lgkmcnt(10)\n\tv_mfma_f32_16x16x32_bf16 v[36:39], %[w4], v[76:79], v[36:39]\n\tds_read_b128 v[72:75], %[op] offset:20480\n\tds_read_b128 v[76:79], %[op] offset:20736\n\ts_waitcnt lgkmcnt(11)\n\tv_mfma_f32_16x16x32_bf16 v[32:35], %[w5], v[80:83], v[32:35]\n\ts_waitcnt lgkmcnt(10)\n\tv_mfma_f32_16x16x32_bf16 v[36:39], %[w5], v[84:87], v[36:39]\n\tds_read_b128 v[80:83], %[op] offset:22528\n\tds_read_b128 v[84:87], %[op] offset:22784\n\ts_waitcnt lgkmcnt(11)\n\tv_mfma_f32_16x16x32_bf16 v[32:35], %[w6], v[40:43], v[32:35]\n\ts_waitcnt lgkmcnt(10)\n\tv_mfma_f32_16x16x32_bf16 v[36:39], %[w6], v[44:47], v[36:39]\n\tds_read_b128 v[40:43], %[op] offset:24576\n\tds_read_b128 v[44:47], %[op] offset:24832\n\ts_waitcnt lgkmcnt(11)\n\tv_mfma_f32_16x16x32_bf16 v[32:35], %[w7], v[48:51], v[32:35]\n\ts_waitcnt lgkmcnt(10)\n\tv_mfma_f32_16x16x32_bf16 v[36:39], %[w7], v[52:55], v[36:39]\n\tds_read_b128 v[48:51], %[op] offset:26624\n\tds_read_b128 v[52:55], %[op] offset:26880\n\ts_waitcnt lgkmcnt(11)\n\tv_mfma_f32_16x16x32_bf16 v[32:35], %[w8], v[56:59], v[32:35]\n\ts_waitcnt lgkmcnt(10)\n\tv_mfma_f32_16x16x32_bf16 v[36:39], %[w8], v[60:63], v[36:39]\n\tds_read_b128 v[56:59], %[op] offset:28672\n\tds_read_b128 v[60:63], %[op] offset:28928\n\ts_waitcnt lgkmcnt(11)\n\tv_mfma_f32_16x16x32_bf16 v[32:35], %[w9], v[64:67], v[32:35]\n\ts_waitcnt lgkmcnt(10)\n\tv_mfma_f32_16x16x32_bf16 v[36:39], %[w9], v[68:71], v[36:39]\n\tds_read_b128 v[64:67], %[op] offset:30720\n\tds_read_b128 v[68:71], %[op] offset:30976\n\ts_waitcnt lgkmcnt(11)\n\tv_mfma_f32_16x16x32_bf16 v[32:35], %[w10], v[72:75], v[32:35]\n\ts_waitcnt lgkmcnt(10)\n\tv_mfma_f32_16x16x32_bf16 v[36:39], %[w10], v[76:79], v[36:39]\n\ts_waitcnt lgkmcnt(9)\n\tv_mfma_f32_16x16x32_bf16 v[32:35], %[w11], v[80:83], v[32:35]\n\ts_waitcnt lgkmcnt(8)\n\tv_mfma_f32_16x16x32_bf16 v[36:39], %[w11], v[84:87], v[36:39]\n\ts_waitcnt lgkmcnt(7)\n\tv_mfma_f32_16x16x32_bf16 v[32:35], %[w12], v[40:43], v[32:35]\n\ts_waitcnt lgkmcnt(6)\n\tv_mfma_f32_16x16x32_bf16 v[36:39], %[w12], v[44:47], v[36:39]\n\ts_waitcnt lgkmcnt(5)\n\tv_mfma_f32_16x16x32_bf16 v[32:35], %[w13], v[48:51], v[32:35]\n\ts_waitcnt lgkmcnt(4)\n\tv_mfma_f32_16x16x32_bf16 v[36:39], %[w13], v[52:55], v[36:39]\n\ts_waitcnt lgkmcnt(3)\n\tv_mfma_f32_16x16x32_bf16 v[32:35], %[w14], v[56:59], v[32:35]\n\ts_waitcnt lgkmcnt(2)\n\tv_mfma_f32_16x16x32_bf16 v[36:39], %[w14], v[60:63], v[36:39]\n\ts_waitcnt lgkmcnt(1)\n\tv_mfma_f32_16x16x32_bf16 v[32:35], %[w15], v[64:67], v[32:35]\n\ts_waitcnt lgkmcnt(0)\n\tv_mfma_f32_16x16x32_bf16 v[36:39], %[w15], v[68:71], v[36:39]\n\ts_nop 7\n\ts_nop 1" : "=&{v[32:35]}"(oa[0]), "=&{v[36:39]}"(oa[1]) : [op] "v"(op), [w0] "v"(wof[0]), [w1] "v"(wof[1]), [w2] "v"(wof[2]), [w3] "v"(wof[3]), [w4] "v"(wof[4]), [w5] "v"(wof[5]), [w6] "v"(wof[6]), [w7] "v"(wof[7]), [w8] "v"(wof[8]), [w9] "v"(wof[9]), [w10] "v"(wof[10]), [w11] "v"(wof[11]), [w12] "v"(wof[12]), [w13] "v"(wof[13]), [w14] "v"(wof[14]), [w15] "v"(wof[15]) : "v40", "v41", "v42", "v43", "v44", "v45", "v46", "v47", "v48", "v49", "v50", "v51", "v52", "v53", "v54", "v55", "v56", "v57", "v58", "v59", "v60", "v61", "v62", "v63", "v64", "v65", "v66", "v67", "v68", "v69", "v70", "v71", "v72", "v73", "v74", "v75", "v76", "v77", "v78", "v79", "v80", "v81", "v82", "v83", "v84", "v85", "v86", "v87", "memory");
    }
    float yv[4] = {0.f, 0.f, 0.f, 0.f};
    if (has_idx) {
        _Pragma("nounroll") for (int i = 0; i < SPIN_CAP; ++i) {
            if (__all(yg[1] == tag && yg[3] == tag)) break;
            FENCE();
            yg = __builtin_amdgcn_raw_buffer_load_b128(rs, yoff, WS_YG, 16);
        }
        float y0 = u2f(yg[0]), y1 = u2f(yg[2]);
        #pragma unroll
        for (int o = 8; o < 64; o <<= 1) { y0 += __shfl_xor(y0, o); y1 += __shfl_xor(y1, o); }
        unsigned ord = rkey >> 16;
        unsigned rb = (ord & 0x8000u) ? (ord & 0x7fffu) : (~ord & 0xffffu);
        const float rsel = bfr(sigmoidf_(bflo(rb)));
        yv[0] = bfr(bfr(__shfl(y0, 2 * g4)) * rsel);     yv[1] = bfr(bfr(__shfl(y1, 2 * g4)) * rsel);
        yv[2] = bfr(bfr(__shfl(y0, 2 * g4 + 1)) * rsel); yv[3] = bfr(bfr(__shfl(y1, 2 * g4 + 1)) * rsel);
    }
    #pragma unroll
    for (int t = 0; t < 2; ++t) {
        const int tok = tb * 64 + tho * 32 + 16 * t + l15;
        const float g = gres[t], omg = bfr(1.f - g);
        float bx[4] = {bflo(xres[t][0]), bfhi(xres[t][0]), bflo(xres[t][1]), bfhi(xres[t][1])};
        float r4[4];
        #pragma unroll
        for (int i = 0; i < 4; ++i) {
            float bb = (has_idx && tok == idx) ? bfr(bx[i] + yv[i]) : bx[i];
            r4[i] = bfr(g * bfr(oa[t][i])) + bfr(omg * bb);
        }
        u32x2 o2 = {pk2(r4[0], r4[1]), pk2(r4[2], r4[3])};
        *(GAS u32x2*)(out + (size_t)tok * 512 + n0 + 4 * g4) = o2;
    }
    ST(11); AP(11);
}

extern "C" void kernel_launch(void* const* d_in, const int* in_sizes, int n_in, void* d_out, int out_size,
                              void* d_ws, size_t ws_size, hipStream_t stream) {
    (void)in_sizes; (void)n_in; (void)out_size; (void)ws_size;
    (void)hipFuncSetAttribute((const void*)mqa_moe_fused, hipFuncAttributeMaxDynamicSharedMemorySize, LDS_BYTES);
    mqa_moe_fused<<<256, 512, LDS_BYTES, stream>>>(
        (const u16*)d_in[0], (const u16*)d_in[1], (const u16*)d_in[2], (const u16*)d_in[3], (const u16*)d_in[4],
        (const u16*)d_in[6], (const u16*)d_in[7], (const u16*)d_in[8], (const u16*)d_in[9], (const u16*)d_in[10],
        (u16*)d_out, (char*)d_ws);
}
```

```cpp
#include <hip/hip_runtime.h>
#include <stdint.h>
#define PRIO_N 3
#define AMP_K 0
#define AMP_A 0
#define AMP_B 11

typedef unsigned short u16;
typedef __attribute__((ext_vector_type(8))) __bf16 bf16x8;
typedef __attribute__((ext_vector_type(16))) __bf16 bf16x16;
typedef __attribute__((ext_vector_type(2))) __bf16 bf16x2;
typedef __attribute__((ext_vector_type(4))) short s16x4;
typedef __attribute__((ext_vector_type(2))) float f32x2;
typedef __attribute__((ext_vector_type(4))) float f32x4;
typedef __attribute__((ext_vector_type(8))) float f32x8;
typedef __attribute__((ext_vector_type(16))) float f32x16;
typedef __attribute__((ext_vector_type(4))) unsigned u32x4;
typedef __attribute__((ext_vector_type(2))) unsigned u32x2;
#define LAS __attribute__((address_space(3)))
#define GAS __attribute__((address_space(1)))
#define RLX_AGENT __ATOMIC_RELAXED, __HIP_MEMORY_SCOPE_AGENT
#define RLX_WG __ATOMIC_RELAXED, __HIP_MEMORY_SCOPE_WORKGROUP

constexpr unsigned WS_EPOCH = 0;
constexpr unsigned WS_F1   = 4096;
constexpr unsigned WS_F2   = 16384;
constexpr unsigned WS_KF   = 32768;
constexpr unsigned WS_VF   = 98304;
constexpr unsigned WS_WOF  = 163840;
constexpr unsigned WS_OF   = 688128;
constexpr unsigned WS_GATE = 2785280;
constexpr unsigned WS_HG   = 2793472;
constexpr unsigned WS_YG   = 2801664;
constexpr unsigned WS_END  = 2834432;
constexpr unsigned WS_DBG  = 4194304;

constexpr unsigned REC     = 2080;
constexpr unsigned L_WKV   = 0;
constexpr unsigned L_WOST  = 16384;
constexpr unsigned L_XT    = 24960;
constexpr unsigned L_WQ    = 91520;
constexpr unsigned L_FW1   = 133120;
constexpr unsigned L_FW2   = 149504;
constexpr unsigned L_QX    = 157056;
constexpr unsigned L_MISC  = 161152;
constexpr unsigned L_C2 = L_MISC + 0, L_C4 = L_MISC + 4, L_C5 = L_MISC + 8, L_C6 = L_MISC + 12, L_C7 = L_MISC + 16,
                   L_TAG = L_MISC + 24  , L_KEY = L_MISC + 32;
constexpr unsigned L_XSEL  = L_MISC + 128;
constexpr unsigned L_HBUF  = L_XSEL + 1024;
constexpr unsigned LDS_BYTES = 163840;
constexpr int SPIN_CAP = 1 << 18;

__device__ __forceinline__ unsigned pk2(float a, float b) {
    f32x2 v = {a, b};
    return __builtin_bit_cast(unsigned, __builtin_convertvector(v, bf16x2));
}
__device__ __forceinline__ float bfr(float a) {
    return __builtin_bit_cast(float, pk2(a, a) & 0xffff0000u);
}
__device__ __forceinline__ unsigned f2u(float f) { return __builtin_bit_cast(unsigned, f); }
__device__ __forceinline__ float u2f(unsigned u) { return __builtin_bit_cast(float, u); }
__device__ __forceinline__ float bflo(unsigned u) { return __builtin_bit_cast(float, u << 16); }
__device__ __forceinline__ float bfhi(unsigned u) { return __builtin_bit_cast(float, u & 0xffff0000u); }
__device__ __forceinline__ float sigmoidf_(float z) { return __builtin_amdgcn_rcpf(1.f + __builtin_amdgcn_exp2f(-1.4426950408889634f * z)); }

__device__ __forceinline__ void dma16(unsigned voff, const void* sbase, unsigned lds) {
    unsigned keep;
    asm volatile("s_mov_b32 %0, m0\n\ts_mov_b32 m0, %3\n\ts_nop 0\n\tglobal_load_lds_dwordx4 %1, %2\n\ts_mov_b32 m0, %0"
                 : "=&s"(keep) : "v"(voff), "s"(sbase), "s"(lds) : "memory");
}
__device__ __forceinline__ void dma16p(const void* gsrc, unsigned lds) {
    unsigned keep;
    asm volatile("s_mov_b32 %0, m0\n\ts_mov_b32 m0, %2\n\ts_nop 0\n\tglobal_load_lds_dwordx4 %1, off\n\ts_mov_b32 m0, %0"
                 : "=&s"(keep) : "v"(gsrc), "s"(lds) : "memory");
}
#define WAIT_VM0()   asm volatile("s_waitcnt vmcnt(0)" ::: "memory")
#define WAIT_LGKM0() asm volatile("s_waitcnt lgkmcnt(0)" ::: "memory")
#define BARRIER()    asm volatile("s_waitcnt lgkmcnt(0)\n\ts_barrier" ::: "memory")
#define FENCE()      asm volatile("" ::: "memory")

__device__ __forceinline__ u32x4 tr_frag(LAS char* p) {
    s16x4 a0 = __builtin_amdgcn_ds_read_tr16_b64_v4i16((LAS s16x4*)p);
    s16x4 a1 = __builtin_amdgcn_ds_read_tr16_b64_v4i16((LAS s16x4*)(p + 512));
    u32x2 p0 = __builtin_bit_cast(u32x2, a0), p1 = __builtin_bit_cast(u32x2, a1);
    u32x4 a = {p0[0], p0[1], p1[0], p1[1]};
    return a;
}
__device__ __forceinline__ f32x4 mfma16(u32x4 a, u32x4 b, f32x4 c) {
    return __builtin_amdgcn_mfma_f32_16x16x32_bf16(__builtin_bit_cast(bf16x8, a), __builtin_bit_cast(bf16x8, b), c, 0, 0, 0);
}
__device__ __forceinline__ f32x16 mfma32(u32x4 a, u32x4 b, f32x16 c) {
    return __builtin_amdgcn_mfma_f32_32x32x16_bf16(__builtin_bit_cast(bf16x8, a), __builtin_bit_cast(bf16x8, b), c, 0, 0, 0);
}
__device__ __forceinline__ f32x16 smf(u32x4 v, f32x8 p, f32x16 acc) {
    return __builtin_amdgcn_smfmac_f32_32x32x32_bf16(__builtin_bit_cast(bf16x8, v), __builtin_bit_cast(bf16x16, p), acc, (int)0xDDDDDDDD, 0, 0);
}
__device__ __forceinline__ void lds_wait_ge(LAS char* L, unsigned off, unsigned want) {
    _Pragma("nounroll") for (int i = 0; i < SPIN_CAP; ++i) {
        if (__hip_atomic_load((LAS unsigned*)(L + off), RLX_WG) >= want) break;
        __builtin_amdgcn_s_sleep(1);
    }
    FENCE();
}
__device__ __forceinline__ void lds_signal(LAS char* L, unsigned off, int l) {
    WAIT_LGKM0();
    if (l == 0) __hip_atomic_fetch_add((LAS unsigned*)(L + off), 1u, RLX_WG);
    FENCE();
}
__device__ __forceinline__ unsigned get_tag(LAS char* L) {
    lds_wait_ge(L, L_TAG, 1u);
    return __builtin_amdgcn_readfirstlane(*(LAS unsigned*)(L + L_TAG + 4));
}
#define ST(i) do {} while (0)
#define STC(i) do {} while (0)
#define AP(i) do { if ((i) == AMP_A) tA = __builtin_amdgcn_s_memrealtime(); if ((i) == AMP_B) tB = __builtin_amdgcn_s_memrealtime(); } while (0)

__device__ __forceinline__ f32x16 attn_true_max(LAS char* kp, LAS char* vp, u32x4 q) {
    f32x16 z; for (int i = 0; i < 16; ++i) z[i] = 0.f;
    float m = -__builtin_inff();
    #pragma unroll 1
    for (int t = 0; t < 64; ++t) {
        f32x16 s = mfma32(*(LAS u32x4*)(kp + t * REC), q, z);
        #pragma unroll
        for (int i = 0; i < 16; ++i) m = fmaxf(m, s[i]);
    }
    m = fmaxf(m, __shfl_xor(m, 32));
    f32x16 acc = z;
    #pragma unroll 1
    for (int t = 0; t < 64; ++t) {
        f32x16 s = mfma32(*(LAS u32x4*)(kp + t * REC), q, z);
        f32x8 p0, p1;
        #pragma unroll
        for (int i = 0; i < 8; ++i) { p0[i] = __builtin_amdgcn_exp2f(s[i] - m); p1[i] = __builtin_amdgcn_exp2f(s[8 + i] - m); }
        acc = smf(*(LAS u32x4*)(vp + t * REC), p0, acc);
        acc = smf(*(LAS u32x4*)(vp + t * REC + 528), p1, acc);
    }
    return acc;
}

__global__ void __launch_bounds__(512, 2) mqa_moe_fused(
        const u16* __restrict__ x, const u16* __restrict__ wq, const u16* __restrict__ wk, const u16* __restrict__ wv,
        const u16* __restrict__ wo, const u16* __restrict__ w_r, const u16* __restrict__ w_gate, const u16* __restrict__ w_up,
        const u16* __restrict__ w_down, const u16* __restrict__ gate_w, u16* __restrict__ out, char* ws) {
    extern __shared__ __attribute__((aligned(16))) char smem[];
    LAS char* L = (LAS char*)smem;
    const unsigned LB = (unsigned)(size_t)L;
    const int l = threadIdx.x & 63;
    const int w = __builtin_amdgcn_readfirstlane(threadIdx.x >> 6);
    const int b = blockIdx.x, xcd = b & 7, j = b >> 3;
    const int tb = xcd * 4 + (j & 3), hg = j >> 2;
    const int g4 = l >> 4, l15 = l & 15;
    const __amdgpu_buffer_rsrc_t rs = __builtin_amdgcn_make_buffer_rsrc((void*)ws, 0, WS_END, 0x00020000);
    const int sg = (l >> 3) & 3, sjh = l >> 5, sq = (l >> 1) & 3;
    const int krow = 8 * sg + 4 * sjh + sq;
    unsigned tag = 0;
    u32x4 qf;
    unsigned long long tA = 0, tB = 0;
    ST(0); STC(29); AP(0);

    if (w == 4) {
        const int type = hg & 1, tg = hg >> 1;
        const u16* W = type ? wv : wk;
        const u16* xr = x + (size_t)(tb * 64 + tg * 16 + l15) * 512 + 8 * g4;
        u32x4 xf[16];
        const unsigned wvo = krow * 32 + (l & 1) * 16;
        #pragma unroll
        for (int s = 0; s < 16; ++s) {
            dma16(wvo, (const char*)W + s * 1024, LB + L_WKV + s * 1024);
            xf[s] = *(const GAS u32x4*)(xr + 32 * s);
        }
        unsigned E = __hip_atomic_load((GAS unsigned*)(ws + WS_EPOCH), RLX_AGENT);
        asm volatile("s_barrier" ::: "memory");
        WAIT_VM0();
        ST(1);
        tag = __builtin_amdgcn_readfirstlane(E) + 1u;
        if (l == 0) { u32x2 tv = {1u, tag}; *(LAS u32x2*)(L + L_TAG) = tv; }
        f32x4 acc;
        const unsigned wp = LB + L_WKV + 8 * l;
        if (type == 0) asm volatile("ds_read_b64_tr_b16 v[36:37], %[wp] offset:0\n\tds_read_b64_tr_b16 v[38:39], %[wp] offset:512\n\tds_read_b64_tr_b16 v[40:41], %[wp] offset:1024\n\tds_read_b64_tr_b16 v[42:43], %[wp] offset:1536\n\tds_read_b64_tr_b16 v[44:45], %[wp] offset:2048\n\tds_read_b64_tr_b16 v[46:47], %[wp] offset:2560\n\tds_read_b64_tr_b16 v[48:49], %[wp] offset:3072\n\tds_read_b64_tr_b16 v[50:51], %[wp] offset:3584\n\tds_read_b64_tr_b16 v[52:53], %[wp] offset:4096\n\tds_read_b64_tr_b16 v[54:55], %[wp] offset:4608\n\tds_read_b64_tr_b16 v[56:57], %[wp] offset:5120\n\tds_read_b64_tr_b16 v[58:59], %[wp] offset:5632\n\ts_waitcnt lgkmcnt(10)\n\tv_mfma_f32_16x16x32_bf16 v[32:35], v[36:39], %[x0], 0\n\tds_read_b64_tr_b16 v[36:37], %[wp] offset:6144\n\tds_read_b64_tr_b16 v[38:39], %[wp] offset:6656\n\ts_waitcnt lgkmcnt(10)\n\tv_mfma_f32_16x16x32_bf16 v[32:35], v[40:43], %[x1], v[32:35]\n\tds_read_b64_tr_b16 v[40:41], %[wp] offset:7168\n\tds_read_b64_tr_b16 v[42:43], %[wp] offset:7680\n\ts_waitcnt lgkmcnt(10)\n\tv_mfma_f32_16x16x32_bf16 v[32:35], v[44:47], %[x2], v[32:35]\n\tds_read_b64_tr_b16 v[44:45], %[wp] offset:8192\n\tds_read_b64_tr_b16 v[46:47], %[wp] offset:8704\n\ts_waitcnt lgkmcnt(10)\n\tv_mfma_f32_16x16x32_bf16 v[32:35], v[48:51], %[x3], v[32:35]\n\tds_read_b64_tr_b16 v[48:49], %[wp] offset:9216\n\tds_read_b64_tr_b16 v[50:51], %[wp] offset:9728\n\ts_waitcnt lgkmcnt(10)\n\tv_mfma_f32_16x16x32_bf16 v[32:35], v[52:55], %[x4], v[32:35]\n\tds_read_b64_tr_b16 v[52:53], %[wp] offset:10240\n\tds_read_b64_tr_b16 v[54:55], %[wp] offset:10752\n\ts_waitcnt lgkmcnt(10)\n\tv_mfma_f32_16x16x32_bf16 v[32:35], v[56:59], %[x5], v[32:35]\n\tds_read_b64_tr_b16 v[56:57], %[wp] offset:11264\n\tds_read_b64_tr_b16 v[58:59], %[wp] offset:11776\n\ts_waitcnt lgkmcnt(10)\n\tv_mfma_f32_16x16x32_bf16 v[32:35], v[36:39], %[x6], v[32:35]\n\tds_read_b64_tr_b16 v[36:37], %[wp] offset:12288\n\tds_read_b64_tr_b16 v[38:39], %[wp] offset:12800\n\ts_waitcnt lgkmcnt(10)\n\tv_mfma_f32_16x16x32_bf16 v[32:35], v[40:43], %[x7], v[32:35]\n\tds_read_b64_tr_b16 v[40:41], %[wp] offset:13312\n\tds_read_b64_tr_b16 v[42:43], %[wp] offset:13824\n\ts_waitcnt lgkmcnt(10)\n\tv_mfma_f32_16x16x32_bf16 v[32:35], v[44:47], %[x8], v[32:35]\n\tds_read_b64_tr_b16 v[44:45], %[wp] offset:14336\n\tds_read_b64_tr_b16 v[46:47], %[wp] offset:14848\n\ts_waitcnt lgkmcnt(10)\n\tv_mfma_f32_16x16x32_bf16 v[32:35], v[48:51], %[x9], v[32:35]\n\tds_read_b64_tr_b16 v[48:49], %[wp] offset:15360\n\tds_read_b64_tr_b16 v[50:51], %[wp] offset:15872\n\ts_waitcnt lgkmcnt(10)\n\tv_mfma_f32_16x16x32_bf16 v[32:35], v[52:55], %[x10], v[32:35]\n\ts_waitcnt lgkmcnt(8)\n\tv_mfma_f32_16x16x32_bf16 v[32:35], v[56:59], %[x11], v[32:35]\n\ts_waitcnt lgkmcnt(6)\n\tv_mfma_f32_16x16x32_bf16 v[32:35], v[36:39], %[x12], v[32:35]\n\ts_waitcnt lgkmcnt(4)\n\tv_mfma_f32_16x16x32_bf16 v[32:35], v[40:43], %[x13], v[32:35]\n\ts_waitcnt lgkmcnt(2)\n\tv_mfma_f32_16x16x32_bf16 v[32:35], v[44:47], %[x14], v[32:35]\n\ts_waitcnt lgkmcnt(0)\n\tv_mfma_f32_16x16x32_bf16 v[32:35], v[48:51], %[x15], v[32:35]\n\ts_nop 7\n\ts_nop 1" : "=&{v[32:35]}"(acc) : [wp] "v"(wp), [x0] "v"(xf[0]), [x1] "v"(xf[1]), [x2] "v"(xf[2]), [x3] "v"(xf[3]), [x4] "v"(xf[4]), [x5] "v"(xf[5]), [x6] "v"(xf[6]), [x7] "v"(xf[7]), [x8] "v"(xf[8]), [x9] "v"(xf[9]), [x10] "v"(xf[10]), [x11] "v"(xf[11]), [x12] "v"(xf[12]), [x13] "v"(xf[13]), [x14] "v"(xf[14]), [x15] "v"(xf[15]) : "v36", "v37", "v38", "v39", "v40", "v41", "v42", "v43", "v44", "v45", "v46", "v47", "v48", "v49", "v50", "v51", "v52", "v53", "v54", "v55", "v56", "v57", "v58", "v59", "memory");
        else           asm volatile("ds_read_b64_tr_b16 v[36:37], %[wp] offset:0\n\tds_read_b64_tr_b16 v[38:39], %[wp] offset:512\n\tds_read_b64_tr_b16 v[40:41], %[wp] offset:1024\n\tds_read_b64_tr_b16 v[42:43], %[wp] offset:1536\n\tds_read_b64_tr_b16 v[44:45], %[wp] offset:2048\n\tds_read_b64_tr_b16 v[46:47], %[wp] offset:2560\n\tds_read_b64_tr_b16 v[48:49], %[wp] offset:3072\n\tds_read_b64_tr_b16 v[50:51], %[wp] offset:3584\n\tds_read_b64_tr_b16 v[52:53], %[wp] offset:4096\n\tds_read_b64_tr_b16 v[54:55], %[wp] offset:4608\n\tds_read_b64_tr_b16 v[56:57], %[wp] offset:5120\n\tds_read_b64_tr_b16 v[58:59], %[wp] offset:5632\n\ts_waitcnt lgkmcnt(10)\n\tv_mfma_f32_16x16x32_bf16 v[32:35], %[x0], v[36:39], 0\n\tds_read_b64_tr_b16 v[36:37], %[wp] offset:6144\n\tds_read_b64_tr_b16 v[38:39], %[wp] offset:6656\n\ts_waitcnt lgkmcnt(10)\n\tv_mfma_f32_16x16x32_bf16 v[32:35], %[x1], v[40:43], v[32:35]\n\tds_read_b64_tr_b16 v[40:41], %[wp] offset:7168\n\tds_read_b64_tr_b16 v[42:43], %[wp] offset:7680\n\ts_waitcnt lgkmcnt(10)\n\tv_mfma_f32_16x16x32_bf16 v[32:35], %[x2], v[44:47], v[32:35]\n\tds_read_b64_tr_b16 v[44:45], %[wp] offset:8192\n\tds_read_b64_tr_b16 v[46:47], %[wp] offset:8704\n\ts_waitcnt lgkmcnt(10)\n\tv_mfma_f32_16x16x32_bf16 v[32:35], %[x3], v[48:51], v[32:35]\n\tds_read_b64_tr_b16 v[48:49], %[wp] offset:9216\n\tds_read_b64_tr_b16 v[50:51], %[wp] offset:9728\n\ts_waitcnt lgkmcnt(10)\n\tv_mfma_f32_16x16x32_bf16 v[32:35], %[x4], v[52:55], v[32:35]\n\tds_read_b64_tr_b16 v[52:53], %[wp] offset:10240\n\tds_read_b64_tr_b16 v[54:55], %[wp] offset:10752\n\ts_waitcnt lgkmcnt(10)\n\tv_mfma_f32_16x16x32_bf16 v[32:35], %[x5], v[56:59], v[32:35]\n\tds_read_b64_tr_b16 v[56:57], %[wp] offset:11264\n\tds_read_b64_tr_b16 v[58:59], %[wp] offset:11776\n\ts_waitcnt lgkmcnt(10)\n\tv_mfma_f32_16x16x32_bf16 v[32:35], %[x6], v[36:39], v[32:35]\n\tds_read_b64_tr_b16 v[36:37], %[wp] offset:12288\n\tds_read_b64_tr_b16 v[38:39], %[wp] offset:12800\n\ts_waitcnt lgkmcnt(10)\n\tv_mfma_f32_16x16x32_bf16 v[32:35], %[x7], v[40:43], v[32:35]\n\tds_read_b64_tr_b16 v[40:41], %[wp] offset:13312\n\tds_read_b64_tr_b16 v[42:43], %[wp] offset:13824\n\ts_waitcnt lgkmcnt(10)\n\tv_mfma_f32_16x16x32_bf16 v[32:35], %[x8], v[44:47], v[32:35]\n\tds_read_b64_tr_b16 v[44:45], %[wp] offset:14336\n\tds_read_b64_tr_b16 v[46:47], %[wp] offset:14848\n\ts_waitcnt lgkmcnt(10)\n\tv_mfma_f32_16x16x32_bf16 v[32:35], %[x9], v[48:51], v[32:35]\n\tds_read_b64_tr_b16 v[48:49], %[wp] offset:15360\n\tds_read_b64_tr_b16 v[50:51], %[wp] offset:15872\n\ts_waitcnt lgkmcnt(10)\n\tv_mfma_f32_16x16x32_bf16 v[32:35], %[x10], v[52:55], v[32:35]\n\ts_waitcnt lgkmcnt(8)\n\tv_mfma_f32_16x16x32_bf16 v[32:35], %[x11], v[56:59], v[32:35]\n\ts_waitcnt lgkmcnt(6)\n\tv_mfma_f32_16x16x32_bf16 v[32:35], %[x12], v[36:39], v[32:35]\n\ts_waitcnt lgkmcnt(4)\n\tv_mfma_f32_16x16x32_bf16 v[32:35], %[x13], v[40:43], v[32:35]\n\ts_waitcnt lgkmcnt(2)\n\tv_mfma_f32_16x16x32_bf16 v[32:35], %[x14], v[44:47], v[32:35]\n\ts_waitcnt lgkmcnt(0)\n\tv_mfma_f32_16x16x32_bf16 v[32:35], %[x15], v[48:51], v[32:35]\n\ts_nop 7\n\ts_nop 1" : "=&{v[32:35]}"(acc) : [wp] "v"(wp), [x0] "v"(xf[0]), [x1] "v"(xf[1]), [x2] "v"(xf[2]), [x3] "v"(xf[3]), [x4] "v"(xf[4]), [x5] "v"(xf[5]), [x6] "v"(xf[6]), [x7] "v"(xf[7]), [x8] "v"(xf[8]), [x9] "v"(xf[9]), [x10] "v"(xf[10]), [x11] "v"(xf[11]), [x12] "v"(xf[12]), [x13] "v"(xf[13]), [x14] "v"(xf[14]), [x15] "v"(xf[15]) : "v36", "v37", "v38", "v39", "v40", "v41", "v42", "v43", "v44", "v45", "v46", "v47", "v48", "v49", "v50", "v51", "v52", "v53", "v54", "v55", "v56", "v57", "v58", "v59", "memory");
        unsigned long long pv = (unsigned long long)pk2(acc[0], acc[1]) | ((unsigned long long)pk2(acc[2], acc[3]) << 32);
        unsigned dst;
        if (type == 0) dst = WS_KF + (tb * 2 + (tg >> 1)) * 1024 + ((tg & 1) * 16 + l15 + 32 * (g4 & 1)) * 16 + (g4 >> 1) * 8;
        else           dst = WS_VF + (tb * 4 + tg) * 512 + (g4 >> 1) * 256 + l15 * 16 + (g4 & 1) * 8;
        __hip_atomic_store((GAS unsigned long long*)(ws + dst), pv, RLX_AGENT);
        lds_signal(L, L_C5, l);
        WAIT_VM0();
        ST(2);
        const int kt = tb * 2 + (tg >> 1);
        const int p = (kt & 3) * 64 + (kt >> 2) * 4 + (tg & 1) * 2 + type;
        if (l < 8) __hip_atomic_store((GAS unsigned*)(ws + WS_F1 + l * 1024 + p * 4), tag, RLX_AGENT);
    } else if (w == 5) {
        const int type = hg & 1, tg = hg >> 1;
        const int wrow = 16 * (sg >> 1) + 8 * sjh + 4 * (sg & 1) + sq;
        const char* wsrc = (const char*)(wo + (size_t)(64 * (b & 7)) * 512 + 16 * (b >> 3));
        const unsigned wovo = wrow * 1024 + (l & 1) * 16;
        dma16(wovo, wsrc, LB + L_WOST);
        dma16(wovo, wsrc + 32 * 1024, LB + L_WOST + 1024);
        u32x4 af[16];
        if (type == 0) {
            const u16* ap = ((l & 1) ? gate_w : w_r) + 8 * g4;
            #pragma unroll
            for (int s = 0; s < 16; ++s) af[s] = *(const GAS u32x4*)(ap + 32 * s);
        }
        asm volatile("s_barrier" ::: "memory");
        {
            u32x4 sent = {0xFFFFFFFFu, 0xFFFFFFFFu, 0xFFFFFFFFu, 0xFFFFFFFFu};
            #pragma unroll
            for (int i = 0; i < 8; ++i)
                __builtin_amdgcn_raw_buffer_store_b128(sent, rs, l * 16, WS_OF + ((tb * 2 + (i >> 2)) * 32 + 4 * hg + (i & 3)) * 1024, 16);
        }
        WAIT_VM0();
        #pragma unroll
        for (int i = 0; i < 2; ++i) {
            u32x4 f = tr_frag(L + L_WOST + i * 1024 + 8 * l);
            __builtin_amdgcn_raw_buffer_store_b128(f, rs, l * 16, WS_WOF + (2 * b + i) * 1024, 16);
        }
        lds_signal(L, L_C5, l);
        tag = get_tag(L);
        unsigned key = 0;
        if (type == 0) {
            lds_wait_ge(L, L_C2, 4u);
            f32x4 acc = {0.f, 0.f, 0.f, 0.f};
            LAS char* xp = L + L_XT + (tg * 16 + l15) * 1040 + g4 * 16;
            u32x4 bx[16];
            #pragma unroll
            for (int s = 0; s < 16; ++s) bx[s] = *(LAS u32x4*)(xp + s * 64);
            __builtin_amdgcn_sched_barrier(0);
            #pragma unroll
            for (int s = 0; s < 16; ++s) acc = mfma16(af[s], bx[s], acc);
            const int tok = tb * 64 + tg * 16 + l15;
            float gt = bfr(sigmoidf_(bfr(acc[1])));
            if (l < 16) __hip_atomic_store((GAS unsigned*)(ws + WS_GATE) + tok, __builtin_bit_cast(unsigned, gt), RLX_AGENT);
            unsigned u = pk2(acc[0], acc[0]) & 0xffffu;
            unsigned ord = (u & 0x8000u) ? (~u & 0xffffu) : (u | 0x8000u);
            if ((u & 0x7fffu) > 0x7f80u) ord = 0u;
            key = (l < 16) ? ((ord << 16) | (unsigned)(2047 - tok)) : 0u;
            #pragma unroll
            for (int o = 1; o < 16; o <<= 1) { unsigned k2 = __shfl_xor(key, o); key = key > k2 ? key : k2; }
            key = __builtin_amdgcn_readfirstlane(key);
        }
        WAIT_VM0();
        if (l < 8) __hip_atomic_store((GAS unsigned long long*)(ws + WS_F2 + l * 2048 + b * 8), ((unsigned long long)tag << 32) | key, RLX_AGENT);
        ST(1);
    } else {
        if (w == 0 && l < 16) *(LAS unsigned*)(L + L_MISC + 4 * l) = 0u;
        BARRIER();
        if (w < 4) {
            #pragma unroll 4
            for (int i = 0; i < 16; ++i) {
                const int row = 16 * w + i;
                dma16(l * 16, (const char*)x + (size_t)(tb * 64 + row) * 1024, LB + L_XT + row * 1040);
            }
            const unsigned qvo = (l >> 3) * 1024 + (((l & 7) ^ (((l >> 4) & 1) << 2)) * 16);
            #pragma unroll 4
            for (int i = 0; i < 16; ++i) {
                const int blk = 16 * w + i;
                dma16(qvo, (const char*)wq + (size_t)blk * 8192 + hg * 128, LB + L_WQ + blk * 1024);
            }
            WAIT_VM0();
            ST(1);
            lds_signal(L, L_C2, l);
            lds_wait_ge(L, L_C2, 4u);
            ST(2);
            const int cp = w & 1, th = w >> 1, h = l >> 5, nhi = g4 & 1, q = (l >> 2) & 3, p = l & 3;
            const unsigned ap = LB + L_WQ + h * 1024 + q * 128 + ((64 * cp + 32 * nhi + 8 * p) ^ (((q >> 1) & 1) << 6));
            const unsigned bp = LB + L_XT + (32 * th + (l & 31)) * 1040 + h * 16;
            f32x16 acc;
            asm volatile("ds_read_b64_tr_b16 v[100:101], %[ap] offset:0\n\tds_read_b64_tr_b16 v[102:103], %[ap] offset:512\n\tds_read_b128 v[104:107], %[bp] offset:0\n\tds_read_b64_tr_b16 v[108:109], %[ap] offset:2048\n\tds_read_b64_tr_b16 v[110:111], %[ap] offset:2560\n\tds_read_b128 v[112:115], %[bp] offset:32\n\tds_read_b64_tr_b16 v[116:117], %[ap] offset:4096\n\tds_read_b64_tr_b16 v[118:119], %[ap] offset:4608\n\tds_read_b128 v[120:123], %[bp] offset:64\n\tds_read_b64_tr_b16 v[124:125], %[ap] offset:6144\n\tds_read_b64_tr_b16 v[126:127], %[ap] offset:6656\n\tds_read_b128 v[128:131], %[bp] offset:96\n\ts_waitcnt lgkmcnt(9)\n\tv_mfma_f32_32x32x16_bf16 v[32:47], v[100:103], v[104:107], 0\n\tds_read_b64_tr_b16 v[100:101], %[ap] offset:8192\n\tds_read_b64_tr_b16 v[102:103], %[ap] offset:8704\n\tds_read_b128 v[104:107], %[bp] offset:128\n\ts_waitcnt lgkmcnt(9)\n\tv_mfma_f32_32x32x16_bf16 v[32:47], v[108:111], v[112:115], v[32:47]\n\tds_read_b64_tr_b16 v[108:109], %[ap] offset:10240\n\tds_read_b64_tr_b16 v[110:111], %[ap] offset:10752\n\tds_read_b128 v[112:115], %[bp] offset:160\n\ts_waitcnt lgkmcnt(9)\n\tv_mfma_f32_32x32x16_bf16 v[32:47], v[116:119], v[120:123], v[32:47]\n\tds_read_b64_tr_b16 v[116:117], %[ap] offset:12288\n\tds_read_b64_tr_b16 v[118:119], %[ap] offset:12800\n\tds_read_b128 v[120:123], %[bp] offset:192\n\ts_waitcnt lgkmcnt(9)\n\tv_mfma_f32_32x32x16_bf16 v[32:47], v[124:127], v[128:131], v[32:47]\n\tds_read_b64_tr_b16 v[124:125], %[ap] offset:14336\n\tds_read_b64_tr_b16 v[126:127], %[ap] offset:14848\n\tds_read_b128 v[128:131], %[bp] offset:224\n\ts_waitcnt lgkmcnt(9)\n\tv_mfma_f32_32x32x16_bf16 v[32:47], v[100:103], v[104:107], v[32:47]\n\tds_read_b64_tr_b16 v[100:101], %[ap] offset:16384\n\tds_read_b64_tr_b16 v[102:103], %[ap] offset:16896\n\tds_read_b128 v[104:107], %[bp] offset:256\n\ts_waitcnt lgkmcnt(9)\n\tv_mfma_f32_32x32x16_bf16 v[32:47], v[108:111], v[112:115], v[32:47]\n\tds_read_b64_tr_b16 v[108:109], %[ap] offset:18432\n\tds_read_b64_tr_b16 v[110:111], %[ap] offset:18944\n\tds_read_b128 v[112:115], %[bp] offset:288\n\ts_waitcnt lgkmcnt(9)\n\tv_mfma_f32_32x32x16_bf16 v[32:47], v[116:119], v[120:123], v[32:47]\n\tds_read_b64_tr_b16 v[116:117], %[ap] offset:20480\n\tds_read_b64_tr_b16 v[118:119], %[ap] offset:20992\n\tds_read_b128 v[120:123], %[bp] offset:320\n\ts_waitcnt lgkmcnt(9)\n\tv_mfma_f32_32x32x16_bf16 v[32:47], v[124:127], v[128:131], v[32:47]\n\tds_read_b64_tr_b16 v[124:125], %[ap] offset:22528\n\tds_read_b64_tr_b16 v[126:127], %[ap] offset:23040\n\tds_read_b128 v[128:131], %[bp] offset:352\n\ts_waitcnt lgkmcnt(9)\n\tv_mfma_f32_32x32x16_bf16 v[32:47], v[100:103], v[104:107], v[32:47]\n\tds_read_b64_tr_b16 v[100:101], %[ap] offset:24576\n\tds_read_b64_tr_b16 v[102:103], %[ap] offset:25088\n\tds_read_b128 v[104:107], %[bp] offset:384\n\ts_waitcnt lgkmcnt(9)\n\tv_mfma_f32_32x32x16_bf16 v[32:47], v[108:111], v[112:115], v[32:47]\n\tds_read_b64_tr_b16 v[108:109], %[ap] offset:26624\n\tds_read_b64_tr_b16 v[110:111], %[ap] offset:27136\n\tds_read_b128 v[112:115], %[bp] offset:416\n\ts_waitcnt lgkmcnt(9)\n\tv_mfma_f32_32x32x16_bf16 v[32:47], v[116:119], v[120:123], v[32:47]\n\tds_read_b64_tr_b16 v[116:117], %[ap] offset:28672\n\tds_read_b64_tr_b16 v[118:119], %[ap] offset:29184\n\tds_read_b128 v[120:123], %[bp] offset:448\n\ts_waitcnt lgkmcnt(9)\n\tv_mfma_f32_32x32x16_bf16 v[32:47], v[124:127], v[128:131], v[32:47]\n\tds_read_b64_tr_b16 v[124:125], %[ap] offset:30720\n\tds_read_b64_tr_b16 v[126:127], %[ap] offset:31232\n\tds_read_b128 v[128:131], %[bp] offset:480\n\ts_waitcnt lgkmcnt(9)\n\tv_mfma_f32_32x32x16_bf16 v[32:47], v[100:103], v[104:107], v[32:47]\n\tds_read_b64_tr_b16 v[100:101], %[ap] offset:32768\n\tds_read_b64_tr_b16 v[102:103], %[ap] offset:33280\n\tds_read_b128 v[104:107], %[bp] offset:512\n\ts_waitcnt lgkmcnt(9)\n\tv_mfma_f32_32x32x16_bf16 v[32:47], v[108:111], v[112:115], v[32:47]\n\tds_read_b64_tr_b16 v[108:109], %[ap] offset:34816\n\tds_read_b64_tr_b16 v[110:111], %[ap] offset:35328\n\tds_read_b128 v[112:115], %[bp] offset:544\n\ts_waitcnt lgkmcnt(9)\n\tv_mfma_f32_32x32x16_bf16 v[32:47], v[116:119], v[120:123], v[32:47]\n\tds_read_b64_tr_b16 v[116:117], %[ap] offset:36864\n\tds_read_b64_tr_b16 v[118:119], %[ap] offset:37376\n\tds_read_b128 v[120:123], %[bp] offset:576\n\ts_waitcnt lgkmcnt(9)\n\tv_mfma_f32_32x32x16_bf16 v[32:47], v[124:127], v[128:131], v[32:47]\n\tds_read_b64_tr_b16 v[124:125], %[ap] offset:38912\n\tds_read_b64_tr_b16 v[126:127], %[ap] offset:39424\n\tds_read_b128 v[128:131], %[bp] offset:608\n\ts_waitcnt lgkmcnt(9)\n\tv_mfma_f32_32x32x16_bf16 v[32:47], v[100:103], v[104:107], v[32:47]\n\tds_read_b64_tr_b16 v[100:101], %[ap] offset:40960\n\tds_read_b64_tr_b16 v[102:103], %[ap] offset:41472\n\tds_read_b128 v[104:107], %[bp] offset:640\n\ts_waitcnt lgkmcnt(9)\n\tv_mfma_f32_32x32x16_bf16 v[32:47], v[108:111], v[112:115], v[32:47]\n\tds_read_b64_tr_b16 v[108:109], %[ap] offset:43008\n\tds_read_b64_tr_b16 v[110:111], %[ap] offset:43520\n\tds_read_b128 v[112:115], %[bp] offset:672\n\ts_waitcnt lgkmcnt(9)\n\tv_mfma_f32_32x32x16_bf16 v[32:47], v[116:119], v[120:123], v[32:47]\n\tds_read_b64_tr_b16 v[116:117], %[ap] offset:45056\n\tds_read_b64_tr_b16 v[118:119], %[ap] offset:45568\n\tds_read_b128 v[120:123], %[bp] offset:704\n\ts_waitcnt lgkmcnt(9)\n\tv_mfma_f32_32x32x16_bf16 v[32:47], v[124:127], v[128:131], v[32:47]\n\tds_read_b64_tr_b16 v[124:125], %[ap] offset:47104\n\tds_read_b64_tr_b16 v[126:127], %[ap] offset:47616\n\tds_read_b128 v[128:131], %[bp] offset:736\n\ts_waitcnt lgkmcnt(9)\n\tv_mfma_f32_32x32x16_bf16 v[32:47], v[100:103], v[104:107], v[32:47]\n\tds_read_b64_tr_b16 v[100:101], %[ap] offset:49152\n\tds_read_b64_tr_b16 v[102:103], %[ap] offset:49664\n\tds_read_b128 v[104:107], %[bp] offset:768\n\ts_waitcnt lgkmcnt(9)\n\tv_mfma_f32_32x32x16_bf16 v[32:47], v[108:111], v[112:115], v[32:47]\n\tds_read_b64_tr_b16 v[108:109], %[ap] offset:51200\n\tds_read_b64_tr_b16 v[110:111], %[ap] offset:51712\n\tds_read_b128 v[112:115], %[bp] offset:800\n\ts_waitcnt lgkmcnt(9)\n\tv_mfma_f32_32x32x16_bf16 v[32:47], v[116:119], v[120:123], v[32:47]\n\tds_read_b64_tr_b16 v[116:117], %[ap] offset:53248\n\tds_read_b64_tr_b16 v[118:119], %[ap] offset:53760\n\tds_read_b128 v[120:123], %[bp] offset:832\n\ts_waitcnt lgkmcnt(9)\n\tv_mfma_f32_32x32x16_bf16 v[32:47], v[124:127], v[128:131], v[32:47]\n\tds_read_b64_tr_b16 v[124:125], %[ap] offset:55296\n\tds_read_b64_tr_b16 v[126:127], %[ap] offset:55808\n\tds_read_b128 v[128:131], %[bp] offset:864\n\ts_waitcnt lgkmcnt(9)\n\tv_mfma_f32_32x32x16_bf16 v[32:47], v[100:103], v[104:107], v[32:47]\n\tds_read_b64_tr_b16 v[100:101], %[ap] offset:57344\n\tds_read_b64_tr_b16 v[102:103], %[ap] offset:57856\n\tds_read_b128 v[104:107], %[bp] offset:896\n\ts_waitcnt lgkmcnt(9)\n\tv_mfma_f32_32x32x16_bf16 v[32:47], v[108:111], v[112:115], v[32:47]\n\tds_read_b64_tr_b16 v[108:109], %[ap] offset:59392\n\tds_read_b64_tr_b16 v[110:111], %[ap] offset:59904\n\tds_read_b128 v[112:115], %[bp] offset:928\n\ts_waitcnt lgkmcnt(9)\n\tv_mfma_f32_32x32x16_bf16 v[32:47], v[116:119], v[120:123], v[32:47]\n\tds_read_b64_tr_b16 v[116:117], %[ap] offset:61440\n\tds_read_b64_tr_b16 v[118:119], %[ap] offset:61952\n\tds_read_b128 v[120:123], %[bp] offset:960\n\ts_waitcnt lgkmcnt(9)\n\tv_mfma_f32_32x32x16_bf16 v[32:47], v[124:127], v[128:131], v[32:47]\n\tds_read_b64_tr_b16 v[124:125], %[ap] offset:63488\n\tds_read_b64_tr_b16 v[126:127], %[ap] offset:64000\n\tds_read_b128 v[128:131], %[bp] offset:992\n\ts_waitcnt lgkmcnt(9)\n\tv_mfma_f32_32x32x16_bf16 v[32:47], v[100:103], v[104:107], v[32:47]\n\ts_waitcnt lgkmcnt(6)\n\tv_mfma_f32_32x32x16_bf16 v[32:47], v[108:111], v[112:115], v[32:47]\n\ts_waitcnt lgkmcnt(3)\n\tv_mfma_f32_32x32x16_bf16 v[32:47], v[116:119], v[120:123], v[32:47]\n\ts_waitcnt lgkmcnt(0)\n\tv_mfma_f32_32x32x16_bf16 v[32:47], v[124:127], v[128:131], v[32:47]\n\ts_nop 15\n\ts_nop 3" : "=&{v[32:47]}"(acc) : [ap] "v"(ap), [bp] "v"(bp) : "v100", "v101", "v102", "v103", "v104", "v105", "v106", "v107", "v108", "v109", "v110", "v111", "v112", "v113", "v114", "v115", "v116", "v117", "v118", "v119", "v120", "v121", "v122", "v123", "v124", "v125", "v126", "v127", "v128", "v129", "v130", "v131", "memory");
            const float sc = 0.25f * 1.4426950408889634f;
            u32x4 qo;
            #pragma unroll
            for (int i = 0; i < 4; ++i) { qf[i] = pk2(acc[2 * i] * sc, acc[2 * i + 1] * sc); qo[i] = pk2(acc[8 + 2 * i] * sc, acc[9 + 2 * i] * sc); }
            *(LAS u32x4*)(L + L_QX + w * 1024 + l * 16) = qo;
            ST(3);
        }
    }

    const int rep = b & 7;
    if (w >= 4) {
        const int bw = w - 4;
        if (w >= 6) tag = get_tag(L);
        {
            GAS unsigned* fp = (GAS unsigned*)(ws + WS_F1 + rep * 1024) + bw * 64 + l;
            _Pragma("nounroll") for (int i = 0; i < SPIN_CAP; ++i) {
                unsigned v = __hip_atomic_load(fp, RLX_AGENT);
                if (__all(v == tag)) break;
                __builtin_amdgcn_s_sleep(1);
            }
            FENCE();
        }
        ST(4);
        u32x4 kf[16], vf[16];
        #pragma unroll
        for (int i = 0; i < 16; ++i) {
            kf[i] = __builtin_amdgcn_raw_buffer_load_b128(rs, l * 16, WS_KF + (4 * i + bw) * 1024, 16);
            vf[i] = __builtin_amdgcn_raw_buffer_load_b128(rs, l * 16, WS_VF + (4 * i + bw) * 1024, 16);
        }
        lds_wait_ge(L, L_C5, 2u);
        LAS char* kd = L + bw * REC + l * 16;
        LAS char* vd = L + bw * REC + 1024 + (l >> 5) * 528 + (l & 31) * 16;
        const u32x4 ones = {0x3f803f80u, 0x3f803f80u, 0x3f803f80u, 0x3f803f80u};
        LAS char* od = L + ((l >> 1) * 4 + bw) * REC + 1024 + (l & 1) * 528 + 512;
        #pragma unroll
        for (int i = 0; i < 3; ++i) { *(LAS u32x4*)(kd + i * 4 * REC) = kf[i]; *(LAS u32x4*)(vd + i * 4 * REC) = vf[i]; }
        if (l < 6) *(LAS u32x4*)od = ones;
        ST(5);
        BARRIER();
        ST(6); AP(6);
        qf = *(LAS u32x4*)(L + L_QX + bw * 1024 + l * 16);
        #pragma unroll
        for (int i = 3; i < 16; ++i) { *(LAS u32x4*)(kd + i * 4 * REC) = kf[i]; *(LAS u32x4*)(vd + i * 4 * REC) = vf[i]; }
        if (l >= 6 && l < 32) *(LAS u32x4*)od = ones;
        lds_signal(L, L_C4, l);
        if (b == 0 && w == 4 && l == 0) __hip_atomic_store((GAS unsigned*)(ws + WS_EPOCH), tag, RLX_AGENT);
    } else {
        BARRIER();
        ST(6); AP(6);
        tag = get_tag(L);
    }

    const int ft = xcd * 28 + j;
    if (j < 28) {
        const u16* wsrc = ((l & 1) ? w_up : w_gate) + (size_t)krow * 1792 + 8 * ft;
        #pragma unroll
        for (int i = 0; i < 2; ++i) { const int s = 2 * w + i; dma16p(wsrc + (size_t)s * 32 * 1792, LB + L_FW1 + s * 1024); }
    }
    if (w < 7) dma16(krow * 1024 + (l & 1) * 16, (const char*)(w_down + (size_t)(224 * xcd + 32 * w) * 512 + 16 * j), LB + L_FW2 + w * 1024);

    const int hl = 2 * (w & 1) + (w >> 2), tha = (w >> 1) & 1;
    unsigned kp = LB + l * 16;
    unsigned vp = LB + 1024 + ((l & 31) < 16 ? (l >> 5) * 256 + (l & 31) * 16 : 512);
    const int idxv = (int)0xDDDDDDDD;
    f32x16 X, Y, acc; u32x4 KR, V0, V1;
    for (int i = 0; i < 16; ++i) acc[i] = 0.f;
    if (w >= 4) __builtin_amdgcn_s_setprio(1);
    ST(7); STC(30); AP(7);
    asm volatile("s_branch MQA_OVER\n\tMQA_BODY:\n\tv_exp_f32 v32, v32\n\tv_exp_f32 v33, v33\n\ts_waitcnt lgkmcnt(2)\n\tv_smfmac_f32_32x32x32_bf16 v[64:79], v[88:91], v[56:63], v96\n\tds_read_b128 v[88:91], v98 offset:528\n\tv_exp_f32 v34, v34\n\tv_exp_f32 v35, v35\n\tv_exp_f32 v36, v36\n\tv_exp_f32 v37, v37\n\ts_waitcnt lgkmcnt(2)\n\tv_mfma_f32_32x32x16_bf16 v[48:63], v[80:83], v[92:95], 0\n\tds_read_b128 v[80:83], v97 offset:4160\n\tv_exp_f32 v38, v38\n\tv_exp_f32 v39, v39\n\tv_exp_f32 v40, v40\n\tv_exp_f32 v41, v41\n\ts_waitcnt lgkmcnt(2)\n\tv_smfmac_f32_32x32x32_bf16 v[64:79], v[84:87], v[32:39], v96\n\tds_read_b128 v[84:87], v98 offset:2080\n\tv_exp_f32 v42, v42\n\tv_exp_f32 v43, v43\n\tv_exp_f32 v44, v44\n\tv_exp_f32 v45, v45\n\tv_exp_f32 v46, v46\n\tv_exp_f32 v47, v47\n\tv_exp_f32 v48, v48\n\tv_exp_f32 v49, v49\n\ts_waitcnt lgkmcnt(2)\n\tv_smfmac_f32_32x32x32_bf16 v[64:79], v[88:91], v[40:47], v96\n\tds_read_b128 v[88:91], v98 offset:2608\n\tv_exp_f32 v50, v50\n\tv_exp_f32 v51, v51\n\tv_exp_f32 v52, v52\n\tv_exp_f32 v53, v53\n\ts_waitcnt lgkmcnt(2)\n\tv_mfma_f32_32x32x16_bf16 v[32:47], v[80:83], v[92:95], 0\n\tds_read_b128 v[80:83], v97 offset:6240\n\tv_exp_f32 v54, v54\n\tv_exp_f32 v55, v55\n\tv_exp_f32 v56, v56\n\tv_exp_f32 v57, v57\n\ts_waitcnt lgkmcnt(2)\n\tv_smfmac_f32_32x32x32_bf16 v[64:79], v[84:87], v[48:55], v96\n\tds_read_b128 v[84:87], v98 offset:4160\n\tv_exp_f32 v58, v58\n\tv_exp_f32 v59, v59\n\tv_exp_f32 v60, v60\n\tv_exp_f32 v61, v61\n\tv_exp_f32 v62, v62\n\tv_exp_f32 v63, v63\n\tv_exp_f32 v32, v32\n\tv_exp_f32 v33, v33\n\ts_waitcnt lgkmcnt(2)\n\tv_smfmac_f32_32x32x32_bf16 v[64:79], v[88:91], v[56:63], v96\n\tds_read_b128 v[88:91], v98 offset:4688\n\tv_exp_f32 v34, v34\n\tv_exp_f32 v35, v35\n\tv_exp_f32 v36, v36\n\tv_exp_f32 v37, v37\n\ts_waitcnt lgkmcnt(2)\n\tv_mfma_f32_32x32x16_bf16 v[48:63], v[80:83], v[92:95], 0\n\tds_read_b128 v[80:83], v97 offset:8320\n\tv_exp_f32 v38, v38\n\tv_exp_f32 v39, v39\n\tv_exp_f32 v40, v40\n\tv_exp_f32 v41, v41\n\ts_waitcnt lgkmcnt(2)\n\tv_smfmac_f32_32x32x32_bf16 v[64:79], v[84:87], v[32:39], v96\n\tds_read_b128 v[84:87], v98 offset:6240\n\tv_exp_f32 v42, v42\n\tv_exp_f32 v43, v43\n\tv_exp_f32 v44, v44\n\tv_exp_f32 v45, v45\n\tv_exp_f32 v46, v46\n\tv_exp_f32 v47, v47\n\tv_exp_f32 v48, v48\n\tv_exp_f32 v49, v49\n\ts_waitcnt lgkmcnt(2)\n\tv_smfmac_f32_32x32x32_bf16 v[64:79], v[88:91], v[40:47], v96\n\tds_read_b128 v[88:91], v98 offset:6768\n\tv_exp_f32 v50, v50\n\tv_exp_f32 v51, v51\n\tv_exp_f32 v52, v52\n\tv_exp_f32 v53, v53\n\ts_waitcnt lgkmcnt(2)\n\tv_mfma_f32_32x32x16_bf16 v[32:47], v[80:83], v[92:95], 0\n\tds_read_b128 v[80:83], v97 offset:10400\n\tv_exp_f32 v54, v54\n\tv_exp_f32 v55, v55\n\tv_exp_f32 v56, v56\n\tv_exp_f32 v57, v57\n\ts_waitcnt lgkmcnt(2)\n\tv_smfmac_f32_32x32x32_bf16 v[64:79], v[84:87], v[48:55], v96\n\tds_read_b128 v[84:87], v98 offset:8320\n\tv_exp_f32 v58, v58\n\tv_exp_f32 v59, v59\n\tv_exp_f32 v60, v60\n\tv_exp_f32 v61, v61\n\tv_exp_f32 v62, v62\n\tv_exp_f32 v63, v63\n\tv_exp_f32 v32, v32\n\tv_exp_f32 v33, v33\n\ts_waitcnt lgkmcnt(2)\n\tv_smfmac_f32_32x32x32_bf16 v[64:79], v[88:91], v[56:63], v96\n\tds_read_b128 v[88:91], v98 offset:8848\n\tv_exp_f32 v34, v34\n\tv_exp_f32 v35, v35\n\tv_exp_f32 v36, v36\n\tv_exp_f32 v37, v37\n\ts_waitcnt lgkmcnt(2)\n\tv_mfma_f32_32x32x16_bf16 v[48:63], v[80:83], v[92:95], 0\n\tds_read_b128 v[80:83], v97 offset:12480\n\tv_exp_f32 v38, v38\n\tv_exp_f32 v39, v39\n\tv_exp_f32 v40, v40\n\tv_exp_f32 v41, v41\n\ts_waitcnt lgkmcnt(2)\n\tv_smfmac_f32_32x32x32_bf16 v[64:79], v[84:87], v[32:39], v96\n\tds_read_b128 v[84:87], v98 offset:10400\n\tv_exp_f32 v42, v42\n\tv_exp_f32 v43, v43\n\tv_exp_f32 v44, v44\n\tv_exp_f32 v45, v45\n\tv_exp_f32 v46, v46\n\tv_exp_f32 v47, v47\n\tv_exp_f32 v48, v48\n\tv_exp_f32 v49, v49\n\ts_waitcnt lgkmcnt(2)\n\tv_smfmac_f32_32x32x32_bf16 v[64:79], v[88:91], v[40:47], v96\n\tds_read_b128 v[88:91], v98 offset:10928\n\tv_exp_f32 v50, v50\n\tv_exp_f32 v51, v51\n\tv_exp_f32 v52, v52\n\tv_exp_f32 v53, v53\n\ts_waitcnt lgkmcnt(2)\n\tv_mfma_f32_32x32x16_bf16 v[32:47], v[80:83], v[92:95], 0\n\tds_read_b128 v[80:83], v97 offset:14560\n\tv_exp_f32 v54, v54\n\tv_exp_f32 v55, v55\n\tv_exp_f32 v56, v56\n\tv_exp_f32 v57, v57\n\ts_waitcnt lgkmcnt(2)\n\tv_smfmac_f32_32x32x32_bf16 v[64:79], v[84:87], v[48:55], v96\n\tds_read_b128 v[84:87], v98 offset:12480\n\tv_exp_f32 v58, v58\n\tv_exp_f32 v59, v59\n\tv_exp_f32 v60, v60\n\tv_exp_f32 v61, v61\n\tv_exp_f32 v62, v62\n\tv_exp_f32 v63, v63\n\tv_exp_f32 v32, v32\n\tv_exp_f32 v33, v33\n\ts_waitcnt lgkmcnt(2)\n\tv_smfmac_f32_32x32x32_bf16 v[64:79], v[88:91], v[56:63], v96\n\tds_read_b128 v[88:91], v98 offset:13008\n\tv_exp_f32 v34, v34\n\tv_exp_f32 v35, v35\n\tv_exp_f32 v36, v36\n\tv_exp_f32 v37, v37\n\ts_waitcnt lgkmcnt(2)\n\tv_mfma_f32_32x32x16_bf16 v[48:63], v[80:83], v[92:95], 0\n\tds_read_b128 v[80:83], v97 offset:16640\n\tv_exp_f32 v38, v38\n\tv_exp_f32 v39, v39\n\tv_exp_f32 v40, v40\n\tv_exp_f32 v41, v41\n\ts_waitcnt lgkmcnt(2)\n\tv_smfmac_f32_32x32x32_bf16 v[64:79], v[84:87], v[32:39], v96\n\tds_read_b128 v[84:87], v98 offset:14560\n\tv_exp_f32 v42, v42\n\tv_exp_f32 v43, v43\n\tv_exp_f32 v44, v44\n\tv_exp_f32 v45, v45\n\tv_exp_f32 v46, v46\n\tv_exp_f32 v47, v47\n\tv_exp_f32 v48, v48\n\tv_exp_f32 v49, v49\n\ts_waitcnt lgkmcnt(2)\n\tv_smfmac_f32_32x32x32_bf16 v[64:79], v[88:91], v[40:47], v96\n\tds_read_b128 v[88:91], v98 offset:15088\n\tv_exp_f32 v50, v50\n\tv_exp_f32 v51, v51\n\tv_exp_f32 v52, v52\n\tv_exp_f32 v53, v53\n\ts_waitcnt lgkmcnt(2)\n\tv_mfma_f32_32x32x16_bf16 v[32:47], v[80:83], v[92:95], 0\n\tds_read_b128 v[80:83], v97 offset:18720\n\tv_exp_f32 v54, v54\n\tv_exp_f32 v55, v55\n\tv_exp_f32 v56, v56\n\tv_exp_f32 v57, v57\n\ts_waitcnt lgkmcnt(2)\n\tv_smfmac_f32_32x32x32_bf16 v[64:79], v[84:87], v[48:55], v96\n\tds_read_b128 v[84:87], v98 offset:16640\n\tv_exp_f32 v58, v58\n\tv_exp_f32 v59, v59\n\tv_exp_f32 v60, v60\n\tv_exp_f32 v61, v61\n\tv_exp_f32 v62, v62\n\tv_exp_f32 v63, v63\n\tv_add_u32 v97, 16640, v97\n\tv_add_u32 v98, 16640, v98\n\ts_sub_u32 s78, s78, 1\n\ts_cmp_lg_u32 s78, 0\n\ts_cbranch_scc1 MQA_BODY\n\ts_waitcnt lgkmcnt(0)\n\ts_nop 7\n\ts_setpc_b64 s[76:77]\n\tMQA_OVER:\n\tds_read_b128 v[80:83], v97 offset:0\n\tds_read_b128 v[84:87], v98 offset:0\n\ts_waitcnt lgkmcnt(1)\n\tv_mfma_f32_32x32x16_bf16 v[32:47], v[80:83], v[92:95], 0\n\tds_read_b128 v[80:83], v97 offset:2080\n\tds_read_b128 v[88:91], v98 offset:528\n\tv_mov_b32 v56, 0\n\tv_mov_b32 v57, 0\n\tv_mov_b32 v58, 0\n\tv_mov_b32 v59, 0\n\tv_mov_b32 v60, 0\n\tv_mov_b32 v61, 0\n\tv_mov_b32 v62, 0\n\tv_mov_b32 v63, 0\n\tv_mov_b32 v48, 0\n\tv_mov_b32 v49, 0\n\tv_mov_b32 v50, 0\n\tv_mov_b32 v51, 0\n\tv_mov_b32 v52, 0\n\tv_mov_b32 v53, 0\n\tv_mov_b32 v54, 0\n\tv_mov_b32 v55, 0\n\ts_nop 7\n\ts_nop 3\n\ts_waitcnt lgkmcnt(0)"
                 : "={v[32:47]}"(X), "={v[48:63]}"(Y), "={v[80:83]}"(KR), "={v[84:87]}"(V0), "={v[88:91]}"(V1)
                 : "{v[92:95]}"(qf), "{v97}"(kp), "{v98}"(vp) : "memory");
#define SEG(N) asm volatile("s_mov_b32 s78, %[n]\n\ts_getpc_b64 s[76:77]\n\ts_add_u32 s76, s76, 12\n\ts_addc_u32 s77, s77, 0\n\ts_branch MQA_BODY" \
                     : "+{v[32:47]}"(X), "+{v[48:63]}"(Y), "+{v[64:79]}"(acc), "+{v[80:83]}"(KR), "+{v[84:87]}"(V0), "+{v[88:91]}"(V1), \
                       "+{v97}"(kp), "+{v98}"(vp) \
                     : [n] "n"(N), "{v[92:95]}"(qf), "{v96}"(idxv) : "s76", "s77", "s78", "scc", "memory")
    const int tho = hg >> 2, cg = hg & 3;
    const int n0 = cg * 128 + 16 * w;
    u32x4 fl0, fl1;
    if (w == 0) {
        fl0 = __builtin_amdgcn_raw_buffer_load_b128(rs, l * 16, WS_F2 + rep * 2048, 16);
        fl1 = __builtin_amdgcn_raw_buffer_load_b128(rs, l * 16, WS_F2 + rep * 2048 + 1024, 16);
    }
    SEG(1);
    ST(12);
    WAIT_VM0();
    lds_signal(L, L_C7, l);
    if (w == 0) {
        _Pragma("nounroll") for (int i = 0; i < SPIN_CAP; ++i) {
            if (__all(fl0[1] == tag && fl0[3] == tag && fl1[1] == tag && fl1[3] == tag)) break;
            FENCE();
            fl0 = __builtin_amdgcn_raw_buffer_load_b128(rs, l * 16, WS_F2 + rep * 2048, 16);
            fl1 = __builtin_amdgcn_raw_buffer_load_b128(rs, l * 16, WS_F2 + rep * 2048 + 1024, 16);
        }
        unsigned key = max(max(fl0[0], fl0[2]), max(fl1[0], fl1[2]));
        #pragma unroll
        for (int o = 1; o < 64; o <<= 1) { unsigned k2 = __shfl_xor(key, o); key = key > k2 ? key : k2; }
        key = __builtin_amdgcn_readfirstlane(key);
        if (l == 0) *(LAS unsigned*)(L + L_KEY) = key;
        lds_signal(L, L_C6, l);
        const int idx = 2047 - (int)(key & 0x7ffu);
        dma16(l * 16, (const char*)x + (size_t)idx * 1024, LB + L_XSEL);
    }
    lds_wait_ge(L, L_C4, 4u);
    SEG(1);
    ST(13);
    if (w == 0 && j < 28) {
        WAIT_VM0();
        lds_wait_ge(L, L_C7, 8u);
        f32x4 a = {0.f, 0.f, 0.f, 0.f};
        u32x4 fa[8], fb[8];
        #pragma unroll
        for (int hh = 0; hh < 2; ++hh) {
            #pragma unroll
            for (int s = 0; s < 8; ++s) {
                fa[s] = tr_frag(L + L_FW1 + (8 * hh + s) * 1024 + 8 * l);
                fb[s] = *(LAS u32x4*)(L + L_XSEL + (8 * hh + s) * 64 + g4 * 16);
            }
            __builtin_amdgcn_sched_barrier(0);
            #pragma unroll
            for (int s = 0; s < 8; ++s) a = mfma16(fa[s], fb[s], a);
            __builtin_amdgcn_sched_barrier(0);
        }
        float hv[4];
        #pragma unroll
        for (int i = 0; i < 4; ++i) {
            float u = bfr(__shfl_xor(a[i], 32));
            float gq = bfr(a[i]);
            hv[i] = bfr(gq * sigmoidf_(gq)) * u;
        }
        u32x4 gr = {pk2(hv[0], hv[1]), tag, pk2(hv[2], hv[3]), tag};
        if (l == 0 || l == 16) __builtin_amdgcn_raw_buffer_store_b128(gr, rs, g4 * 16, WS_HG + ft * 32, 16);
    }
    SEG(1);
    ST(14);
    lds_wait_ge(L, L_C6, 1u);
    const unsigned rkey = __builtin_amdgcn_readfirstlane(*(LAS unsigned*)(L + L_KEY));
    if (w == 1) fl0 = __builtin_amdgcn_raw_buffer_load_b128(rs, (l < 56 ? l : 55) * 16, WS_HG + xcd * 896, 16);
    u32x4 wof[16];
    u32x2 xres[2];
    float gres[2];
    #pragma unroll
    for (int s = 0; s < 16; ++s)
        wof[s] = __builtin_amdgcn_raw_buffer_load_b128(rs, l * 16, WS_WOF + ((cg * 8 + w) * 16 + s) * 1024, 16);
    #pragma unroll
    for (int t = 0; t < 2; ++t) {
        const int tok = tb * 64 + tho * 32 + 16 * t + l15;
        xres[t] = *(const GAS u32x2*)(x + (size_t)tok * 512 + n0 + 4 * g4);
        gres[t] = u2f(__hip_atomic_load((GAS unsigned*)(ws + WS_GATE) + tok, RLX_AGENT));
    }
    SEG(1);
    ST(15);
    if (w == 1) {
        _Pragma("nounroll") for (int i = 0; i < SPIN_CAP; ++i) {
            if (__all(fl0[1] == tag && fl0[3] == tag)) break;
            FENCE();
            fl0 = __builtin_amdgcn_raw_buffer_load_b128(rs, (l < 56 ? l : 55) * 16, WS_HG + xcd * 896, 16);
        }
        u32x2 hv = {fl0[0], fl0[2]};
        if (l < 56) *(LAS u32x2*)(L + L_HBUF + l * 8) = hv;
        WAIT_LGKM0();
        f32x4 a = {0.f, 0.f, 0.f, 0.f};
        u32x4 fa[7], fb[7];
        #pragma unroll
        for (int s = 0; s < 7; ++s) {
            fa[s] = tr_frag(L + L_FW2 + s * 1024 + 8 * l);
            fb[s] = *(LAS u32x4*)(L + L_HBUF + s * 64 + g4 * 16);
        }
        __builtin_amdgcn_sched_barrier(0);
        #pragma unroll
        for (int s = 0; s < 7; ++s) a = mfma16(fa[s], fb[s], a);
        u32x4 g0 = {f2u(a[0]), tag, f2u(a[1]), tag};
        u32x4 g1 = {f2u(a[2]), tag, f2u(a[3]), tag};
        if (l15 == 0) {
            __builtin_amdgcn_raw_buffer_store_b128(g0, rs, g4 * 32, WS_YG + (xcd * 512 + 16 * j) * 8, 16);
            __builtin_amdgcn_raw_buffer_store_b128(g1, rs, g4 * 32 + 16, WS_YG + (xcd * 512 + 16 * j) * 8, 16);
        }
    }
    SEG(PRIO_N);
    ST(16);
    __builtin_amdgcn_s_setprio(0);
    SEG(4 - PRIO_N);
    asm volatile("s_nop 1\n\tv_smfmac_f32_32x32x32_bf16 v[64:79], v[88:91], v[56:63], v96\n\ts_nop 15\n\ts_nop 7" : "+{v[64:79]}"(acc) : "{v[48:63]}"(Y), "{v[88:91]}"(V1), "{v96}"(idxv) : "memory");
    __builtin_amdgcn_s_setprio(0);
    ST(8); STC(31); AP(8);

    {
        const float ls = acc[8];
        bool ok = ls >= 8.6736174e-19f && ls <= 1.2676506e30f;
        #pragma unroll
        for (int i = 0; i < 8; ++i) ok = ok && (__builtin_fabsf(acc[i]) < __builtin_inff());
        if (!__all(ok)) {
            LAS char* kq = L + l * 16;
            LAS char* vq = L + 1024 + ((l & 31) < 16 ? (l >> 5) * 256 + (l & 31) * 16 : 512);
            acc = attn_true_max(kq, vq, qf);
        }
    }
    {
        const float inv = 1.f / acc[8];
        u32x4 o;
        #pragma unroll
        for (int i = 0; i < 4; ++i) { unsigned u = pk2(acc[2 * i] * inv, acc[2 * i + 1] * inv); o[i] = u < 0xFFFFFFFEu ? u : 0xFFFFFFFEu; }
        __builtin_amdgcn_raw_buffer_store_b128(o, rs, l * 16, WS_OF + ((tb * 2 + tha) * 32 + 4 * hg + hl) * 1024, 16);
    }
    const int idx = 2047 - (int)(rkey & 0x7ffu);
    const bool has_idx = (idx >> 5) == tb * 2 + tho;
    u32x4 yg = {0u, tag, 0u, tag};
    const unsigned yoff = ((l >> 3) * 512 + n0 + 2 * (l & 7)) * 8;
    if (has_idx) yg = __builtin_amdgcn_raw_buffer_load_b128(rs, yoff, WS_YG, 16);
    BARRIER();
    ST(9); AP(9);
    {
        u32x4 f[4];
        const unsigned src = WS_OF + ((tb * 2 + tho) * 32 + 4 * w) * 1024;
        _Pragma("nounroll") for (int i = 0; i < SPIN_CAP; ++i) {
            bool ok = true;
            #pragma unroll
            for (int k = 0; k < 4; ++k) {
                f[k] = __builtin_amdgcn_raw_buffer_load_b128(rs, l * 16, src + k * 1024, 16);
                ok = ok && f[k][0] != 0xFFFFFFFFu && f[k][1] != 0xFFFFFFFFu && f[k][2] != 0xFFFFFFFFu && f[k][3] != 0xFFFFFFFFu;
            }
            if (__all(ok)) break;
            FENCE();
        }
        #pragma unroll
        for (int k = 0; k < 4; ++k) *(LAS u32x4*)(L + (4 * w + k) * 1024 + l * 16) = f[k];
    }
    ST(10); AP(10);
    BARRIER();
    f32x4 oa[2];
    {
        const unsigned op = LB + (g4 >> 1) * 1024 + (g4 & 1) * 512 + l15 * 16;
        asm volatile("ds_read_b128 v[40:43], %[op] offset:0\n\tds_read_b128 v[44:47], %[op] offset:256\n\tds_read_b128 v[48:51], %[op] offset:2048\n\tds_read_b128 v[52:55], %[op] offset:2304\n\tds_read_b128 v[56:59], %[op] offset:4096\n\tds_read_b128 v[60:63], %[op] offset:4352\n\tds_read_b128 v[64:67], %[op] offset:6144\n\tds_read_b128 v[68:71], %[op] offset:6400\n\tds_read_b128 v[72:75], %[op] offset:8192\n\tds_read_b128 v[76:79], %[op] offset:8448\n\tds_read_b128 v[80:83], %[op] offset:10240\n\tds_read_b128 v[84:87], %[op] offset:10496\n\ts_waitcnt lgkmcnt(11)\n\tv_mfma_f32_16x16x32_bf16 v[32:35], %[w0], v[40:43], 0\n\ts_waitcnt lgkmcnt(10)\n\tv_mfma_f32_16x16x32_bf16 v[36:39], %[w0], v[44:47], 0\n\tds_read_b128 v[40:43], %[op] offset:12288\n\tds_read_b128 v[44:47], %[op] offset:12544\n\ts_waitcnt lgkmcnt(11)\n\tv_mfma_f32_16x16x32_bf16 v[32:35], %[w1], v[48:51], v[32:35]\n\ts_waitcnt lgkmcnt(10)\n\tv_mfma_f32_16x16x32_bf16 v[36:39], %[w1], v[52:55], v[36:39]\n\tds_read_b128 v[48:51], %[op] offset:14336\n\tds_read_b128 v[52:55], %[op] offset:14592\n\ts_waitcnt lgkmcnt(11)\n\tv_mfma_f32_16x16x32_bf16 v[32:35], %[w2], v[56:59], v[32:35]\n\ts_waitcnt lgkmcnt(10)\n\tv_mfma_f32_16x16x32_bf16 v[36:39], %[w2], v[60:63], v[36:39]\n\tds_read_b128 v[56:59], %[op] offset:16384\n\tds_read_b128 v[60:63], %[op] offset:16640\n\ts_waitcnt lgkmcnt(11)\n\tv_mfma_f32_16x16x32_bf16 v[32:35], %[w3], v[64:67], v[32:35]\n\ts_waitcnt lgkmcnt(10)\n\tv_mfma_f32_16x16x32_bf16 v[36:39], %[w3], v[68:71], v[36:39]\n\tds_read_b128 v[64:67], %[op] offset:18432\n\tds_read_b128 v[68:71], %[op] offset:18688\n\ts_waitcnt lgkmcnt(11)\n\tv_mfma_f32_16x16x32_bf16 v[32:35], %[w4], v[72:75], v[32:35]\n\ts_waitcnt lgkmcnt(10)\n\tv_mfma_f32_16x16x32_bf16 v[36:39], %[w4], v[76:79], v[36:39]\n\tds_read_b128 v[72:75], %[op] offset:20480\n\tds_read_b128 v[76:79], %[op] offset:20736\n\ts_waitcnt lgkmcnt(11)\n\tv_mfma_f32_16x16x32_bf16 v[32:35], %[w5], v[80:83], v[32:35]\n\ts_waitcnt lgkmcnt(10)\n\tv_mfma_f32_16x16x32_bf16 v[36:39], %[w5], v[84:87], v[36:39]\n\tds_read_b128 v[80:83], %[op] offset:22528\n\tds_read_b128 v[84:87], %[op] offset:22784\n\ts_waitcnt lgkmcnt(11)\n\tv_mfma_f32_16x16x32_bf16 v[32:35], %[w6], v[40:43], v[32:35]\n\ts_waitcnt lgkmcnt(10)\n\tv_mfma_f32_16x16x32_bf16 v[36:39], %[w6], v[44:47], v[36:39]\n\tds_read_b128 v[40:43], %[op] offset:24576\n\tds_read_b128 v[44:47], %[op] offset:24832\n\ts_waitcnt lgkmcnt(11)\n\tv_mfma_f32_16x16x32_bf16 v[32:35], %[w7], v[48:51], v[32:35]\n\ts_waitcnt lgkmcnt(10)\n\tv_mfma_f32_16x16x32_bf16 v[36:39], %[w7], v[52:55], v[36:39]\n\tds_read_b128 v[48:51], %[op] offset:26624\n\tds_read_b128 v[52:55], %[op] offset:26880\n\ts_waitcnt lgkmcnt(11)\n\tv_mfma_f32_16x16x32_bf16 v[32:35], %[w8], v[56:59], v[32:35]\n\ts_waitcnt lgkmcnt(10)\n\tv_mfma_f32_16x16x32_bf16 v[36:39], %[w8], v[60:63], v[36:39]\n\tds_read_b128 v[56:59], %[op] offset:28672\n\tds_read_b128 v[60:63], %[op] offset:28928\n\ts_waitcnt lgkmcnt(11)\n\tv_mfma_f32_16x16x32_bf16 v[32:35], %[w9], v[64:67], v[32:35]\n\ts_waitcnt lgkmcnt(10)\n\tv_mfma_f32_16x16x32_bf16 v[36:39], %[w9], v[68:71], v[36:39]\n\tds_read_b128 v[64:67], %[op] offset:30720\n\tds_read_b128 v[68:71], %[op] offset:30976\n\ts_waitcnt lgkmcnt(11)\n\tv_mfma_f32_16x16x32_bf16 v[32:35], %[w10], v[72:75], v[32:35]\n\ts_waitcnt lgkmcnt(10)\n\tv_mfma_f32_16x16x32_bf16 v[36:39], %[w10], v[76:79], v[36:39]\n\ts_waitcnt lgkmcnt(9)\n\tv_mfma_f32_16x16x32_bf16 v[32:35], %[w11], v[80:83], v[32:35]\n\ts_waitcnt lgkmcnt(8)\n\tv_mfma_f32_16x16x32_bf16 v[36:39], %[w11], v[84:87], v[36:39]\n\ts_waitcnt lgkmcnt(7)\n\tv_mfma_f32_16x16x32_bf16 v[32:35], %[w12], v[40:43], v[32:35]\n\ts_waitcnt lgkmcnt(6)\n\tv_mfma_f32_16x16x32_bf16 v[36:39], %[w12], v[44:47], v[36:39]\n\ts_waitcnt lgkmcnt(5)\n\tv_mfma_f32_16x16x32_bf16 v[32:35], %[w13], v[48:51], v[32:35]\n\ts_waitcnt lgkmcnt(4)\n\tv_mfma_f32_16x16x32_bf16 v[36:39], %[w13], v[52:55], v[36:39]\n\ts_waitcnt lgkmcnt(3)\n\tv_mfma_f32_16x16x32_bf16 v[32:35], %[w14], v[56:59], v[32:35]\n\ts_waitcnt lgkmcnt(2)\n\tv_mfma_f32_16x16x32_bf16 v[36:39], %[w14], v[60:63], v[36:39]\n\ts_waitcnt lgkmcnt(1)\n\tv_mfma_f32_16x16x32_bf16 v[32:35], %[w15], v[64:67], v[32:35]\n\ts_waitcnt lgkmcnt(0)\n\tv_mfma_f32_16x16x32_bf16 v[36:39], %[w15], v[68:71], v[36:39]\n\ts_nop 7\n\ts_nop 1" : "=&{v[32:35]}"(oa[0]), "=&{v[36:39]}"(oa[1]) : [op] "v"(op), [w0] "v"(wof[0]), [w1] "v"(wof[1]), [w2] "v"(wof[2]), [w3] "v"(wof[3]), [w4] "v"(wof[4]), [w5] "v"(wof[5]), [w6] "v"(wof[6]), [w7] "v"(wof[7]), [w8] "v"(wof[8]), [w9] "v"(wof[9]), [w10] "v"(wof[10]), [w11] "v"(wof[11]), [w12] "v"(wof[12]), [w13] "v"(wof[13]), [w14] "v"(wof[14]), [w15] "v"(wof[15]) : "v40", "v41", "v42", "v43", "v44", "v45", "v46", "v47", "v48", "v49", "v50", "v51", "v52", "v53", "v54", "v55", "v56", "v57", "v58", "v59", "v60", "v61", "v62", "v63", "v64", "v65", "v66", "v67", "v68", "v69", "v70", "v71", "v72", "v73", "v74", "v75", "v76", "v77", "v78", "v79", "v80", "v81", "v82", "v83", "v84", "v85", "v86", "v87", "memory");
    }
    float yv[4] = {0.f, 0.f, 0.f, 0.f};
    if (has_idx) {
        _Pragma("nounroll") for (int i = 0; i < SPIN_CAP; ++i) {
            if (__all(yg[1] == tag && yg[3] == tag)) break;
            FENCE();
            yg = __builtin_amdgcn_raw_buffer_load_b128(rs, yoff, WS_YG, 16);
        }
        float y0 = u2f(yg[0]), y1 = u2f(yg[2]);
        #pragma unroll
        for (int o = 8; o < 64; o <<= 1) { y0 += __shfl_xor(y0, o); y1 += __shfl_xor(y1, o); }
        unsigned ord = rkey >> 16;
        unsigned rb = (ord & 0x8000u) ? (ord & 0x7fffu) : (~ord & 0xffffu);
        const float rsel = bfr(sigmoidf_(bflo(rb)));
        yv[0] = bfr(bfr(__shfl(y0, 2 * g4)) * rsel);     yv[1] = bfr(bfr(__shfl(y1, 2 * g4)) * rsel);
        yv[2] = bfr(bfr(__shfl(y0, 2 * g4 + 1)) * rsel); yv[3] = bfr(bfr(__shfl(y1, 2 * g4 + 1)) * rsel);
    }
    #pragma unroll
    for (int t = 0; t < 2; ++t) {
        const int tok = tb * 64 + tho * 32 + 16 * t + l15;
        const float g = gres[t], omg = bfr(1.f - g);
        float bx[4] = {bflo(xres[t][0]), bfhi(xres[t][0]), bflo(xres[t][1]), bfhi(xres[t][1])};
        float r4[4];
        #pragma unroll
        for (int i = 0; i < 4; ++i) {
            float bb = (has_idx && tok == idx) ? bfr(bx[i] + yv[i]) : bx[i];
            r4[i] = bfr(g * bfr(oa[t][i])) + bfr(omg * bb);
        }
        u32x2 o2 = {pk2(r4[0], r4[1]), pk2(r4[2], r4[3])};
        *(GAS u32x2*)(out + (size_t)tok * 512 + n0 + 4 * g4) = o2;
    }
    ST(11); AP(11);
    { const unsigned long long t1 = __builtin_amdgcn_s_memrealtime(), d = (tB - tA) * AMP_K;
      _Pragma("nounroll") for (int i = 0; i < (1 << 20); ++i) { if (__builtin_amdgcn_s_memrealtime() - t1 >= d) break; __builtin_amdgcn_s_sleep(2); } }
}

extern "C" void kernel_launch(void* const* d_in, const int* in_sizes, int n_in, void* d_out, int out_size,
                              void* d_ws, size_t ws_size, hipStream_t stream) {
    (void)in_sizes; (void)n_in; (void)out_size; (void)ws_size;
    (void)hipFuncSetAttribute((const void*)mqa_moe_fused, hipFuncAttributeMaxDynamicSharedMemorySize, LDS_BYTES);
    mqa_moe_fused<<<256, 512, LDS_BYTES, stream>>>(
        (const u16*)d_in[0], (const u16*)d_in[1], (const u16*)d_in[2], (const u16*)d_in[3], (const u16*)d_in[4],
        (const u16*)d_in[6], (const u16*)d_in[7], (const u16*)d_in[8], (const u16*)d_in[9], (const u16*)d_in[10],
        (u16*)d_out, (char*)d_ws);
}
```

```cpp
#include <hip/hip_runtime.h>
#include <stdint.h>
#define PRIO_N 3
#define XK 7
#define XKK 0

typedef unsigned short u16;
typedef __attribute__((ext_vector_type(8))) __bf16 bf16x8;
typedef __attribute__((ext_vector_type(16))) __bf16 bf16x16;
typedef __attribute__((ext_vector_type(2))) __bf16 bf16x2;
typedef __attribute__((ext_vector_type(4))) short s16x4;
typedef __attribute__((ext_vector_type(2))) float f32x2;
typedef __attribute__((ext_vector_type(4))) float f32x4;
typedef __attribute__((ext_vector_type(8))) float f32x8;
typedef __attribute__((ext_vector_type(16))) float f32x16;
typedef __attribute__((ext_vector_type(4))) unsigned u32x4;
typedef __attribute__((ext_vector_type(2))) unsigned u32x2;
#define LAS __attribute__((address_space(3)))
#define GAS __attribute__((address_space(1)))
#define RLX_AGENT __ATOMIC_RELAXED, __HIP_MEMORY_SCOPE_AGENT
#define RLX_WG __ATOMIC_RELAXED, __HIP_MEMORY_SCOPE_WORKGROUP

constexpr unsigned WS_EPOCH = 0;
constexpr unsigned WS_F1   = 4096;
constexpr unsigned WS_F2   = 16384;
constexpr unsigned WS_KF   = 32768;
constexpr unsigned WS_VF   = 98304;
constexpr unsigned WS_WOF  = 163840;
constexpr unsigned WS_OF   = 688128;
constexpr unsigned WS_GATE = 2785280;
constexpr unsigned WS_HG   = 2793472;
constexpr unsigned WS_YG   = 2801664;
constexpr unsigned WS_END  = 2834432;
constexpr unsigned WS_DBG  = 4194304;

constexpr unsigned REC     = 2080;
constexpr unsigned L_WKV   = 0;
constexpr unsigned L_WOST  = 16384;
constexpr unsigned L_XT    = 24960;
constexpr unsigned L_WQ    = 91520;
constexpr unsigned L_FW1   = 133120;
constexpr unsigned L_FW2   = 149504;
constexpr unsigned L_QX    = 157056;
constexpr unsigned L_MISC  = 161152;
constexpr unsigned L_C2 = L_MISC + 0, L_C4 = L_MISC + 4, L_C5 = L_MISC + 8, L_C6 = L_MISC + 12, L_C7 = L_MISC + 16,
                   L_TAG = L_MISC + 24  , L_KEY = L_MISC + 32;
constexpr unsigned L_XSEL  = L_MISC + 128;
constexpr unsigned L_HBUF  = L_XSEL + 1024;
constexpr unsigned LDS_BYTES = 163840;
constexpr int SPIN_CAP = 1 << 18;

__device__ __forceinline__ unsigned pk2(float a, float b) {
    f32x2 v = {a, b};
    return __builtin_bit_cast(unsigned, __builtin_convertvector(v, bf16x2));
}
__device__ __forceinline__ float bfr(float a) {
    return __builtin_bit_cast(float, pk2(a, a) & 0xffff0000u);
}
__device__ __forceinline__ unsigned f2u(float f) { return __builtin_bit_cast(unsigned, f); }
__device__ __forceinline__ float u2f(unsigned u) { return __builtin_bit_cast(float, u); }
__device__ __forceinline__ float bflo(unsigned u) { return __builtin_bit_cast(float, u << 16); }
__device__ __forceinline__ float bfhi(unsigned u) { return __builtin_bit_cast(float, u & 0xffff0000u); }
__device__ __forceinline__ float sigmoidf_(float z) { return __builtin_amdgcn_rcpf(1.f + __builtin_amdgcn_exp2f(-1.4426950408889634f * z)); }

__device__ __forceinline__ void dma16(unsigned voff, const void* sbase, unsigned lds) {
    unsigned keep;
    asm volatile("s_mov_b32 %0, m0\n\ts_mov_b32 m0, %3\n\ts_nop 0\n\tglobal_load_lds_dwordx4 %1, %2\n\ts_mov_b32 m0, %0"
                 : "=&s"(keep) : "v"(voff), "s"(sbase), "s"(lds) : "memory");
}
__device__ __forceinline__ void dma16p(const void* gsrc, unsigned lds) {
    unsigned keep;
    asm volatile("s_mov_b32 %0, m0\n\ts_mov_b32 m0, %2\n\ts_nop 0\n\tglobal_load_lds_dwordx4 %1, off\n\ts_mov_b32 m0, %0"
                 : "=&s"(keep) : "v"(gsrc), "s"(lds) : "memory");
}
#define WAIT_VM0()   asm volatile("s_waitcnt vmcnt(0)" ::: "memory")
#define WAIT_LGKM0() asm volatile("s_waitcnt lgkmcnt(0)" ::: "memory")
#define BARRIER()    asm volatile("s_waitcnt lgkmcnt(0)\n\ts_barrier" ::: "memory")
#define FENCE()      asm volatile("" ::: "memory")

__device__ __forceinline__ u32x4 tr_frag(LAS char* p) {
    s16x4 a0 = __builtin_amdgcn_ds_read_tr16_b64_v4i16((LAS s16x4*)p);
    s16x4 a1 = __builtin_amdgcn_ds_read_tr16_b64_v4i16((LAS s16x4*)(p + 512));
    u32x2 p0 = __builtin_bit_cast(u32x2, a0), p1 = __builtin_bit_cast(u32x2, a1);
    u32x4 a = {p0[0], p0[1], p1[0], p1[1]};
    return a;
}
__device__ __forceinline__ f32x4 mfma16(u32x4 a, u32x4 b, f32x4 c) {
    return __builtin_amdgcn_mfma_f32_16x16x32_bf16(__builtin_bit_cast(bf16x8, a), __builtin_bit_cast(bf16x8, b), c, 0, 0, 0);
}
__device__ __forceinline__ f32x16 mfma32(u32x4 a, u32x4 b, f32x16 c) {
    return __builtin_amdgcn_mfma_f32_32x32x16_bf16(__builtin_bit_cast(bf16x8, a), __builtin_bit_cast(bf16x8, b), c, 0, 0, 0);
}
__device__ __forceinline__ f32x16 smf(u32x4 v, f32x8 p, f32x16 acc) {
    return __builtin_amdgcn_smfmac_f32_32x32x32_bf16(__builtin_bit_cast(bf16x8, v), __builtin_bit_cast(bf16x16, p), acc, (int)0xDDDDDDDD, 0, 0);
}
__device__ __forceinline__ void lds_wait_ge(LAS char* L, unsigned off, unsigned want) {
    _Pragma("nounroll") for (int i = 0; i < SPIN_CAP; ++i) {
        if (__hip_atomic_load((LAS unsigned*)(L + off), RLX_WG) >= want) break;
        __builtin_amdgcn_s_sleep(1);
    }
    FENCE();
}
__device__ __forceinline__ void lds_signal(LAS char* L, unsigned off, int l) {
    WAIT_LGKM0();
    if (l == 0) __hip_atomic_fetch_add((LAS unsigned*)(L + off), 1u, RLX_WG);
    FENCE();
}
__device__ __forceinline__ unsigned get_tag(LAS char* L) {
    lds_wait_ge(L, L_TAG, 1u);
    return __builtin_amdgcn_readfirstlane(*(LAS unsigned*)(L + L_TAG + 4));
}
#define ST(i) do {} while (0)
#define STC(i) do {} while (0)
#define AP(i) do {} while (0)

__device__ __forceinline__ f32x16 attn_true_max(LAS char* kp, LAS char* vp, u32x4 q) {
    f32x16 z; for (int i = 0; i < 16; ++i) z[i] = 0.f;
    float m = -__builtin_inff();
    #pragma unroll 1
    for (int t = 0; t < 64; ++t) {
        f32x16 s = mfma32(*(LAS u32x4*)(kp + t * REC), q, z);
        #pragma unroll
        for (int i = 0; i < 16; ++i) m = fmaxf(m, s[i]);
    }
    m = fmaxf(m, __shfl_xor(m, 32));
    f32x16 acc = z;
    #pragma unroll 1
    for (int t = 0; t < 64; ++t) {
        f32x16 s = mfma32(*(LAS u32x4*)(kp + t * REC), q, z);
        f32x8 p0, p1;
        #pragma unroll
        for (int i = 0; i < 8; ++i) { p0[i] = __builtin_amdgcn_exp2f(s[i] - m); p1[i] = __builtin_amdgcn_exp2f(s[8 + i] - m); }
        acc = smf(*(LAS u32x4*)(vp + t * REC), p0, acc);
        acc = smf(*(LAS u32x4*)(vp + t * REC + 528), p1, acc);
    }
    return acc;
}

__global__ void __launch_bounds__(512, 2) mqa_moe_fused(
        const u16* __restrict__ x, const u16* __restrict__ wq, const u16* __restrict__ wk, const u16* __restrict__ wv,
        const u16* __restrict__ wo, const u16* __restrict__ w_r, const u16* __restrict__ w_gate, const u16* __restrict__ w_up,
        const u16* __restrict__ w_down, const u16* __restrict__ gate_w, u16* __restrict__ out, char* ws) {
    extern __shared__ __attribute__((aligned(16))) char smem[];
    LAS char* L = (LAS char*)smem;
    const unsigned LB = (unsigned)(size_t)L;
    const int l = threadIdx.x & 63;
    const int w = __builtin_amdgcn_readfirstlane(threadIdx.x >> 6);
    const int b = blockIdx.x, xcd = b & 7, j = b >> 3;
    const int tb = xcd * 4 + (j & 3), hg = j >> 2;
    const int g4 = l >> 4, l15 = l & 15;
    const __amdgpu_buffer_rsrc_t rs = __builtin_amdgcn_make_buffer_rsrc((void*)ws, 0, WS_END, 0x00020000);
    const int sg = (l >> 3) & 3, sjh = l >> 5, sq = (l >> 1) & 3;
    const int krow = 8 * sg + 4 * sjh + sq;
    unsigned tag = 0;
    u32x4 qf;
    ST(0); STC(29); AP(0);

    if (w == 4) {
        const int type = hg & 1, tg = hg >> 1;
        const u16* W = type ? wv : wk;
        const u16* xr = x + (size_t)(tb * 64 + tg * 16 + l15) * 512 + 8 * g4;
        u32x4 xf[16];
        const unsigned wvo = krow * 32 + (l & 1) * 16;
        #pragma unroll
        for (int s = 0; s < 16; ++s) {
            dma16(wvo, (const char*)W + s * 1024, LB + L_WKV + s * 1024);
            xf[s] = *(const GAS u32x4*)(xr + 32 * s);
        }
        unsigned E = __hip_atomic_load((GAS unsigned*)(ws + WS_EPOCH), RLX_AGENT);
        asm volatile("s_barrier" ::: "memory");
        WAIT_VM0();
        ST(1);
        tag = __builtin_amdgcn_readfirstlane(E) + 1u;
        if (l == 0) { u32x2 tv = {1u, tag}; *(LAS u32x2*)(L + L_TAG) = tv; }
        f32x4 acc;
        const unsigned wp = LB + L_WKV + 8 * l;
        if (type == 0) asm volatile("ds_read_b64_tr_b16 v[36:37], %[wp] offset:0\n\tds_read_b64_tr_b16 v[38:39], %[wp] offset:512\n\tds_read_b64_tr_b16 v[40:41], %[wp] offset:1024\n\tds_read_b64_tr_b16 v[42:43], %[wp] offset:1536\n\tds_read_b64_tr_b16 v[44:45], %[wp] offset:2048\n\tds_read_b64_tr_b16 v[46:47], %[wp] offset:2560\n\tds_read_b64_tr_b16 v[48:49], %[wp] offset:3072\n\tds_read_b64_tr_b16 v[50:51], %[wp] offset:3584\n\tds_read_b64_tr_b16 v[52:53], %[wp] offset:4096\n\tds_read_b64_tr_b16 v[54:55], %[wp] offset:4608\n\tds_read_b64_tr_b16 v[56:57], %[wp] offset:5120\n\tds_read_b64_tr_b16 v[58:59], %[wp] offset:5632\n\ts_waitcnt lgkmcnt(10)\n\tv_mfma_f32_16x16x32_bf16 v[32:35], v[36:39], %[x0], 0\n\tds_read_b64_tr_b16 v[36:37], %[wp] offset:6144\n\tds_read_b64_tr_b16 v[38:39], %[wp] offset:6656\n\ts_waitcnt lgkmcnt(10)\n\tv_mfma_f32_16x16x32_bf16 v[32:35], v[40:43], %[x1], v[32:35]\n\tds_read_b64_tr_b16 v[40:41], %[wp] offset:7168\n\tds_read_b64_tr_b16 v[42:43], %[wp] offset:7680\n\ts_waitcnt lgkmcnt(10)\n\tv_mfma_f32_16x16x32_bf16 v[32:35], v[44:47], %[x2], v[32:35]\n\tds_read_b64_tr_b16 v[44:45], %[wp] offset:8192\n\tds_read_b64_tr_b16 v[46:47], %[wp] offset:8704\n\ts_waitcnt lgkmcnt(10)\n\tv_mfma_f32_16x16x32_bf16 v[32:35], v[48:51], %[x3], v[32:35]\n\tds_read_b64_tr_b16 v[48:49], %[wp] offset:9216\n\tds_read_b64_tr_b16 v[50:51], %[wp] offset:9728\n\ts_waitcnt lgkmcnt(10)\n\tv_mfma_f32_16x16x32_bf16 v[32:35], v[52:55], %[x4], v[32:35]\n\tds_read_b64_tr_b16 v[52:53], %[wp] offset:10240\n\tds_read_b64_tr_b16 v[54:55], %[wp] offset:10752\n\ts_waitcnt lgkmcnt(10)\n\tv_mfma_f32_16x16x32_bf16 v[32:35], v[56:59], %[x5], v[32:35]\n\tds_read_b64_tr_b16 v[56:57], %[wp] offset:11264\n\tds_read_b64_tr_b16 v[58:59], %[wp] offset:11776\n\ts_waitcnt lgkmcnt(10)\n\tv_mfma_f32_16x16x32_bf16 v[32:35], v[36:39], %[x6], v[32:35]\n\tds_read_b64_tr_b16 v[36:37], %[wp] offset:12288\n\tds_read_b64_tr_b16 v[38:39], %[wp] offset:12800\n\ts_waitcnt lgkmcnt(10)\n\tv_mfma_f32_16x16x32_bf16 v[32:35], v[40:43], %[x7], v[32:35]\n\tds_read_b64_tr_b16 v[40:41], %[wp] offset:13312\n\tds_read_b64_tr_b16 v[42:43], %[wp] offset:13824\n\ts_waitcnt lgkmcnt(10)\n\tv_mfma_f32_16x16x32_bf16 v[32:35], v[44:47], %[x8], v[32:35]\n\tds_read_b64_tr_b16 v[44:45], %[wp] offset:14336\n\tds_read_b64_tr_b16 v[46:47], %[wp] offset:14848\n\ts_waitcnt lgkmcnt(10)\n\tv_mfma_f32_16x16x32_bf16 v[32:35], v[48:51], %[x9], v[32:35]\n\tds_read_b64_tr_b16 v[48:49], %[wp] offset:15360\n\tds_read_b64_tr_b16 v[50:51], %[wp] offset:15872\n\ts_waitcnt lgkmcnt(10)\n\tv_mfma_f32_16x16x32_bf16 v[32:35], v[52:55], %[x10], v[32:35]\n\ts_waitcnt lgkmcnt(8)\n\tv_mfma_f32_16x16x32_bf16 v[32:35], v[56:59], %[x11], v[32:35]\n\ts_waitcnt lgkmcnt(6)\n\tv_mfma_f32_16x16x32_bf16 v[32:35], v[36:39], %[x12], v[32:35]\n\ts_waitcnt lgkmcnt(4)\n\tv_mfma_f32_16x16x32_bf16 v[32:35], v[40:43], %[x13], v[32:35]\n\ts_waitcnt lgkmcnt(2)\n\tv_mfma_f32_16x16x32_bf16 v[32:35], v[44:47], %[x14], v[32:35]\n\ts_waitcnt lgkmcnt(0)\n\tv_mfma_f32_16x16x32_bf16 v[32:35], v[48:51], %[x15], v[32:35]\n\ts_nop 7\n\ts_nop 1" : "=&{v[32:35]}"(acc) : [wp] "v"(wp), [x0] "v"(xf[0]), [x1] "v"(xf[1]), [x2] "v"(xf[2]), [x3] "v"(xf[3]), [x4] "v"(xf[4]), [x5] "v"(xf[5]), [x6] "v"(xf[6]), [x7] "v"(xf[7]), [x8] "v"(xf[8]), [x9] "v"(xf[9]), [x10] "v"(xf[10]), [x11] "v"(xf[11]), [x12] "v"(xf[12]), [x13] "v"(xf[13]), [x14] "v"(xf[14]), [x15] "v"(xf[15]) : "v36", "v37", "v38", "v39", "v40", "v41", "v42", "v43", "v44", "v45", "v46", "v47", "v48", "v49", "v50", "v51", "v52", "v53", "v54", "v55", "v56", "v57", "v58", "v59", "memory");
        else           asm volatile("ds_read_b64_tr_b16 v[36:37], %[wp] offset:0\n\tds_read_b64_tr_b16 v[38:39], %[wp] offset:512\n\tds_read_b64_tr_b16 v[40:41], %[wp] offset:1024\n\tds_read_b64_tr_b16 v[42:43], %[wp] offset:1536\n\tds_read_b64_tr_b16 v[44:45], %[wp] offset:2048\n\tds_read_b64_tr_b16 v[46:47], %[wp] offset:2560\n\tds_read_b64_tr_b16 v[48:49], %[wp] offset:3072\n\tds_read_b64_tr_b16 v[50:51], %[wp] offset:3584\n\tds_read_b64_tr_b16 v[52:53], %[wp] offset:4096\n\tds_read_b64_tr_b16 v[54:55], %[wp] offset:4608\n\tds_read_b64_tr_b16 v[56:57], %[wp] offset:5120\n\tds_read_b64_tr_b16 v[58:59], %[wp] offset:5632\n\ts_waitcnt lgkmcnt(10)\n\tv_mfma_f32_16x16x32_bf16 v[32:35], %[x0], v[36:39], 0\n\tds_read_b64_tr_b16 v[36:37], %[wp] offset:6144\n\tds_read_b64_tr_b16 v[38:39], %[wp] offset:6656\n\ts_waitcnt lgkmcnt(10)\n\tv_mfma_f32_16x16x32_bf16 v[32:35], %[x1], v[40:43], v[32:35]\n\tds_read_b64_tr_b16 v[40:41], %[wp] offset:7168\n\tds_read_b64_tr_b16 v[42:43], %[wp] offset:7680\n\ts_waitcnt lgkmcnt(10)\n\tv_mfma_f32_16x16x32_bf16 v[32:35], %[x2], v[44:47], v[32:35]\n\tds_read_b64_tr_b16 v[44:45], %[wp] offset:8192\n\tds_read_b64_tr_b16 v[46:47], %[wp] offset:8704\n\ts_waitcnt lgkmcnt(10)\n\tv_mfma_f32_16x16x32_bf16 v[32:35], %[x3], v[48:51], v[32:35]\n\tds_read_b64_tr_b16 v[48:49], %[wp] offset:9216\n\tds_read_b64_tr_b16 v[50:51], %[wp] offset:9728\n\ts_waitcnt lgkmcnt(10)\n\tv_mfma_f32_16x16x32_bf16 v[32:35], %[x4], v[52:55], v[32:35]\n\tds_read_b64_tr_b16 v[52:53], %[wp] offset:10240\n\tds_read_b64_tr_b16 v[54:55], %[wp] offset:10752\n\ts_waitcnt lgkmcnt(10)\n\tv_mfma_f32_16x16x32_bf16 v[32:35], %[x5], v[56:59], v[32:35]\n\tds_read_b64_tr_b16 v[56:57], %[wp] offset:11264\n\tds_read_b64_tr_b16 v[58:59], %[wp] offset:11776\n\ts_waitcnt lgkmcnt(10)\n\tv_mfma_f32_16x16x32_bf16 v[32:35], %[x6], v[36:39], v[32:35]\n\tds_read_b64_tr_b16 v[36:37], %[wp] offset:12288\n\tds_read_b64_tr_b16 v[38:39], %[wp] offset:12800\n\ts_waitcnt lgkmcnt(10)\n\tv_mfma_f32_16x16x32_bf16 v[32:35], %[x7], v[40:43], v[32:35]\n\tds_read_b64_tr_b16 v[40:41], %[wp] offset:13312\n\tds_read_b64_tr_b16 v[42:43], %[wp] offset:13824\n\ts_waitcnt lgkmcnt(10)\n\tv_mfma_f32_16x16x32_bf16 v[32:35], %[x8], v[44:47], v[32:35]\n\tds_read_b64_tr_b16 v[44:45], %[wp] offset:14336\n\tds_read_b64_tr_b16 v[46:47], %[wp] offset:14848\n\ts_waitcnt lgkmcnt(10)\n\tv_mfma_f32_16x16x32_bf16 v[32:35], %[x9], v[48:51], v[32:35]\n\tds_read_b64_tr_b16 v[48:49], %[wp] offset:15360\n\tds_read_b64_tr_b16 v[50:51], %[wp] offset:15872\n\ts_waitcnt lgkmcnt(10)\n\tv_mfma_f32_16x16x32_bf16 v[32:35], %[x10], v[52:55], v[32:35]\n\ts_waitcnt lgkmcnt(8)\n\tv_mfma_f32_16x16x32_bf16 v[32:35], %[x11], v[56:59], v[32:35]\n\ts_waitcnt lgkmcnt(6)\n\tv_mfma_f32_16x16x32_bf16 v[32:35], %[x12], v[36:39], v[32:35]\n\ts_waitcnt lgkmcnt(4)\n\tv_mfma_f32_16x16x32_bf16 v[32:35], %[x13], v[40:43], v[32:35]\n\ts_waitcnt lgkmcnt(2)\n\tv_mfma_f32_16x16x32_bf16 v[32:35], %[x14], v[44:47], v[32:35]\n\ts_waitcnt lgkmcnt(0)\n\tv_mfma_f32_16x16x32_bf16 v[32:35], %[x15], v[48:51], v[32:35]\n\ts_nop 7\n\ts_nop 1" : "=&{v[32:35]}"(acc) : [wp] "v"(wp), [x0] "v"(xf[0]), [x1] "v"(xf[1]), [x2] "v"(xf[2]), [x3] "v"(xf[3]), [x4] "v"(xf[4]), [x5] "v"(xf[5]), [x6] "v"(xf[6]), [x7] "v"(xf[7]), [x8] "v"(xf[8]), [x9] "v"(xf[9]), [x10] "v"(xf[10]), [x11] "v"(xf[11]), [x12] "v"(xf[12]), [x13] "v"(xf[13]), [x14] "v"(xf[14]), [x15] "v"(xf[15]) : "v36", "v37", "v38", "v39", "v40", "v41", "v42", "v43", "v44", "v45", "v46", "v47", "v48", "v49", "v50", "v51", "v52", "v53", "v54", "v55", "v56", "v57", "v58", "v59", "memory");
        unsigned long long pv = (unsigned long long)pk2(acc[0], acc[1]) | ((unsigned long long)pk2(acc[2], acc[3]) << 32);
        unsigned dst;
        if (type == 0) dst = WS_KF + (tb * 2 + (tg >> 1)) * 1024 + ((tg & 1) * 16 + l15 + 32 * (g4 & 1)) * 16 + (g4 >> 1) * 8;
        else           dst = WS_VF + (tb * 4 + tg) * 512 + (g4 >> 1) * 256 + l15 * 16 + (g4 & 1) * 8;
        __hip_atomic_store((GAS unsigned long long*)(ws + dst), pv, RLX_AGENT);
        lds_signal(L, L_C5, l);
        WAIT_VM0();
        ST(2);
        const int kt = tb * 2 + (tg >> 1);
        const int p = (kt & 3) * 64 + (kt >> 2) * 4 + (tg & 1) * 2 + type;
        if (l < 8) __hip_atomic_store((GAS unsigned*)(ws + WS_F1 + l * 1024 + p * 4), tag, RLX_AGENT);
    } else if (w == 5) {
        const int type = hg & 1, tg = hg >> 1;
        const int wrow = 16 * (sg >> 1) + 8 * sjh + 4 * (sg & 1) + sq;
        const char* wsrc = (const char*)(wo + (size_t)(64 * (b & 7)) * 512 + 16 * (b >> 3));
        const unsigned wovo = wrow * 1024 + (l & 1) * 16;
        dma16(wovo, wsrc, LB + L_WOST);
        dma16(wovo, wsrc + 32 * 1024, LB + L_WOST + 1024);
        u32x4 af[16];
        if (type == 0) {
            const u16* ap = ((l & 1) ? gate_w : w_r) + 8 * g4;
            #pragma unroll
            for (int s = 0; s < 16; ++s) af[s] = *(const GAS u32x4*)(ap + 32 * s);
        }
        asm volatile("s_barrier" ::: "memory");
        {
            u32x4 sent = {0xFFFFFFFFu, 0xFFFFFFFFu, 0xFFFFFFFFu, 0xFFFFFFFFu};
            #pragma unroll
            for (int i = 0; i < 8; ++i)
                __builtin_amdgcn_raw_buffer_store_b128(sent, rs, l * 16, WS_OF + ((tb * 2 + (i >> 2)) * 32 + 4 * hg + (i & 3)) * 1024, 16);
        }
        WAIT_VM0();
        #pragma unroll
        for (int i = 0; i < 2; ++i) {
            u32x4 f = tr_frag(L + L_WOST + i * 1024 + 8 * l);
            __builtin_amdgcn_raw_buffer_store_b128(f, rs, l * 16, WS_WOF + (2 * b + i) * 1024, 16);
        }
        lds_signal(L, L_C5, l);
        tag = get_tag(L);
        unsigned key = 0;
        if (type == 0) {
            lds_wait_ge(L, L_C2, 4u);
            f32x4 acc = {0.f, 0.f, 0.f, 0.f};
            LAS char* xp = L + L_XT + (tg * 16 + l15) * 1040 + g4 * 16;
            u32x4 bx[16];
            #pragma unroll
            for (int s = 0; s < 16; ++s) bx[s] = *(LAS u32x4*)(xp + s * 64);
            __builtin_amdgcn_sched_barrier(0);
            #pragma unroll
            for (int s = 0; s < 16; ++s) acc = mfma16(af[s], bx[s], acc);
            const int tok = tb * 64 + tg * 16 + l15;
            float gt = bfr(sigmoidf_(bfr(acc[1])));
            if (l < 16) __hip_atomic_store((GAS unsigned*)(ws + WS_GATE) + tok, __builtin_bit_cast(unsigned, gt), RLX_AGENT);
            unsigned u = pk2(acc[0], acc[0]) & 0xffffu;
            unsigned ord = (u & 0x8000u) ? (~u & 0xffffu) : (u | 0x8000u);
            if ((u & 0x7fffu) > 0x7f80u) ord = 0u;
            key = (l < 16) ? ((ord << 16) | (unsigned)(2047 - tok)) : 0u;
            #pragma unroll
            for (int o = 1; o < 16; o <<= 1) { unsigned k2 = __shfl_xor(key, o); key = key > k2 ? key : k2; }
            key = __builtin_amdgcn_readfirstlane(key);
        }
        WAIT_VM0();
        if (l < 8) __hip_atomic_store((GAS unsigned long long*)(ws + WS_F2 + l * 2048 + b * 8), ((unsigned long long)tag << 32) | key, RLX_AGENT);
        ST(1);
    } else {
        if (w == 0 && l < 16) *(LAS unsigned*)(L + L_MISC + 4 * l) = 0u;
        BARRIER();
        if (w < 4) {
            #pragma unroll 4
            for (int i = 0; i < 16; ++i) {
                const int row = 16 * w + i;
                dma16(l * 16, (const char*)x + (size_t)(tb * 64 + row) * 1024, LB + L_XT + row * 1040);
            }
            const unsigned qvo = (l >> 3) * 1024 + (((l & 7) ^ (((l >> 4) & 1) << 2)) * 16);
            #pragma unroll 4
            for (int i = 0; i < 16; ++i) {
                const int blk = 16 * w + i;
                dma16(qvo, (const char*)wq + (size_t)blk * 8192 + hg * 128, LB + L_WQ + blk * 1024);
            }
            WAIT_VM0();
            ST(1);
            lds_signal(L, L_C2, l);
            lds_wait_ge(L, L_C2, 4u);
            ST(2);
            const int cp = w & 1, th = w >> 1, h = l >> 5, nhi = g4 & 1, q = (l >> 2) & 3, p = l & 3;
            const unsigned ap = LB + L_WQ + h * 1024 + q * 128 + ((64 * cp + 32 * nhi + 8 * p) ^ (((q >> 1) & 1) << 6));
            const unsigned bp = LB + L_XT + (32 * th + (l & 31)) * 1040 + h * 16;
            f32x16 acc;
            asm volatile("ds_read_b64_tr_b16 v[100:101], %[ap] offset:0\n\tds_read_b64_tr_b16 v[102:103], %[ap] offset:512\n\tds_read_b128 v[104:107], %[bp] offset:0\n\tds_read_b64_tr_b16 v[108:109], %[ap] offset:2048\n\tds_read_b64_tr_b16 v[110:111], %[ap] offset:2560\n\tds_read_b128 v[112:115], %[bp] offset:32\n\tds_read_b64_tr_b16 v[116:117], %[ap] offset:4096\n\tds_read_b64_tr_b16 v[118:119], %[ap] offset:4608\n\tds_read_b128 v[120:123], %[bp] offset:64\n\tds_read_b64_tr_b16 v[124:125], %[ap] offset:6144\n\tds_read_b64_tr_b16 v[126:127], %[ap] offset:6656\n\tds_read_b128 v[128:131], %[bp] offset:96\n\ts_waitcnt lgkmcnt(9)\n\tv_mfma_f32_32x32x16_bf16 v[32:47], v[100:103], v[104:107], 0\n\tds_read_b64_tr_b16 v[100:101], %[ap] offset:8192\n\tds_read_b64_tr_b16 v[102:103], %[ap] offset:8704\n\tds_read_b128 v[104:107], %[bp] offset:128\n\ts_waitcnt lgkmcnt(9)\n\tv_mfma_f32_32x32x16_bf16 v[32:47], v[108:111], v[112:115], v[32:47]\n\tds_read_b64_tr_b16 v[108:109], %[ap] offset:10240\n\tds_read_b64_tr_b16 v[110:111], %[ap] offset:10752\n\tds_read_b128 v[112:115], %[bp] offset:160\n\ts_waitcnt lgkmcnt(9)\n\tv_mfma_f32_32x32x16_bf16 v[32:47], v[116:119], v[120:123], v[32:47]\n\tds_read_b64_tr_b16 v[116:117], %[ap] offset:12288\n\tds_read_b64_tr_b16 v[118:119], %[ap] offset:12800\n\tds_read_b128 v[120:123], %[bp] offset:192\n\ts_waitcnt lgkmcnt(9)\n\tv_mfma_f32_32x32x16_bf16 v[32:47], v[124:127], v[128:131], v[32:47]\n\tds_read_b64_tr_b16 v[124:125], %[ap] offset:14336\n\tds_read_b64_tr_b16 v[126:127], %[ap] offset:14848\n\tds_read_b128 v[128:131], %[bp] offset:224\n\ts_waitcnt lgkmcnt(9)\n\tv_mfma_f32_32x32x16_bf16 v[32:47], v[100:103], v[104:107], v[32:47]\n\tds_read_b64_tr_b16 v[100:101], %[ap] offset:16384\n\tds_read_b64_tr_b16 v[102:103], %[ap] offset:16896\n\tds_read_b128 v[104:107], %[bp] offset:256\n\ts_waitcnt lgkmcnt(9)\n\tv_mfma_f32_32x32x16_bf16 v[32:47], v[108:111], v[112:115], v[32:47]\n\tds_read_b64_tr_b16 v[108:109], %[ap] offset:18432\n\tds_read_b64_tr_b16 v[110:111], %[ap] offset:18944\n\tds_read_b128 v[112:115], %[bp] offset:288\n\ts_waitcnt lgkmcnt(9)\n\tv_mfma_f32_32x32x16_bf16 v[32:47], v[116:119], v[120:123], v[32:47]\n\tds_read_b64_tr_b16 v[116:117], %[ap] offset:20480\n\tds_read_b64_tr_b16 v[118:119], %[ap] offset:20992\n\tds_read_b128 v[120:123], %[bp] offset:320\n\ts_waitcnt lgkmcnt(9)\n\tv_mfma_f32_32x32x16_bf16 v[32:47], v[124:127], v[128:131], v[32:47]\n\tds_read_b64_tr_b16 v[124:125], %[ap] offset:22528\n\tds_read_b64_tr_b16 v[126:127], %[ap] offset:23040\n\tds_read_b128 v[128:131], %[bp] offset:352\n\ts_waitcnt lgkmcnt(9)\n\tv_mfma_f32_32x32x16_bf16 v[32:47], v[100:103], v[104:107], v[32:47]\n\tds_read_b64_tr_b16 v[100:101], %[ap] offset:24576\n\tds_read_b64_tr_b16 v[102:103], %[ap] offset:25088\n\tds_read_b128 v[104:107], %[bp] offset:384\n\ts_waitcnt lgkmcnt(9)\n\tv_mfma_f32_32x32x16_bf16 v[32:47], v[108:111], v[112:115], v[32:47]\n\tds_read_b64_tr_b16 v[108:109], %[ap] offset:26624\n\tds_read_b64_tr_b16 v[110:111], %[ap] offset:27136\n\tds_read_b128 v[112:115], %[bp] offset:416\n\ts_waitcnt lgkmcnt(9)\n\tv_mfma_f32_32x32x16_bf16 v[32:47], v[116:119], v[120:123], v[32:47]\n\tds_read_b64_tr_b16 v[116:117], %[ap] offset:28672\n\tds_read_b64_tr_b16 v[118:119], %[ap] offset:29184\n\tds_read_b128 v[120:123], %[bp] offset:448\n\ts_waitcnt lgkmcnt(9)\n\tv_mfma_f32_32x32x16_bf16 v[32:47], v[124:127], v[128:131], v[32:47]\n\tds_read_b64_tr_b16 v[124:125], %[ap] offset:30720\n\tds_read_b64_tr_b16 v[126:127], %[ap] offset:31232\n\tds_read_b128 v[128:131], %[bp] offset:480\n\ts_waitcnt lgkmcnt(9)\n\tv_mfma_f32_32x32x16_bf16 v[32:47], v[100:103], v[104:107], v[32:47]\n\tds_read_b64_tr_b16 v[100:101], %[ap] offset:32768\n\tds_read_b64_tr_b16 v[102:103], %[ap] offset:33280\n\tds_read_b128 v[104:107], %[bp] offset:512\n\ts_waitcnt lgkmcnt(9)\n\tv_mfma_f32_32x32x16_bf16 v[32:47], v[108:111], v[112:115], v[32:47]\n\tds_read_b64_tr_b16 v[108:109], %[ap] offset:34816\n\tds_read_b64_tr_b16 v[110:111], %[ap] offset:35328\n\tds_read_b128 v[112:115], %[bp] offset:544\n\ts_waitcnt lgkmcnt(9)\n\tv_mfma_f32_32x32x16_bf16 v[32:47], v[116:119], v[120:123], v[32:47]\n\tds_read_b64_tr_b16 v[116:117], %[ap] offset:36864\n\tds_read_b64_tr_b16 v[118:119], %[ap] offset:37376\n\tds_read_b128 v[120:123], %[bp] offset:576\n\ts_waitcnt lgkmcnt(9)\n\tv_mfma_f32_32x32x16_bf16 v[32:47], v[124:127], v[128:131], v[32:47]\n\tds_read_b64_tr_b16 v[124:125], %[ap] offset:38912\n\tds_read_b64_tr_b16 v[126:127], %[ap] offset:39424\n\tds_read_b128 v[128:131], %[bp] offset:608\n\ts_waitcnt lgkmcnt(9)\n\tv_mfma_f32_32x32x16_bf16 v[32:47], v[100:103], v[104:107], v[32:47]\n\tds_read_b64_tr_b16 v[100:101], %[ap] offset:40960\n\tds_read_b64_tr_b16 v[102:103], %[ap] offset:41472\n\tds_read_b128 v[104:107], %[bp] offset:640\n\ts_waitcnt lgkmcnt(9)\n\tv_mfma_f32_32x32x16_bf16 v[32:47], v[108:111], v[112:115], v[32:47]\n\tds_read_b64_tr_b16 v[108:109], %[ap] offset:43008\n\tds_read_b64_tr_b16 v[110:111], %[ap] offset:43520\n\tds_read_b128 v[112:115], %[bp] offset:672\n\ts_waitcnt lgkmcnt(9)\n\tv_mfma_f32_32x32x16_bf16 v[32:47], v[116:119], v[120:123], v[32:47]\n\tds_read_b64_tr_b16 v[116:117], %[ap] offset:45056\n\tds_read_b64_tr_b16 v[118:119], %[ap] offset:45568\n\tds_read_b128 v[120:123], %[bp] offset:704\n\ts_waitcnt lgkmcnt(9)\n\tv_mfma_f32_32x32x16_bf16 v[32:47], v[124:127], v[128:131], v[32:47]\n\tds_read_b64_tr_b16 v[124:125], %[ap] offset:47104\n\tds_read_b64_tr_b16 v[126:127], %[ap] offset:47616\n\tds_read_b128 v[128:131], %[bp] offset:736\n\ts_waitcnt lgkmcnt(9)\n\tv_mfma_f32_32x32x16_bf16 v[32:47], v[100:103], v[104:107], v[32:47]\n\tds_read_b64_tr_b16 v[100:101], %[ap] offset:49152\n\tds_read_b64_tr_b16 v[102:103], %[ap] offset:49664\n\tds_read_b128 v[104:107], %[bp] offset:768\n\ts_waitcnt lgkmcnt(9)\n\tv_mfma_f32_32x32x16_bf16 v[32:47], v[108:111], v[112:115], v[32:47]\n\tds_read_b64_tr_b16 v[108:109], %[ap] offset:51200\n\tds_read_b64_tr_b16 v[110:111], %[ap] offset:51712\n\tds_read_b128 v[112:115], %[bp] offset:800\n\ts_waitcnt lgkmcnt(9)\n\tv_mfma_f32_32x32x16_bf16 v[32:47], v[116:119], v[120:123], v[32:47]\n\tds_read_b64_tr_b16 v[116:117], %[ap] offset:53248\n\tds_read_b64_tr_b16 v[118:119], %[ap] offset:53760\n\tds_read_b128 v[120:123], %[bp] offset:832\n\ts_waitcnt lgkmcnt(9)\n\tv_mfma_f32_32x32x16_bf16 v[32:47], v[124:127], v[128:131], v[32:47]\n\tds_read_b64_tr_b16 v[124:125], %[ap] offset:55296\n\tds_read_b64_tr_b16 v[126:127], %[ap] offset:55808\n\tds_read_b128 v[128:131], %[bp] offset:864\n\ts_waitcnt lgkmcnt(9)\n\tv_mfma_f32_32x32x16_bf16 v[32:47], v[100:103], v[104:107], v[32:47]\n\tds_read_b64_tr_b16 v[100:101], %[ap] offset:57344\n\tds_read_b64_tr_b16 v[102:103], %[ap] offset:57856\n\tds_read_b128 v[104:107], %[bp] offset:896\n\ts_waitcnt lgkmcnt(9)\n\tv_mfma_f32_32x32x16_bf16 v[32:47], v[108:111], v[112:115], v[32:47]\n\tds_read_b64_tr_b16 v[108:109], %[ap] offset:59392\n\tds_read_b64_tr_b16 v[110:111], %[ap] offset:59904\n\tds_read_b128 v[112:115], %[bp] offset:928\n\ts_waitcnt lgkmcnt(9)\n\tv_mfma_f32_32x32x16_bf16 v[32:47], v[116:119], v[120:123], v[32:47]\n\tds_read_b64_tr_b16 v[116:117], %[ap] offset:61440\n\tds_read_b64_tr_b16 v[118:119], %[ap] offset:61952\n\tds_read_b128 v[120:123], %[bp] offset:960\n\ts_waitcnt lgkmcnt(9)\n\tv_mfma_f32_32x32x16_bf16 v[32:47], v[124:127], v[128:131], v[32:47]\n\tds_read_b64_tr_b16 v[124:125], %[ap] offset:63488\n\tds_read_b64_tr_b16 v[126:127], %[ap] offset:64000\n\tds_read_b128 v[128:131], %[bp] offset:992\n\ts_waitcnt lgkmcnt(9)\n\tv_mfma_f32_32x32x16_bf16 v[32:47], v[100:103], v[104:107], v[32:47]\n\ts_waitcnt lgkmcnt(6)\n\tv_mfma_f32_32x32x16_bf16 v[32:47], v[108:111], v[112:115], v[32:47]\n\ts_waitcnt lgkmcnt(3)\n\tv_mfma_f32_32x32x16_bf16 v[32:47], v[116:119], v[120:123], v[32:47]\n\ts_waitcnt lgkmcnt(0)\n\tv_mfma_f32_32x32x16_bf16 v[32:47], v[124:127], v[128:131], v[32:47]\n\ts_nop 15\n\ts_nop 3" : "=&{v[32:47]}"(acc) : [ap] "v"(ap), [bp] "v"(bp) : "v100", "v101", "v102", "v103", "v104", "v105", "v106", "v107", "v108", "v109", "v110", "v111", "v112", "v113", "v114", "v115", "v116", "v117", "v118", "v119", "v120", "v121", "v122", "v123", "v124", "v125", "v126", "v127", "v128", "v129", "v130", "v131", "memory");
            const float sc = 0.25f * 1.4426950408889634f;
            u32x4 qo;
            #pragma unroll
            for (int i = 0; i < 4; ++i) { qf[i] = pk2(acc[2 * i] * sc, acc[2 * i + 1] * sc); qo[i] = pk2(acc[8 + 2 * i] * sc, acc[9 + 2 * i] * sc); }
            *(LAS u32x4*)(L + L_QX + w * 1024 + l * 16) = qo;
            ST(3);
        }
    }

    const int rep = b & 7;
    if (w >= 4) {
        const int bw = w - 4;
        if (w >= 6) tag = get_tag(L);
        {
            GAS unsigned* fp = (GAS unsigned*)(ws + WS_F1 + rep * 1024) + bw * 64 + l;
            _Pragma("nounroll") for (int i = 0; i < SPIN_CAP; ++i) {
                unsigned v = __hip_atomic_load(fp, RLX_AGENT);
                if (__all(v == tag)) break;
                __builtin_amdgcn_s_sleep(1);
            }
            FENCE();
        }
        ST(4);
        u32x4 kf[16], vf[16];
        #pragma unroll
        for (int i = 0; i < 16; ++i) {
            kf[i] = __builtin_amdgcn_raw_buffer_load_b128(rs, l * 16, WS_KF + (4 * i + bw) * 1024, 16);
            vf[i] = __builtin_amdgcn_raw_buffer_load_b128(rs, l * 16, WS_VF + (4 * i + bw) * 1024, 16);
        }
        lds_wait_ge(L, L_C5, 2u);
        LAS char* kd = L + bw * REC + l * 16;
        LAS char* vd = L + bw * REC + 1024 + (l >> 5) * 528 + (l & 31) * 16;
        const u32x4 ones = {0x3f803f80u, 0x3f803f80u, 0x3f803f80u, 0x3f803f80u};
        LAS char* od = L + ((l >> 1) * 4 + bw) * REC + 1024 + (l & 1) * 528 + 512;
        #pragma unroll
        for (int i = 0; i < 3; ++i) { *(LAS u32x4*)(kd + i * 4 * REC) = kf[i]; *(LAS u32x4*)(vd + i * 4 * REC) = vf[i]; }
        if (l < 6) *(LAS u32x4*)od = ones;
        ST(5);
        BARRIER();
        ST(6); AP(6);
        qf = *(LAS u32x4*)(L + L_QX + bw * 1024 + l * 16);
        #pragma unroll
        for (int i = 3; i < 16; ++i) { *(LAS u32x4*)(kd + i * 4 * REC) = kf[i]; *(LAS u32x4*)(vd + i * 4 * REC) = vf[i]; }
        if (l >= 6 && l < 32) *(LAS u32x4*)od = ones;
        lds_signal(L, L_C4, l);
        if (b == 0 && w == 4 && l == 0) __hip_atomic_store((GAS unsigned*)(ws + WS_EPOCH), tag, RLX_AGENT);
    } else {
        BARRIER();
        ST(6); AP(6);
        tag = get_tag(L);
    }

    const int ft = xcd * 28 + j;
    if (j < 28) {
        const u16* wsrc = ((l & 1) ? w_up : w_gate) + (size_t)krow * 1792 + 8 * ft;
        #pragma unroll
        for (int i = 0; i < 2; ++i) { const int s = 2 * w + i; dma16p(wsrc + (size_t)s * 32 * 1792, LB + L_FW1 + s * 1024); }
    }
    if (w < 7) dma16(krow * 1024 + (l & 1) * 16, (const char*)(w_down + (size_t)(224 * xcd + 32 * w) * 512 + 16 * j), LB + L_FW2 + w * 1024);

    const int hl = 2 * (w & 1) + (w >> 2), tha = (w >> 1) & 1;
    unsigned kp = LB + l * 16;
    unsigned vp = LB + 1024 + ((l & 31) < 16 ? (l >> 5) * 256 + (l & 31) * 16 : 512);
    const int idxv = (int)0xDDDDDDDD;
    f32x16 X, Y, acc; u32x4 KR, V0, V1;
    for (int i = 0; i < 16; ++i) acc[i] = 0.f;
    if (w >= 4) __builtin_amdgcn_s_setprio(1);
    ST(7); STC(30); AP(7);
    asm volatile("s_branch MQA_OVER\n\tMQA_BODY:\n\tv_exp_f32 v32, v32\n\tv_exp_f32 v33, v33\n\ts_waitcnt lgkmcnt(2)\n\tv_smfmac_f32_32x32x32_bf16 v[64:79], v[88:91], v[56:63], v96\n\tds_read_b128 v[88:91], v98 offset:528\n\tv_exp_f32 v34, v34\n\tv_exp_f32 v35, v35\n\tv_exp_f32 v36, v36\n\tv_exp_f32 v37, v37\n\ts_waitcnt lgkmcnt(2)\n\tv_mfma_f32_32x32x16_bf16 v[48:63], v[80:83], v[92:95], 0\n\tds_read_b128 v[80:83], v97 offset:4160\n\tv_exp_f32 v38, v38\n\tv_exp_f32 v39, v39\n\tv_exp_f32 v40, v40\n\tv_exp_f32 v41, v41\n\ts_waitcnt lgkmcnt(2)\n\tv_smfmac_f32_32x32x32_bf16 v[64:79], v[84:87], v[32:39], v96\n\tds_read_b128 v[84:87], v98 offset:2080\n\tv_exp_f32 v42, v42\n\tv_exp_f32 v43, v43\n\tv_exp_f32 v44, v44\n\tv_exp_f32 v45, v45\n\tv_exp_f32 v46, v46\n\tv_exp_f32 v47, v47\n\tv_exp_f32 v48, v48\n\tv_exp_f32 v49, v49\n\ts_waitcnt lgkmcnt(2)\n\tv_smfmac_f32_32x32x32_bf16 v[64:79], v[88:91], v[40:47], v96\n\tds_read_b128 v[88:91], v98 offset:2608\n\tv_exp_f32 v50, v50\n\tv_exp_f32 v51, v51\n\tv_exp_f32 v52, v52\n\tv_exp_f32 v53, v53\n\ts_waitcnt lgkmcnt(2)\n\tv_mfma_f32_32x32x16_bf16 v[32:47], v[80:83], v[92:95], 0\n\tds_read_b128 v[80:83], v97 offset:6240\n\tv_exp_f32 v54, v54\n\tv_exp_f32 v55, v55\n\tv_exp_f32 v56, v56\n\tv_exp_f32 v57, v57\n\ts_waitcnt lgkmcnt(2)\n\tv_smfmac_f32_32x32x32_bf16 v[64:79], v[84:87], v[48:55], v96\n\tds_read_b128 v[84:87], v98 offset:4160\n\tv_exp_f32 v58, v58\n\tv_exp_f32 v59, v59\n\tv_exp_f32 v60, v60\n\tv_exp_f32 v61, v61\n\tv_exp_f32 v62, v62\n\tv_exp_f32 v63, v63\n\tv_exp_f32 v32, v32\n\tv_exp_f32 v33, v33\n\ts_waitcnt lgkmcnt(2)\n\tv_smfmac_f32_32x32x32_bf16 v[64:79], v[88:91], v[56:63], v96\n\tds_read_b128 v[88:91], v98 offset:4688\n\tv_exp_f32 v34, v34\n\tv_exp_f32 v35, v35\n\tv_exp_f32 v36, v36\n\tv_exp_f32 v37, v37\n\ts_waitcnt lgkmcnt(2)\n\tv_mfma_f32_32x32x16_bf16 v[48:63], v[80:83], v[92:95], 0\n\tds_read_b128 v[80:83], v97 offset:8320\n\tv_exp_f32 v38, v38\n\tv_exp_f32 v39, v39\n\tv_exp_f32 v40, v40\n\tv_exp_f32 v41, v41\n\ts_waitcnt lgkmcnt(2)\n\tv_smfmac_f32_32x32x32_bf16 v[64:79], v[84:87], v[32:39], v96\n\tds_read_b128 v[84:87], v98 offset:6240\n\tv_exp_f32 v42, v42\n\tv_exp_f32 v43, v43\n\tv_exp_f32 v44, v44\n\tv_exp_f32 v45, v45\n\tv_exp_f32 v46, v46\n\tv_exp_f32 v47, v47\n\tv_exp_f32 v48, v48\n\tv_exp_f32 v49, v49\n\ts_waitcnt lgkmcnt(2)\n\tv_smfmac_f32_32x32x32_bf16 v[64:79], v[88:91], v[40:47], v96\n\tds_read_b128 v[88:91], v98 offset:6768\n\tv_exp_f32 v50, v50\n\tv_exp_f32 v51, v51\n\tv_exp_f32 v52, v52\n\tv_exp_f32 v53, v53\n\ts_waitcnt lgkmcnt(2)\n\tv_mfma_f32_32x32x16_bf16 v[32:47], v[80:83], v[92:95], 0\n\tds_read_b128 v[80:83], v97 offset:10400\n\tv_exp_f32 v54, v54\n\tv_exp_f32 v55, v55\n\tv_exp_f32 v56, v56\n\tv_exp_f32 v57, v57\n\ts_waitcnt lgkmcnt(2)\n\tv_smfmac_f32_32x32x32_bf16 v[64:79], v[84:87], v[48:55], v96\n\tds_read_b128 v[84:87], v98 offset:8320\n\tv_exp_f32 v58, v58\n\tv_exp_f32 v59, v59\n\tv_exp_f32 v60, v60\n\tv_exp_f32 v61, v61\n\tv_exp_f32 v62, v62\n\tv_exp_f32 v63, v63\n\tv_exp_f32 v32, v32\n\tv_exp_f32 v33, v33\n\ts_waitcnt lgkmcnt(2)\n\tv_smfmac_f32_32x32x32_bf16 v[64:79], v[88:91], v[56:63], v96\n\tds_read_b128 v[88:91], v98 offset:8848\n\tv_exp_f32 v34, v34\n\tv_exp_f32 v35, v35\n\tv_exp_f32 v36, v36\n\tv_exp_f32 v37, v37\n\ts_waitcnt lgkmcnt(2)\n\tv_mfma_f32_32x32x16_bf16 v[48:63], v[80:83], v[92:95], 0\n\tds_read_b128 v[80:83], v97 offset:12480\n\tv_exp_f32 v38, v38\n\tv_exp_f32 v39, v39\n\tv_exp_f32 v40, v40\n\tv_exp_f32 v41, v41\n\ts_waitcnt lgkmcnt(2)\n\tv_smfmac_f32_32x32x32_bf16 v[64:79], v[84:87], v[32:39], v96\n\tds_read_b128 v[84:87], v98 offset:10400\n\tv_exp_f32 v42, v42\n\tv_exp_f32 v43, v43\n\tv_exp_f32 v44, v44\n\tv_exp_f32 v45, v45\n\tv_exp_f32 v46, v46\n\tv_exp_f32 v47, v47\n\tv_exp_f32 v48, v48\n\tv_exp_f32 v49, v49\n\ts_waitcnt lgkmcnt(2)\n\tv_smfmac_f32_32x32x32_bf16 v[64:79], v[88:91], v[40:47], v96\n\tds_read_b128 v[88:91], v98 offset:10928\n\tv_exp_f32 v50, v50\n\tv_exp_f32 v51, v51\n\tv_exp_f32 v52, v52\n\tv_exp_f32 v53, v53\n\ts_waitcnt lgkmcnt(2)\n\tv_mfma_f32_32x32x16_bf16 v[32:47], v[80:83], v[92:95], 0\n\tds_read_b128 v[80:83], v97 offset:14560\n\tv_exp_f32 v54, v54\n\tv_exp_f32 v55, v55\n\tv_exp_f32 v56, v56\n\tv_exp_f32 v57, v57\n\ts_waitcnt lgkmcnt(2)\n\tv_smfmac_f32_32x32x32_bf16 v[64:79], v[84:87], v[48:55], v96\n\tds_read_b128 v[84:87], v98 offset:12480\n\tv_exp_f32 v58, v58\n\tv_exp_f32 v59, v59\n\tv_exp_f32 v60, v60\n\tv_exp_f32 v61, v61\n\tv_exp_f32 v62, v62\n\tv_exp_f32 v63, v63\n\tv_exp_f32 v32, v32\n\tv_exp_f32 v33, v33\n\ts_waitcnt lgkmcnt(2)\n\tv_smfmac_f32_32x32x32_bf16 v[64:79], v[88:91], v[56:63], v96\n\tds_read_b128 v[88:91], v98 offset:13008\n\tv_exp_f32 v34, v34\n\tv_exp_f32 v35, v35\n\tv_exp_f32 v36, v36\n\tv_exp_f32 v37, v37\n\ts_waitcnt lgkmcnt(2)\n\tv_mfma_f32_32x32x16_bf16 v[48:63], v[80:83], v[92:95], 0\n\tds_read_b128 v[80:83], v97 offset:16640\n\tv_exp_f32 v38, v38\n\tv_exp_f32 v39, v39\n\tv_exp_f32 v40, v40\n\tv_exp_f32 v41, v41\n\ts_waitcnt lgkmcnt(2)\n\tv_smfmac_f32_32x32x32_bf16 v[64:79], v[84:87], v[32:39], v96\n\tds_read_b128 v[84:87], v98 offset:14560\n\tv_exp_f32 v42, v42\n\tv_exp_f32 v43, v43\n\tv_exp_f32 v44, v44\n\tv_exp_f32 v45, v45\n\tv_exp_f32 v46, v46\n\tv_exp_f32 v47, v47\n\tv_exp_f32 v48, v48\n\tv_exp_f32 v49, v49\n\ts_waitcnt lgkmcnt(2)\n\tv_smfmac_f32_32x32x32_bf16 v[64:79], v[88:91], v[40:47], v96\n\tds_read_b128 v[88:91], v98 offset:15088\n\tv_exp_f32 v50, v50\n\tv_exp_f32 v51, v51\n\tv_exp_f32 v52, v52\n\tv_exp_f32 v53, v53\n\ts_waitcnt lgkmcnt(2)\n\tv_mfma_f32_32x32x16_bf16 v[32:47], v[80:83], v[92:95], 0\n\tds_read_b128 v[80:83], v97 offset:18720\n\tv_exp_f32 v54, v54\n\tv_exp_f32 v55, v55\n\tv_exp_f32 v56, v56\n\tv_exp_f32 v57, v57\n\ts_waitcnt lgkmcnt(2)\n\tv_smfmac_f32_32x32x32_bf16 v[64:79], v[84:87], v[48:55], v96\n\tds_read_b128 v[84:87], v98 offset:16640\n\tv_exp_f32 v58, v58\n\tv_exp_f32 v59, v59\n\tv_exp_f32 v60, v60\n\tv_exp_f32 v61, v61\n\tv_exp_f32 v62, v62\n\tv_exp_f32 v63, v63\n\tv_add_u32 v97, 16640, v97\n\tv_add_u32 v98, 16640, v98\n\ts_sub_u32 s78, s78, 1\n\ts_cmp_lg_u32 s78, 0\n\ts_cbranch_scc1 MQA_BODY\n\ts_waitcnt lgkmcnt(0)\n\ts_nop 7\n\ts_setpc_b64 s[76:77]\n\tMQA_OVER:\n\tds_read_b128 v[80:83], v97 offset:0\n\tds_read_b128 v[84:87], v98 offset:0\n\ts_waitcnt lgkmcnt(1)\n\tv_mfma_f32_32x32x16_bf16 v[32:47], v[80:83], v[92:95], 0\n\tds_read_b128 v[80:83], v97 offset:2080\n\tds_read_b128 v[88:91], v98 offset:528\n\tv_mov_b32 v56, 0\n\tv_mov_b32 v57, 0\n\tv_mov_b32 v58, 0\n\tv_mov_b32 v59, 0\n\tv_mov_b32 v60, 0\n\tv_mov_b32 v61, 0\n\tv_mov_b32 v62, 0\n\tv_mov_b32 v63, 0\n\tv_mov_b32 v48, 0\n\tv_mov_b32 v49, 0\n\tv_mov_b32 v50, 0\n\tv_mov_b32 v51, 0\n\tv_mov_b32 v52, 0\n\tv_mov_b32 v53, 0\n\tv_mov_b32 v54, 0\n\tv_mov_b32 v55, 0\n\ts_nop 7\n\ts_nop 3\n\ts_waitcnt lgkmcnt(0)"
                 : "={v[32:47]}"(X), "={v[48:63]}"(Y), "={v[80:83]}"(KR), "={v[84:87]}"(V0), "={v[88:91]}"(V1)
                 : "{v[92:95]}"(qf), "{v97}"(kp), "{v98}"(vp) : "memory");
#define SEG(N) asm volatile("s_mov_b32 s78, %[n]\n\ts_getpc_b64 s[76:77]\n\ts_add_u32 s76, s76, 12\n\ts_addc_u32 s77, s77, 0\n\ts_branch MQA_BODY" \
                     : "+{v[32:47]}"(X), "+{v[48:63]}"(Y), "+{v[64:79]}"(acc), "+{v[80:83]}"(KR), "+{v[84:87]}"(V0), "+{v[88:91]}"(V1), \
                       "+{v97}"(kp), "+{v98}"(vp) \
                     : [n] "n"(N), "{v[92:95]}"(qf), "{v96}"(idxv) : "s76", "s77", "s78", "scc", "memory")
    const int tho = hg >> 2, cg = hg & 3;
    const int n0 = cg * 128 + 16 * w;
    u32x4 fl0, fl1;
    if (w == 0) {
        fl0 = __builtin_amdgcn_raw_buffer_load_b128(rs, l * 16, WS_F2 + rep * 2048, 16);
        fl1 = __builtin_amdgcn_raw_buffer_load_b128(rs, l * 16, WS_F2 + rep * 2048 + 1024, 16);
    }
    SEG(1);
    ST(12);
    WAIT_VM0();
    lds_signal(L, L_C7, l);
    if (w == 0) {
        _Pragma("nounroll") for (int i = 0; i < SPIN_CAP; ++i) {
            if (__all(fl0[1] == tag && fl0[3] == tag && fl1[1] == tag && fl1[3] == tag)) break;
            FENCE();
            fl0 = __builtin_amdgcn_raw_buffer_load_b128(rs, l * 16, WS_F2 + rep * 2048, 16);
            fl1 = __builtin_amdgcn_raw_buffer_load_b128(rs, l * 16, WS_F2 + rep * 2048 + 1024, 16);
        }
        unsigned key = max(max(fl0[0], fl0[2]), max(fl1[0], fl1[2]));
        #pragma unroll
        for (int o = 1; o < 64; o <<= 1) { unsigned k2 = __shfl_xor(key, o); key = key > k2 ? key : k2; }
        key = __builtin_amdgcn_readfirstlane(key);
        if (l == 0) *(LAS unsigned*)(L + L_KEY) = key;
        lds_signal(L, L_C6, l);
        const int idx = 2047 - (int)(key & 0x7ffu);
        dma16(l * 16, (const char*)x + (size_t)idx * 1024, LB + L_XSEL);
    }
    lds_wait_ge(L, L_C4, 4u);
    SEG(1);
    ST(13);
    if (w == 0 && j < 28) {
        WAIT_VM0();
        lds_wait_ge(L, L_C7, 8u);
        f32x4 a = {0.f, 0.f, 0.f, 0.f};
        u32x4 fa[8], fb[8];
        #pragma unroll
        for (int hh = 0; hh < 2; ++hh) {
            #pragma unroll
            for (int s = 0; s < 8; ++s) {
                fa[s] = tr_frag(L + L_FW1 + (8 * hh + s) * 1024 + 8 * l);
                fb[s] = *(LAS u32x4*)(L + L_XSEL + (8 * hh + s) * 64 + g4 * 16);
            }
            __builtin_amdgcn_sched_barrier(0);
            #pragma unroll
            for (int s = 0; s < 8; ++s) a = mfma16(fa[s], fb[s], a);
            __builtin_amdgcn_sched_barrier(0);
        }
        float hv[4];
        #pragma unroll
        for (int i = 0; i < 4; ++i) {
            float u = bfr(__shfl_xor(a[i], 32));
            float gq = bfr(a[i]);
            hv[i] = bfr(gq * sigmoidf_(gq)) * u;
        }
        u32x4 gr = {pk2(hv[0], hv[1]), tag, pk2(hv[2], hv[3]), tag};
        if (l == 0 || l == 16) __builtin_amdgcn_raw_buffer_store_b128(gr, rs, g4 * 16, WS_HG + ft * 32, 16);
    }
    SEG(1);
    ST(14);
    lds_wait_ge(L, L_C6, 1u);
    const unsigned rkey = __builtin_amdgcn_readfirstlane(*(LAS unsigned*)(L + L_KEY));
    if (w == 1) fl0 = __builtin_amdgcn_raw_buffer_load_b128(rs, (l < 56 ? l : 55) * 16, WS_HG + xcd * 896, 16);
    u32x4 wof[16];
    u32x2 xres[2];
    float gres[2];
    #pragma unroll
    for (int s = 0; s < 16; ++s)
        wof[s] = __builtin_amdgcn_raw_buffer_load_b128(rs, l * 16, WS_WOF + ((cg * 8 + w) * 16 + s) * 1024, 16);
    #pragma unroll
    for (int t = 0; t < 2; ++t) {
        const int tok = tb * 64 + tho * 32 + 16 * t + l15;
        xres[t] = *(const GAS u32x2*)(x + (size_t)tok * 512 + n0 + 4 * g4);
        gres[t] = u2f(__hip_atomic_load((GAS unsigned*)(ws + WS_GATE) + tok, RLX_AGENT));
    }
    SEG(1);
    ST(15);
    if (w == 1) {
        _Pragma("nounroll") for (int i = 0; i < SPIN_CAP; ++i) {
            if (__all(fl0[1] == tag && fl0[3] == tag)) break;
            FENCE();
            fl0 = __builtin_amdgcn_raw_buffer_load_b128(rs, (l < 56 ? l : 55) * 16, WS_HG + xcd * 896, 16);
        }
        u32x2 hv = {fl0[0], fl0[2]};
        if (l < 56) *(LAS u32x2*)(L + L_HBUF + l * 8) = hv;
        WAIT_LGKM0();
        f32x4 a = {0.f, 0.f, 0.f, 0.f};
        u32x4 fa[7], fb[7];
        #pragma unroll
        for (int s = 0; s < 7; ++s) {
            fa[s] = tr_frag(L + L_FW2 + s * 1024 + 8 * l);
            fb[s] = *(LAS u32x4*)(L + L_HBUF + s * 64 + g4 * 16);
        }
        __builtin_amdgcn_sched_barrier(0);
        #pragma unroll
        for (int s = 0; s < 7; ++s) a = mfma16(fa[s], fb[s], a);
        u32x4 g0 = {f2u(a[0]), tag, f2u(a[1]), tag};
        u32x4 g1 = {f2u(a[2]), tag, f2u(a[3]), tag};
        if (l15 == 0) {
            __builtin_amdgcn_raw_buffer_store_b128(g0, rs, g4 * 32, WS_YG + (xcd * 512 + 16 * j) * 8, 16);
            __builtin_amdgcn_raw_buffer_store_b128(g1, rs, g4 * 32 + 16, WS_YG + (xcd * 512 + 16 * j) * 8, 16);
        }
    }
    SEG(PRIO_N);
    ST(16);
    __builtin_amdgcn_s_setprio(0);
    SEG(4 - PRIO_N);
    asm volatile("s_nop 1\n\tv_smfmac_f32_32x32x32_bf16 v[64:79], v[88:91], v[56:63], v96\n\ts_nop 15\n\ts_nop 7" : "+{v[64:79]}"(acc) : "{v[48:63]}"(Y), "{v[88:91]}"(V1), "{v96}"(idxv) : "memory");
    __builtin_amdgcn_s_setprio(0);
    ST(8); STC(31); AP(8);

    {
        const float ls = acc[8];
        bool ok = ls >= 8.6736174e-19f && ls <= 1.2676506e30f;
        #pragma unroll
        for (int i = 0; i < 8; ++i) ok = ok && (__builtin_fabsf(acc[i]) < __builtin_inff());
        if (!__all(ok)) {
            LAS char* kq = L + l * 16;
            LAS char* vq = L + 1024 + ((l & 31) < 16 ? (l >> 5) * 256 + (l & 31) * 16 : 512);
            acc = attn_true_max(kq, vq, qf);
        }
    }
    {
        const float inv = 1.f / acc[8];
        u32x4 o;
        #pragma unroll
        for (int i = 0; i < 4; ++i) { unsigned u = pk2(acc[2 * i] * inv, acc[2 * i + 1] * inv); o[i] = u < 0xFFFFFFFEu ? u : 0xFFFFFFFEu; }
        __builtin_amdgcn_raw_buffer_store_b128(o, rs, l * 16, WS_OF + ((tb * 2 + tha) * 32 + 4 * hg + hl) * 1024, 16);
    }
    const int idx = 2047 - (int)(rkey & 0x7ffu);
    const bool has_idx = (idx >> 5) == tb * 2 + tho;
    u32x4 yg = {0u, tag, 0u, tag};
    const unsigned yoff = ((l >> 3) * 512 + n0 + 2 * (l & 7)) * 8;
    if (has_idx) yg = __builtin_amdgcn_raw_buffer_load_b128(rs, yoff, WS_YG, 16);
    BARRIER();
    ST(9); AP(9);
    {
        u32x4 f[4];
        const unsigned src = WS_OF + ((tb * 2 + tho) * 32 + 4 * w) * 1024;
        _Pragma("nounroll") for (int i = 0; i < SPIN_CAP; ++i) {
            bool ok = true;
            #pragma unroll
            for (int k = 0; k < 4; ++k) {
                f[k] = __builtin_amdgcn_raw_buffer_load_b128(rs, l * 16, src + k * 1024, 16);
                ok = ok && f[k][0] != 0xFFFFFFFFu && f[k][1] != 0xFFFFFFFFu && f[k][2] != 0xFFFFFFFFu && f[k][3] != 0xFFFFFFFFu;
            }
            if (__all(ok)) break;
            FENCE();
        }
        #pragma unroll
        for (int k = 0; k < 4; ++k) *(LAS u32x4*)(L + (4 * w + k) * 1024 + l * 16) = f[k];
    }
    ST(10); AP(10);
    BARRIER();
    f32x4 oa[2];
    {
        const unsigned op = LB + (g4 >> 1) * 1024 + (g4 & 1) * 512 + l15 * 16;
        asm volatile("ds_read_b128 v[40:43], %[op] offset:0\n\tds_read_b128 v[44:47], %[op] offset:256\n\tds_read_b128 v[48:51], %[op] offset:2048\n\tds_read_b128 v[52:55], %[op] offset:2304\n\tds_read_b128 v[56:59], %[op] offset:4096\n\tds_read_b128 v[60:63], %[op] offset:4352\n\tds_read_b128 v[64:67], %[op] offset:6144\n\tds_read_b128 v[68:71], %[op] offset:6400\n\tds_read_b128 v[72:75], %[op] offset:8192\n\tds_read_b128 v[76:79], %[op] offset:8448\n\tds_read_b128 v[80:83], %[op] offset:10240\n\tds_read_b128 v[84:87], %[op] offset:10496\n\ts_waitcnt lgkmcnt(11)\n\tv_mfma_f32_16x16x32_bf16 v[32:35], %[w0], v[40:43], 0\n\ts_waitcnt lgkmcnt(10)\n\tv_mfma_f32_16x16x32_bf16 v[36:39], %[w0], v[44:47], 0\n\tds_read_b128 v[40:43], %[op] offset:12288\n\tds_read_b128 v[44:47], %[op] offset:12544\n\ts_waitcnt lgkmcnt(11)\n\tv_mfma_f32_16x16x32_bf16 v[32:35], %[w1], v[48:51], v[32:35]\n\ts_waitcnt lgkmcnt(10)\n\tv_mfma_f32_16x16x32_bf16 v[36:39], %[w1], v[52:55], v[36:39]\n\tds_read_b128 v[48:51], %[op] offset:14336\n\tds_read_b128 v[52:55], %[op] offset:14592\n\ts_waitcnt lgkmcnt(11)\n\tv_mfma_f32_16x16x32_bf16 v[32:35], %[w2], v[56:59], v[32:35]\n\ts_waitcnt lgkmcnt(10)\n\tv_mfma_f32_16x16x32_bf16 v[36:39], %[w2], v[60:63], v[36:39]\n\tds_read_b128 v[56:59], %[op] offset:16384\n\tds_read_b128 v[60:63], %[op] offset:16640\n\ts_waitcnt lgkmcnt(11)\n\tv_mfma_f32_16x16x32_bf16 v[32:35], %[w3], v[64:67], v[32:35]\n\ts_waitcnt lgkmcnt(10)\n\tv_mfma_f32_16x16x32_bf16 v[36:39], %[w3], v[68:71], v[36:39]\n\tds_read_b128 v[64:67], %[op] offset:18432\n\tds_read_b128 v[68:71], %[op] offset:18688\n\ts_waitcnt lgkmcnt(11)\n\tv_mfma_f32_16x16x32_bf16 v[32:35], %[w4], v[72:75], v[32:35]\n\ts_waitcnt lgkmcnt(10)\n\tv_mfma_f32_16x16x32_bf16 v[36:39], %[w4], v[76:79], v[36:39]\n\tds_read_b128 v[72:75], %[op] offset:20480\n\tds_read_b128 v[76:79], %[op] offset:20736\n\ts_waitcnt lgkmcnt(11)\n\tv_mfma_f32_16x16x32_bf16 v[32:35], %[w5], v[80:83], v[32:35]\n\ts_waitcnt lgkmcnt(10)\n\tv_mfma_f32_16x16x32_bf16 v[36:39], %[w5], v[84:87], v[36:39]\n\tds_read_b128 v[80:83], %[op] offset:22528\n\tds_read_b128 v[84:87], %[op] offset:22784\n\ts_waitcnt lgkmcnt(11)\n\tv_mfma_f32_16x16x32_bf16 v[32:35], %[w6], v[40:43], v[32:35]\n\ts_waitcnt lgkmcnt(10)\n\tv_mfma_f32_16x16x32_bf16 v[36:39], %[w6], v[44:47], v[36:39]\n\tds_read_b128 v[40:43], %[op] offset:24576\n\tds_read_b128 v[44:47], %[op] offset:24832\n\ts_waitcnt lgkmcnt(11)\n\tv_mfma_f32_16x16x32_bf16 v[32:35], %[w7], v[48:51], v[32:35]\n\ts_waitcnt lgkmcnt(10)\n\tv_mfma_f32_16x16x32_bf16 v[36:39], %[w7], v[52:55], v[36:39]\n\tds_read_b128 v[48:51], %[op] offset:26624\n\tds_read_b128 v[52:55], %[op] offset:26880\n\ts_waitcnt lgkmcnt(11)\n\tv_mfma_f32_16x16x32_bf16 v[32:35], %[w8], v[56:59], v[32:35]\n\ts_waitcnt lgkmcnt(10)\n\tv_mfma_f32_16x16x32_bf16 v[36:39], %[w8], v[60:63], v[36:39]\n\tds_read_b128 v[56:59], %[op] offset:28672\n\tds_read_b128 v[60:63], %[op] offset:28928\n\ts_waitcnt lgkmcnt(11)\n\tv_mfma_f32_16x16x32_bf16 v[32:35], %[w9], v[64:67], v[32:35]\n\ts_waitcnt lgkmcnt(10)\n\tv_mfma_f32_16x16x32_bf16 v[36:39], %[w9], v[68:71], v[36:39]\n\tds_read_b128 v[64:67], %[op] offset:30720\n\tds_read_b128 v[68:71], %[op] offset:30976\n\ts_waitcnt lgkmcnt(11)\n\tv_mfma_f32_16x16x32_bf16 v[32:35], %[w10], v[72:75], v[32:35]\n\ts_waitcnt lgkmcnt(10)\n\tv_mfma_f32_16x16x32_bf16 v[36:39], %[w10], v[76:79], v[36:39]\n\ts_waitcnt lgkmcnt(9)\n\tv_mfma_f32_16x16x32_bf16 v[32:35], %[w11], v[80:83], v[32:35]\n\ts_waitcnt lgkmcnt(8)\n\tv_mfma_f32_16x16x32_bf16 v[36:39], %[w11], v[84:87], v[36:39]\n\ts_waitcnt lgkmcnt(7)\n\tv_mfma_f32_16x16x32_bf16 v[32:35], %[w12], v[40:43], v[32:35]\n\ts_waitcnt lgkmcnt(6)\n\tv_mfma_f32_16x16x32_bf16 v[36:39], %[w12], v[44:47], v[36:39]\n\ts_waitcnt lgkmcnt(5)\n\tv_mfma_f32_16x16x32_bf16 v[32:35], %[w13], v[48:51], v[32:35]\n\ts_waitcnt lgkmcnt(4)\n\tv_mfma_f32_16x16x32_bf16 v[36:39], %[w13], v[52:55], v[36:39]\n\ts_waitcnt lgkmcnt(3)\n\tv_mfma_f32_16x16x32_bf16 v[32:35], %[w14], v[56:59], v[32:35]\n\ts_waitcnt lgkmcnt(2)\n\tv_mfma_f32_16x16x32_bf16 v[36:39], %[w14], v[60:63], v[36:39]\n\ts_waitcnt lgkmcnt(1)\n\tv_mfma_f32_16x16x32_bf16 v[32:35], %[w15], v[64:67], v[32:35]\n\ts_waitcnt lgkmcnt(0)\n\tv_mfma_f32_16x16x32_bf16 v[36:39], %[w15], v[68:71], v[36:39]\n\ts_nop 7\n\ts_nop 1" : "=&{v[32:35]}"(oa[0]), "=&{v[36:39]}"(oa[1]) : [op] "v"(op), [w0] "v"(wof[0]), [w1] "v"(wof[1]), [w2] "v"(wof[2]), [w3] "v"(wof[3]), [w4] "v"(wof[4]), [w5] "v"(wof[5]), [w6] "v"(wof[6]), [w7] "v"(wof[7]), [w8] "v"(wof[8]), [w9] "v"(wof[9]), [w10] "v"(wof[10]), [w11] "v"(wof[11]), [w12] "v"(wof[12]), [w13] "v"(wof[13]), [w14] "v"(wof[14]), [w15] "v"(wof[15]) : "v40", "v41", "v42", "v43", "v44", "v45", "v46", "v47", "v48", "v49", "v50", "v51", "v52", "v53", "v54", "v55", "v56", "v57", "v58", "v59", "v60", "v61", "v62", "v63", "v64", "v65", "v66", "v67", "v68", "v69", "v70", "v71", "v72", "v73", "v74", "v75", "v76", "v77", "v78", "v79", "v80", "v81", "v82", "v83", "v84", "v85", "v86", "v87", "memory");
    }
    float yv[4] = {0.f, 0.f, 0.f, 0.f};
    if (has_idx) {
        _Pragma("nounroll") for (int i = 0; i < SPIN_CAP; ++i) {
            if (__all(yg[1] == tag && yg[3] == tag)) break;
            FENCE();
            yg = __builtin_amdgcn_raw_buffer_load_b128(rs, yoff, WS_YG, 16);
        }
        float y0 = u2f(yg[0]), y1 = u2f(yg[2]);
        #pragma unroll
        for (int o = 8; o < 64; o <<= 1) { y0 += __shfl_xor(y0, o); y1 += __shfl_xor(y1, o); }
        unsigned ord = rkey >> 16;
        unsigned rb = (ord & 0x8000u) ? (ord & 0x7fffu) : (~ord & 0xffffu);
        const float rsel = bfr(sigmoidf_(bflo(rb)));
        yv[0] = bfr(bfr(__shfl(y0, 2 * g4)) * rsel);     yv[1] = bfr(bfr(__shfl(y1, 2 * g4)) * rsel);
        yv[2] = bfr(bfr(__shfl(y0, 2 * g4 + 1)) * rsel); yv[3] = bfr(bfr(__shfl(y1, 2 * g4 + 1)) * rsel);
    }
    #pragma unroll
    for (int t = 0; t < 2; ++t) {
        const int tok = tb * 64 + tho * 32 + 16 * t + l15;
        const float g = gres[t], omg = bfr(1.f - g);
        float bx[4] = {bflo(xres[t][0]), bfhi(xres[t][0]), bflo(xres[t][1]), bfhi(xres[t][1])};
        float r4[4];
        #pragma unroll
        for (int i = 0; i < 4; ++i) {
            float bb = (has_idx && tok == idx) ? bfr(bx[i] + yv[i]) : bx[i];
            r4[i] = bfr(g * bfr(oa[t][i])) + bfr(omg * bb);
        }
        u32x2 o2 = {pk2(r4[0], r4[1]), pk2(r4[2], r4[3])};
        *(GAS u32x2*)(out + (size_t)tok * 512 + n0 + 4 * g4) = o2;
    }
    ST(11); AP(11);
    if (l == 0) *(GAS unsigned long long*)(ws + WS_DBG + (b * 8 + w) * 8) = __builtin_amdgcn_s_memrealtime();
}

__global__ void xk_small(char* p) {}
__global__ void __launch_bounds__(512, 2) xk_big(char* p) { asm volatile("v_mov_b32 v207, 0" ::: "v207"); }
__global__ void __launch_bounds__(512, 2) xk_store(char* p) {
    asm volatile("v_mov_b32 v207, 0" ::: "v207");
    u32x4 v = {1u, 2u, 3u, threadIdx.x};
    *(GAS u32x4*)(p + (8u << 20) + (blockIdx.x * 512 + threadIdx.x) * 16) = v;
}
__global__ void xk_t0(char* p) { if (threadIdx.x == 0) *(GAS unsigned long long*)(p + WS_DBG) = __builtin_amdgcn_s_memrealtime(); }
__global__ void xk_t1(char* p) {
    const unsigned long long now = __builtin_amdgcn_s_memrealtime();
    unsigned long long m = 0;
    for (int i = 0; i < 32; ++i) { unsigned long long v = *(GAS unsigned long long*)(p + WS_DBG + (i * 64 + threadIdx.x) * 8); m = v > m ? v : m; }
    for (int o = 1; o < 64; o <<= 1) { unsigned long long v = __shfl_xor(m, o); m = v > m ? v : m; }
    const unsigned long long t1 = __builtin_amdgcn_s_memrealtime(), d = (now - m) * XKK;
    _Pragma("nounroll") for (int i = 0; i < (1 << 20); ++i) { if (__builtin_amdgcn_s_memrealtime() - t1 >= d) break; __builtin_amdgcn_s_sleep(2); }
}
extern "C" void kernel_launch(void* const* d_in, const int* in_sizes, int n_in, void* d_out, int out_size,
                              void* d_ws, size_t ws_size, hipStream_t stream) {
    (void)in_sizes; (void)n_in; (void)out_size; (void)ws_size;
    (void)hipFuncSetAttribute((const void*)mqa_moe_fused, hipFuncAttributeMaxDynamicSharedMemorySize, LDS_BYTES);
    (void)hipFuncSetAttribute((const void*)xk_big, hipFuncAttributeMaxDynamicSharedMemorySize, LDS_BYTES);
    (void)hipFuncSetAttribute((const void*)xk_store, hipFuncAttributeMaxDynamicSharedMemorySize, LDS_BYTES);
    if (XK == 1) xk_small<<<1, 64, 0, stream>>>((char*)d_ws);
    if (XK == 2) xk_big<<<256, 512, LDS_BYTES, stream>>>((char*)d_ws);
    if (XK == 3) xk_store<<<256, 512, LDS_BYTES, stream>>>((char*)d_ws);
    if (XK == 6) xk_t0<<<1, 64, 0, stream>>>((char*)d_ws);
    if (XK == 4) xk_big<<<256, 512, 0, stream>>>((char*)d_ws);
    mqa_moe_fused<<<256, 512, LDS_BYTES, stream>>>(
        (const u16*)d_in[0], (const u16*)d_in[1], (const u16*)d_in[2], (const u16*)d_in[3], (const u16*)d_in[4],
        (const u16*)d_in[6], (const u16*)d_in[7], (const u16*)d_in[8], (const u16*)d_in[9], (const u16*)d_in[10],
        (u16*)d_out, (char*)d_ws);
    if (XK == 5) xk_small<<<1, 64, 0, stream>>>((char*)d_ws);
    if (XK == 7) xk_t1<<<1, 64, 0, stream>>>((char*)d_ws);
}
```

```cpp
#include <hip/hip_runtime.h>
#include <stdint.h>
#define PRIO_N 3
#define AMP_K 0
#define AMP_A 6
#define AMP_B 8

typedef unsigned short u16;
typedef __attribute__((ext_vector_type(8))) __bf16 bf16x8;
typedef __attribute__((ext_vector_type(16))) __bf16 bf16x16;
typedef __attribute__((ext_vector_type(2))) __bf16 bf16x2;
typedef __attribute__((ext_vector_type(4))) short s16x4;
typedef __attribute__((ext_vector_type(2))) float f32x2;
typedef __attribute__((ext_vector_type(4))) float f32x4;
typedef __attribute__((ext_vector_type(8))) float f32x8;
typedef __attribute__((ext_vector_type(16))) float f32x16;
typedef __attribute__((ext_vector_type(4))) unsigned u32x4;
typedef __attribute__((ext_vector_type(2))) unsigned u32x2;
#define LAS __attribute__((address_space(3)))
#define GAS __attribute__((address_space(1)))
#define RLX_AGENT __ATOMIC_RELAXED, __HIP_MEMORY_SCOPE_AGENT
#define RLX_WG __ATOMIC_RELAXED, __HIP_MEMORY_SCOPE_WORKGROUP

constexpr unsigned WS_EPOCH = 0;
constexpr unsigned WS_F1   = 4096;
constexpr unsigned WS_F2   = 16384;
constexpr unsigned WS_KF   = 32768;
constexpr unsigned WS_VF   = 98304;
constexpr unsigned WS_WOF  = 163840;
constexpr unsigned WS_OF   = 688128;
constexpr unsigned WS_GATE = 2785280;
constexpr unsigned WS_HG   = 2793472;
constexpr unsigned WS_YG   = 2801664;
constexpr unsigned WS_END  = 2834432;
constexpr unsigned WS_DBG  = 4194304;

constexpr unsigned REC     = 2080;
constexpr unsigned L_WKV   = 0;
constexpr unsigned L_WOST  = 16384;
constexpr unsigned L_XT    = 24960;
constexpr unsigned L_WQ    = 91520;
constexpr unsigned L_FW1   = 133120;
constexpr unsigned L_FW2   = 149504;
constexpr unsigned L_QX    = 157056;
constexpr unsigned L_MISC  = 161152;
constexpr unsigned L_C2 = L_MISC + 0, L_C4 = L_MISC + 4, L_C5 = L_MISC + 8, L_C6 = L_MISC + 12, L_C7 = L_MISC + 16,
                   L_TAG = L_MISC + 24  , L_KEY = L_MISC + 32;
constexpr unsigned L_XSEL  = L_MISC + 128;
constexpr unsigned L_HBUF  = L_XSEL + 1024;
constexpr unsigned LDS_BYTES = 163840;
constexpr int SPIN_CAP = 1 << 18;

__device__ __forceinline__ unsigned pk2(float a, float b) {
    f32x2 v = {a, b};
    return __builtin_bit_cast(unsigned, __builtin_convertvector(v, bf16x2));
}
__device__ __forceinline__ float bfr(float a) {
    return __builtin_bit_cast(float, pk2(a, a) & 0xffff0000u);
}
__device__ __forceinline__ unsigned f2u(float f) { return __builtin_bit_cast(unsigned, f); }
__device__ __forceinline__ float u2f(unsigned u) { return __builtin_bit_cast(float, u); }
__device__ __forceinline__ float bflo(unsigned u) { return __builtin_bit_cast(float, u << 16); }
__device__ __forceinline__ float bfhi(unsigned u) { return __builtin_bit_cast(float, u & 0xffff0000u); }
__device__ __forceinline__ float sigmoidf_(float z) { return __builtin_amdgcn_rcpf(1.f + __builtin_amdgcn_exp2f(-1.4426950408889634f * z)); }

__device__ __forceinline__ void dma16(unsigned voff, const void* sbase, unsigned lds) {
    unsigned keep;
    asm volatile("s_mov_b32 %0, m0\n\ts_mov_b32 m0, %3\n\ts_nop 0\n\tglobal_load_lds_dwordx4 %1, %2\n\ts_mov_b32 m0, %0"
                 : "=&s"(keep) : "v"(voff), "s"(sbase), "s"(lds) : "memory");
}
__device__ __forceinline__ void dma16p(const void* gsrc, unsigned lds) {
    unsigned keep;
    asm volatile("s_mov_b32 %0, m0\n\ts_mov_b32 m0, %2\n\ts_nop 0\n\tglobal_load_lds_dwordx4 %1, off\n\ts_mov_b32 m0, %0"
                 : "=&s"(keep) : "v"(gsrc), "s"(lds) : "memory");
}
#define WAIT_VM0()   asm volatile("s_waitcnt vmcnt(0)" ::: "memory")
#define WAIT_LGKM0() asm volatile("s_waitcnt lgkmcnt(0)" ::: "memory")
#define BARRIER()    asm volatile("s_waitcnt lgkmcnt(0)\n\ts_barrier" ::: "memory")
#define FENCE()      asm volatile("" ::: "memory")

__device__ __forceinline__ u32x4 tr_frag(LAS char* p) {
    s16x4 a0 = __builtin_amdgcn_ds_read_tr16_b64_v4i16((LAS s16x4*)p);
    s16x4 a1 = __builtin_amdgcn_ds_read_tr16_b64_v4i16((LAS s16x4*)(p + 512));
    u32x2 p0 = __builtin_bit_cast(u32x2, a0), p1 = __builtin_bit_cast(u32x2, a1);
    u32x4 a = {p0[0], p0[1], p1[0], p1[1]};
    return a;
}
__device__ __forceinline__ f32x4 mfma16(u32x4 a, u32x4 b, f32x4 c) {
    return __builtin_amdgcn_mfma_f32_16x16x32_bf16(__builtin_bit_cast(bf16x8, a), __builtin_bit_cast(bf16x8, b), c, 0, 0, 0);
}
__device__ __forceinline__ f32x16 mfma32(u32x4 a, u32x4 b, f32x16 c) {
    return __builtin_amdgcn_mfma_f32_32x32x16_bf16(__builtin_bit_cast(bf16x8, a), __builtin_bit_cast(bf16x8, b), c, 0, 0, 0);
}
__device__ __forceinline__ f32x16 smf(u32x4 v, f32x8 p, f32x16 acc) {
    return __builtin_amdgcn_smfmac_f32_32x32x32_bf16(__builtin_bit_cast(bf16x8, v), __builtin_bit_cast(bf16x16, p), acc, (int)0xDDDDDDDD, 0, 0);
}
__device__ __forceinline__ void lds_wait_ge(LAS char* L, unsigned off, unsigned want) {
    _Pragma("nounroll") for (int i = 0; i < SPIN_CAP; ++i) {
        if (__hip_atomic_load((LAS unsigned*)(L + off), RLX_WG) >= want) break;
        __builtin_amdgcn_s_sleep(1);
    }
    FENCE();
}
__device__ __forceinline__ void lds_signal(LAS char* L, unsigned off, int l) {
    WAIT_LGKM0();
    if (l == 0) __hip_atomic_fetch_add((LAS unsigned*)(L + off), 1u, RLX_WG);
    FENCE();
}
__device__ __forceinline__ unsigned get_tag(LAS char* L) {
    lds_wait_ge(L, L_TAG, 1u);
    return __builtin_amdgcn_readfirstlane(*(LAS unsigned*)(L + L_TAG + 4));
}
#define ST(i) do {} while (0)
#define STC(i) do {} while (0)
#define APT() __builtin_amdgcn_s_memrealtime()
#define AMP_CYC 1
#define AMP_W 255
#define AP(i) do { if ((i) == AMP_A) tA = APT(); if ((i) == AMP_B) tB = APT(); } while (0)

__device__ __forceinline__ f32x16 attn_true_max(LAS char* kp, LAS char* vp, u32x4 q) {
    f32x16 z; for (int i = 0; i < 16; ++i) z[i] = 0.f;
    float m = -__builtin_inff();
    #pragma unroll 1
    for (int t = 0; t < 64; ++t) {
        f32x16 s = mfma32(*(LAS u32x4*)(kp + t * REC), q, z);
        #pragma unroll
        for (int i = 0; i < 16; ++i) m = fmaxf(m, s[i]);
    }
    m = fmaxf(m, __shfl_xor(m, 32));
    f32x16 acc = z;
    #pragma unroll 1
    for (int t = 0; t < 64; ++t) {
        f32x16 s = mfma32(*(LAS u32x4*)(kp + t * REC), q, z);
        f32x8 p0, p1;
        #pragma unroll
        for (int i = 0; i < 8; ++i) { p0[i] = __builtin_amdgcn_exp2f(s[i] - m); p1[i] = __builtin_amdgcn_exp2f(s[8 + i] - m); }
        acc = smf(*(LAS u32x4*)(vp + t * REC), p0, acc);
        acc = smf(*(LAS u32x4*)(vp + t * REC + 528), p1, acc);
    }
    return acc;
}

__global__ void __launch_bounds__(512, 2) mqa_moe_fused(
        const u16* __restrict__ x, const u16* __restrict__ wq, const u16* __restrict__ wk, const u16* __restrict__ wv,
        const u16* __restrict__ wo, const u16* __restrict__ w_r, const u16* __restrict__ w_gate, const u16* __restrict__ w_up,
        const u16* __restrict__ w_down, const u16* __restrict__ gate_w, u16* __restrict__ out, char* ws) {
    extern __shared__ __attribute__((aligned(16))) char smem[];
    LAS char* L = (LAS char*)smem;
    const unsigned LB = (unsigned)(size_t)L;
    const int l = threadIdx.x & 63;
    const int w = __builtin_amdgcn_readfirstlane(threadIdx.x >> 6);
    const int b = blockIdx.x, xcd = b & 7, j = b >> 3;
    const int tb = xcd * 4 + (j & 3), hg = j >> 2;
    const int g4 = l >> 4, l15 = l & 15;
    const __amdgpu_buffer_rsrc_t rs = __builtin_amdgcn_make_buffer_rsrc((void*)ws, 0, WS_END, 0x00020000);
    const int sg = (l >> 3) & 3, sjh = l >> 5, sq = (l >> 1) & 3;
    const int krow = 8 * sg + 4 * sjh + sq;
    unsigned tag = 0;
    u32x4 qf;
    unsigned long long tA = 0, tB = 0;
    ST(0); STC(29); AP(0);

    if (w == 4) {
        const int type = hg & 1, tg = hg >> 1;
        const u16* W = type ? wv : wk;
        const u16* xr = x + (size_t)(tb * 64 + tg * 16 + l15) * 512 + 8 * g4;
        u32x4 xf[16];
        const unsigned wvo = krow * 32 + (l & 1) * 16;
        #pragma unroll
        for (int s = 0; s < 16; ++s) {
            dma16(wvo, (const char*)W + s * 1024, LB + L_WKV + s * 1024);
            xf[s] = *(const GAS u32x4*)(xr + 32 * s);
        }
        unsigned E = __hip_atomic_load((GAS unsigned*)(ws + WS_EPOCH), RLX_AGENT);
        asm volatile("s_barrier" ::: "memory");
        WAIT_VM0();
        ST(1); AP(1);
        tag = __builtin_amdgcn_readfirstlane(E) + 1u;
        if (l == 0) { u32x2 tv = {1u, tag}; *(LAS u32x2*)(L + L_TAG) = tv; }
        f32x4 acc;
        const unsigned wp = LB + L_WKV + 8 * l;
        if (type == 0) asm volatile("ds_read_b64_tr_b16 v[36:37], %[wp] offset:0\n\tds_read_b64_tr_b16 v[38:39], %[wp] offset:512\n\tds_read_b64_tr_b16 v[40:41], %[wp] offset:1024\n\tds_read_b64_tr_b16 v[42:43], %[wp] offset:1536\n\tds_read_b64_tr_b16 v[44:45], %[wp] offset:2048\n\tds_read_b64_tr_b16 v[46:47], %[wp] offset:2560\n\tds_read_b64_tr_b16 v[48:49], %[wp] offset:3072\n\tds_read_b64_tr_b16 v[50:51], %[wp] offset:3584\n\tds_read_b64_tr_b16 v[52:53], %[wp] offset:4096\n\tds_read_b64_tr_b16 v[54:55], %[wp] offset:4608\n\tds_read_b64_tr_b16 v[56:57], %[wp] offset:5120\n\tds_read_b64_tr_b16 v[58:59], %[wp] offset:5632\n\ts_waitcnt lgkmcnt(10)\n\tv_mfma_f32_16x16x32_bf16 v[32:35], v[36:39], %[x0], 0\n\tds_read_b64_tr_b16 v[36:37], %[wp] offset:6144\n\tds_read_b64_tr_b16 v[38:39], %[wp] offset:6656\n\ts_waitcnt lgkmcnt(10)\n\tv_mfma_f32_16x16x32_bf16 v[32:35], v[40:43], %[x1], v[32:35]\n\tds_read_b64_tr_b16 v[40:41], %[wp] offset:7168\n\tds_read_b64_tr_b16 v[42:43], %[wp] offset:7680\n\ts_waitcnt lgkmcnt(10)\n\tv_mfma_f32_16x16x32_bf16 v[32:35], v[44:47], %[x2], v[32:35]\n\tds_read_b64_tr_b16 v[44:45], %[wp] offset:8192\n\tds_read_b64_tr_b16 v[46:47], %[wp] offset:8704\n\ts_waitcnt lgkmcnt(10)\n\tv_mfma_f32_16x16x32_bf16 v[32:35], v[48:51], %[x3], v[32:35]\n\tds_read_b64_tr_b16 v[48:49], %[wp] offset:9216\n\tds_read_b64_tr_b16 v[50:51], %[wp] offset:9728\n\ts_waitcnt lgkmcnt(10)\n\tv_mfma_f32_16x16x32_bf16 v[32:35], v[52:55], %[x4], v[32:35]\n\tds_read_b64_tr_b16 v[52:53], %[wp] offset:10240\n\tds_read_b64_tr_b16 v[54:55], %[wp] offset:10752\n\ts_waitcnt lgkmcnt(10)\n\tv_mfma_f32_16x16x32_bf16 v[32:35], v[56:59], %[x5], v[32:35]\n\tds_read_b64_tr_b16 v[56:57], %[wp] offset:11264\n\tds_read_b64_tr_b16 v[58:59], %[wp] offset:11776\n\ts_waitcnt lgkmcnt(10)\n\tv_mfma_f32_16x16x32_bf16 v[32:35], v[36:39], %[x6], v[32:35]\n\tds_read_b64_tr_b16 v[36:37], %[wp] offset:12288\n\tds_read_b64_tr_b16 v[38:39], %[wp] offset:12800\n\ts_waitcnt lgkmcnt(10)\n\tv_mfma_f32_16x16x32_bf16 v[32:35], v[40:43], %[x7], v[32:35]\n\tds_read_b64_tr_b16 v[40:41], %[wp] offset:13312\n\tds_read_b64_tr_b16 v[42:43], %[wp] offset:13824\n\ts_waitcnt lgkmcnt(10)\n\tv_mfma_f32_16x16x32_bf16 v[32:35], v[44:47], %[x8], v[32:35]\n\tds_read_b64_tr_b16 v[44:45], %[wp] offset:14336\n\tds_read_b64_tr_b16 v[46:47], %[wp] offset:14848\n\ts_waitcnt lgkmcnt(10)\n\tv_mfma_f32_16x16x32_bf16 v[32:35], v[48:51], %[x9], v[32:35]\n\tds_read_b64_tr_b16 v[48:49], %[wp] offset:15360\n\tds_read_b64_tr_b16 v[50:51], %[wp] offset:15872\n\ts_waitcnt lgkmcnt(10)\n\tv_mfma_f32_16x16x32_bf16 v[32:35], v[52:55], %[x10], v[32:35]\n\ts_waitcnt lgkmcnt(8)\n\tv_mfma_f32_16x16x32_bf16 v[32:35], v[56:59], %[x11], v[32:35]\n\ts_waitcnt lgkmcnt(6)\n\tv_mfma_f32_16x16x32_bf16 v[32:35], v[36:39], %[x12], v[32:35]\n\ts_waitcnt lgkmcnt(4)\n\tv_mfma_f32_16x16x32_bf16 v[32:35], v[40:43], %[x13], v[32:35]\n\ts_waitcnt lgkmcnt(2)\n\tv_mfma_f32_16x16x32_bf16 v[32:35], v[44:47], %[x14], v[32:35]\n\ts_waitcnt lgkmcnt(0)\n\tv_mfma_f32_16x16x32_bf16 v[32:35], v[48:51], %[x15], v[32:35]\n\ts_nop 7\n\ts_nop 1" : "=&{v[32:35]}"(acc) : [wp] "v"(wp), [x0] "v"(xf[0]), [x1] "v"(xf[1]), [x2] "v"(xf[2]), [x3] "v"(xf[3]), [x4] "v"(xf[4]), [x5] "v"(xf[5]), [x6] "v"(xf[6]), [x7] "v"(xf[7]), [x8] "v"(xf[8]), [x9] "v"(xf[9]), [x10] "v"(xf[10]), [x11] "v"(xf[11]), [x12] "v"(xf[12]), [x13] "v"(xf[13]), [x14] "v"(xf[14]), [x15] "v"(xf[15]) : "v36", "v37", "v38", "v39", "v40", "v41", "v42", "v43", "v44", "v45", "v46", "v47", "v48", "v49", "v50", "v51", "v52", "v53", "v54", "v55", "v56", "v57", "v58", "v59", "memory");
        else           asm volatile("ds_read_b64_tr_b16 v[36:37], %[wp] offset:0\n\tds_read_b64_tr_b16 v[38:39], %[wp] offset:512\n\tds_read_b64_tr_b16 v[40:41], %[wp] offset:1024\n\tds_read_b64_tr_b16 v[42:43], %[wp] offset:1536\n\tds_read_b64_tr_b16 v[44:45], %[wp] offset:2048\n\tds_read_b64_tr_b16 v[46:47], %[wp] offset:2560\n\tds_read_b64_tr_b16 v[48:49], %[wp] offset:3072\n\tds_read_b64_tr_b16 v[50:51], %[wp] offset:3584\n\tds_read_b64_tr_b16 v[52:53], %[wp] offset:4096\n\tds_read_b64_tr_b16 v[54:55], %[wp] offset:4608\n\tds_read_b64_tr_b16 v[56:57], %[wp] offset:5120\n\tds_read_b64_tr_b16 v[58:59], %[wp] offset:5632\n\ts_waitcnt lgkmcnt(10)\n\tv_mfma_f32_16x16x32_bf16 v[32:35], %[x0], v[36:39], 0\n\tds_read_b64_tr_b16 v[36:37], %[wp] offset:6144\n\tds_read_b64_tr_b16 v[38:39], %[wp] offset:6656\n\ts_waitcnt lgkmcnt(10)\n\tv_mfma_f32_16x16x32_bf16 v[32:35], %[x1], v[40:43], v[32:35]\n\tds_read_b64_tr_b16 v[40:41], %[wp] offset:7168\n\tds_read_b64_tr_b16 v[42:43], %[wp] offset:7680\n\ts_waitcnt lgkmcnt(10)\n\tv_mfma_f32_16x16x32_bf16 v[32:35], %[x2], v[44:47], v[32:35]\n\tds_read_b64_tr_b16 v[44:45], %[wp] offset:8192\n\tds_read_b64_tr_b16 v[46:47], %[wp] offset:8704\n\ts_waitcnt lgkmcnt(10)\n\tv_mfma_f32_16x16x32_bf16 v[32:35], %[x3], v[48:51], v[32:35]\n\tds_read_b64_tr_b16 v[48:49], %[wp] offset:9216\n\tds_read_b64_tr_b16 v[50:51], %[wp] offset:9728\n\ts_waitcnt lgkmcnt(10)\n\tv_mfma_f32_16x16x32_bf16 v[32:35], %[x4], v[52:55], v[32:35]\n\tds_read_b64_tr_b16 v[52:53], %[wp] offset:10240\n\tds_read_b64_tr_b16 v[54:55], %[wp] offset:10752\n\ts_waitcnt lgkmcnt(10)\n\tv_mfma_f32_16x16x32_bf16 v[32:35], %[x5], v[56:59], v[32:35]\n\tds_read_b64_tr_b16 v[56:57], %[wp] offset:11264\n\tds_read_b64_tr_b16 v[58:59], %[wp] offset:11776\n\ts_waitcnt lgkmcnt(10)\n\tv_mfma_f32_16x16x32_bf16 v[32:35], %[x6], v[36:39], v[32:35]\n\tds_read_b64_tr_b16 v[36:37], %[wp] offset:12288\n\tds_read_b64_tr_b16 v[38:39], %[wp] offset:12800\n\ts_waitcnt lgkmcnt(10)\n\tv_mfma_f32_16x16x32_bf16 v[32:35], %[x7], v[40:43], v[32:35]\n\tds_read_b64_tr_b16 v[40:41], %[wp] offset:13312\n\tds_read_b64_tr_b16 v[42:43], %[wp] offset:13824\n\ts_waitcnt lgkmcnt(10)\n\tv_mfma_f32_16x16x32_bf16 v[32:35], %[x8], v[44:47], v[32:35]\n\tds_read_b64_tr_b16 v[44:45], %[wp] offset:14336\n\tds_read_b64_tr_b16 v[46:47], %[wp] offset:14848\n\ts_waitcnt lgkmcnt(10)\n\tv_mfma_f32_16x16x32_bf16 v[32:35], %[x9], v[48:51], v[32:35]\n\tds_read_b64_tr_b16 v[48:49], %[wp] offset:15360\n\tds_read_b64_tr_b16 v[50:51], %[wp] offset:15872\n\ts_waitcnt lgkmcnt(10)\n\tv_mfma_f32_16x16x32_bf16 v[32:35], %[x10], v[52:55], v[32:35]\n\ts_waitcnt lgkmcnt(8)\n\tv_mfma_f32_16x16x32_bf16 v[32:35], %[x11], v[56:59], v[32:35]\n\ts_waitcnt lgkmcnt(6)\n\tv_mfma_f32_16x16x32_bf16 v[32:35], %[x12], v[36:39], v[32:35]\n\ts_waitcnt lgkmcnt(4)\n\tv_mfma_f32_16x16x32_bf16 v[32:35], %[x13], v[40:43], v[32:35]\n\ts_waitcnt lgkmcnt(2)\n\tv_mfma_f32_16x16x32_bf16 v[32:35], %[x14], v[44:47], v[32:35]\n\ts_waitcnt lgkmcnt(0)\n\tv_mfma_f32_16x16x32_bf16 v[32:35], %[x15], v[48:51], v[32:35]\n\ts_nop 7\n\ts_nop 1" : "=&{v[32:35]}"(acc) : [wp] "v"(wp), [x0] "v"(xf[0]), [x1] "v"(xf[1]), [x2] "v"(xf[2]), [x3] "v"(xf[3]), [x4] "v"(xf[4]), [x5] "v"(xf[5]), [x6] "v"(xf[6]), [x7] "v"(xf[7]), [x8] "v"(xf[8]), [x9] "v"(xf[9]), [x10] "v"(xf[10]), [x11] "v"(xf[11]), [x12] "v"(xf[12]), [x13] "v"(xf[13]), [x14] "v"(xf[14]), [x15] "v"(xf[15]) : "v36", "v37", "v38", "v39", "v40", "v41", "v42", "v43", "v44", "v45", "v46", "v47", "v48", "v49", "v50", "v51", "v52", "v53", "v54", "v55", "v56", "v57", "v58", "v59", "memory");
        unsigned long long pv = (unsigned long long)pk2(acc[0], acc[1]) | ((unsigned long long)pk2(acc[2], acc[3]) << 32);
        unsigned dst;
        if (type == 0) dst = WS_KF + (tb * 2 + (tg >> 1)) * 1024 + ((tg & 1) * 16 + l15 + 32 * (g4 & 1)) * 16 + (g4 >> 1) * 8;
        else           dst = WS_VF + (tb * 4 + tg) * 512 + (g4 >> 1) * 256 + l15 * 16 + (g4 & 1) * 8;
        __hip_atomic_store((GAS unsigned long long*)(ws + dst), pv, RLX_AGENT);
        lds_signal(L, L_C5, l);
        WAIT_VM0();
        ST(2); AP(2);
        const int kt = tb * 2 + (tg >> 1);
        const int p = (kt & 3) * 64 + (kt >> 2) * 4 + (tg & 1) * 2 + type;
        if (l < 8) __hip_atomic_store((GAS unsigned*)(ws + WS_F1 + l * 1024 + p * 4), tag, RLX_AGENT);
    } else if (w == 5) {
        const int type = hg & 1, tg = hg >> 1;
        const int wrow = 16 * (sg >> 1) + 8 * sjh + 4 * (sg & 1) + sq;
        const char* wsrc = (const char*)(wo + (size_t)(64 * (b & 7)) * 512 + 16 * (b >> 3));
        const unsigned wovo = wrow * 1024 + (l & 1) * 16;
        dma16(wovo, wsrc, LB + L_WOST);
        dma16(wovo, wsrc + 32 * 1024, LB + L_WOST + 1024);
        u32x4 af[16];
        if (type == 0) {
            const u16* ap = ((l & 1) ? gate_w : w_r) + 8 * g4;
            #pragma unroll
            for (int s = 0; s < 16; ++s) af[s] = *(const GAS u32x4*)(ap + 32 * s);
        }
        asm volatile("s_barrier" ::: "memory");
        {
            u32x4 sent = {0xFFFFFFFFu, 0xFFFFFFFFu, 0xFFFFFFFFu, 0xFFFFFFFFu};
            #pragma unroll
            for (int i = 0; i < 8; ++i)
                __builtin_amdgcn_raw_buffer_store_b128(sent, rs, l * 16, WS_OF + ((tb * 2 + (i >> 2)) * 32 + 4 * hg + (i & 3)) * 1024, 16);
        }
        WAIT_VM0();
        #pragma unroll
        for (int i = 0; i < 2; ++i) {
            u32x4 f = tr_frag(L + L_WOST + i * 1024 + 8 * l);
            __builtin_amdgcn_raw_buffer_store_b128(f, rs, l * 16, WS_WOF + (2 * b + i) * 1024, 16);
        }
        lds_signal(L, L_C5, l);
        tag = get_tag(L);
        unsigned key = 0;
        if (type == 0) {
            lds_wait_ge(L, L_C2, 4u);
            f32x4 acc = {0.f, 0.f, 0.f, 0.f};
            LAS char* xp = L + L_XT + (tg * 16 + l15) * 1040 + g4 * 16;
            u32x4 bx[16];
            #pragma unroll
            for (int s = 0; s < 16; ++s) bx[s] = *(LAS u32x4*)(xp + s * 64);
            __builtin_amdgcn_sched_barrier(0);
            #pragma unroll
            for (int s = 0; s < 16; ++s) acc = mfma16(af[s], bx[s], acc);
            const int tok = tb * 64 + tg * 16 + l15;
            float gt = bfr(sigmoidf_(bfr(acc[1])));
            if (l < 16) __hip_atomic_store((GAS unsigned*)(ws + WS_GATE) + tok, __builtin_bit_cast(unsigned, gt), RLX_AGENT);
            unsigned u = pk2(acc[0], acc[0]) & 0xffffu;
            unsigned ord = (u & 0x8000u) ? (~u & 0xffffu) : (u | 0x8000u);
            if ((u & 0x7fffu) > 0x7f80u) ord = 0u;
            key = (l < 16) ? ((ord << 16) | (unsigned)(2047 - tok)) : 0u;
            #pragma unroll
            for (int o = 1; o < 16; o <<= 1) { unsigned k2 = __shfl_xor(key, o); key = key > k2 ? key : k2; }
            key = __builtin_amdgcn_readfirstlane(key);
        }
        WAIT_VM0();
        if (l < 8) __hip_atomic_store((GAS unsigned long long*)(ws + WS_F2 + l * 2048 + b * 8), ((unsigned long long)tag << 32) | key, RLX_AGENT);
        ST(1);
    } else {
        if (w == 0 && l < 16) *(LAS unsigned*)(L + L_MISC + 4 * l) = 0u;
        BARRIER();
        if (w < 4) {
            #pragma unroll 4
            for (int i = 0; i < 16; ++i) {
                const int row = 16 * w + i;
                dma16(l * 16, (const char*)x + (size_t)(tb * 64 + row) * 1024, LB + L_XT + row * 1040);
            }
            const unsigned qvo = (l >> 3) * 1024 + (((l & 7) ^ (((l >> 4) & 1) << 2)) * 16);
            #pragma unroll 4
            for (int i = 0; i < 16; ++i) {
                const int blk = 16 * w + i;
                dma16(qvo, (const char*)wq + (size_t)blk * 8192 + hg * 128, LB + L_WQ + blk * 1024);
            }
            WAIT_VM0();
            ST(1); AP(1);
            lds_signal(L, L_C2, l);
            lds_wait_ge(L, L_C2, 4u);
            ST(2);
            const int cp = w & 1, th = w >> 1, h = l >> 5, nhi = g4 & 1, q = (l >> 2) & 3, p = l & 3;
            const unsigned ap = LB + L_WQ + h * 1024 + q * 128 + ((64 * cp + 32 * nhi + 8 * p) ^ (((q >> 1) & 1) << 6));
            const unsigned bp = LB + L_XT + (32 * th + (l & 31)) * 1040 + h * 16;
            f32x16 acc;
            asm volatile("ds_read_b64_tr_b16 v[100:101], %[ap] offset:0\n\tds_read_b64_tr_b16 v[102:103], %[ap] offset:512\n\tds_read_b128 v[104:107], %[bp] offset:0\n\tds_read_b64_tr_b16 v[108:109], %[ap] offset:2048\n\tds_read_b64_tr_b16 v[110:111], %[ap] offset:2560\n\tds_read_b128 v[112:115], %[bp] offset:32\n\tds_read_b64_tr_b16 v[116:117], %[ap] offset:4096\n\tds_read_b64_tr_b16 v[118:119], %[ap] offset:4608\n\tds_read_b128 v[120:123], %[bp] offset:64\n\tds_read_b64_tr_b16 v[124:125], %[ap] offset:6144\n\tds_read_b64_tr_b16 v[126:127], %[ap] offset:6656\n\tds_read_b128 v[128:131], %[bp] offset:96\n\ts_waitcnt lgkmcnt(9)\n\tv_mfma_f32_32x32x16_bf16 v[32:47], v[100:103], v[104:107], 0\n\tds_read_b64_tr_b16 v[100:101], %[ap] offset:8192\n\tds_read_b64_tr_b16 v[102:103], %[ap] offset:8704\n\tds_read_b128 v[104:107], %[bp] offset:128\n\ts_waitcnt lgkmcnt(9)\n\tv_mfma_f32_32x32x16_bf16 v[32:47], v[108:111], v[112:115], v[32:47]\n\tds_read_b64_tr_b16 v[108:109], %[ap] offset:10240\n\tds_read_b64_tr_b16 v[110:111], %[ap] offset:10752\n\tds_read_b128 v[112:115], %[bp] offset:160\n\ts_waitcnt lgkmcnt(9)\n\tv_mfma_f32_32x32x16_bf16 v[32:47], v[116:119], v[120:123], v[32:47]\n\tds_read_b64_tr_b16 v[116:117], %[ap] offset:12288\n\tds_read_b64_tr_b16 v[118:119], %[ap] offset:12800\n\tds_read_b128 v[120:123], %[bp] offset:192\n\ts_waitcnt lgkmcnt(9)\n\tv_mfma_f32_32x32x16_bf16 v[32:47], v[124:127], v[128:131], v[32:47]\n\tds_read_b64_tr_b16 v[124:125], %[ap] offset:14336\n\tds_read_b64_tr_b16 v[126:127], %[ap] offset:14848\n\tds_read_b128 v[128:131], %[bp] offset:224\n\ts_waitcnt lgkmcnt(9)\n\tv_mfma_f32_32x32x16_bf16 v[32:47], v[100:103], v[104:107], v[32:47]\n\tds_read_b64_tr_b16 v[100:101], %[ap] offset:16384\n\tds_read_b64_tr_b16 v[102:103], %[ap] offset:16896\n\tds_read_b128 v[104:107], %[bp] offset:256\n\ts_waitcnt lgkmcnt(9)\n\tv_mfma_f32_32x32x16_bf16 v[32:47], v[108:111], v[112:115], v[32:47]\n\tds_read_b64_tr_b16 v[108:109], %[ap] offset:18432\n\tds_read_b64_tr_b16 v[110:111], %[ap] offset:18944\n\tds_read_b128 v[112:115], %[bp] offset:288\n\ts_waitcnt lgkmcnt(9)\n\tv_mfma_f32_32x32x16_bf16 v[32:47], v[116:119], v[120:123], v[32:47]\n\tds_read_b64_tr_b16 v[116:117], %[ap] offset:20480\n\tds_read_b64_tr_b16 v[118:119], %[ap] offset:20992\n\tds_read_b128 v[120:123], %[bp] offset:320\n\ts_waitcnt lgkmcnt(9)\n\tv_mfma_f32_32x32x16_bf16 v[32:47], v[124:127], v[128:131], v[32:47]\n\tds_read_b64_tr_b16 v[124:125], %[ap] offset:22528\n\tds_read_b64_tr_b16 v[126:127], %[ap] offset:23040\n\tds_read_b128 v[128:131], %[bp] offset:352\n\ts_waitcnt lgkmcnt(9)\n\tv_mfma_f32_32x32x16_bf16 v[32:47], v[100:103], v[104:107], v[32:47]\n\tds_read_b64_tr_b16 v[100:101], %[ap] offset:24576\n\tds_read_b64_tr_b16 v[102:103], %[ap] offset:25088\n\tds_read_b128 v[104:107], %[bp] offset:384\n\ts_waitcnt lgkmcnt(9)\n\tv_mfma_f32_32x32x16_bf16 v[32:47], v[108:111], v[112:115], v[32:47]\n\tds_read_b64_tr_b16 v[108:109], %[ap] offset:26624\n\tds_read_b64_tr_b16 v[110:111], %[ap] offset:27136\n\tds_read_b128 v[112:115], %[bp] offset:416\n\ts_waitcnt lgkmcnt(9)\n\tv_mfma_f32_32x32x16_bf16 v[32:47], v[116:119], v[120:123], v[32:47]\n\tds_read_b64_tr_b16 v[116:117], %[ap] offset:28672\n\tds_read_b64_tr_b16 v[118:119], %[ap] offset:29184\n\tds_read_b128 v[120:123], %[bp] offset:448\n\ts_waitcnt lgkmcnt(9)\n\tv_mfma_f32_32x32x16_bf16 v[32:47], v[124:127], v[128:131], v[32:47]\n\tds_read_b64_tr_b16 v[124:125], %[ap] offset:30720\n\tds_read_b64_tr_b16 v[126:127], %[ap] offset:31232\n\tds_read_b128 v[128:131], %[bp] offset:480\n\ts_waitcnt lgkmcnt(9)\n\tv_mfma_f32_32x32x16_bf16 v[32:47], v[100:103], v[104:107], v[32:47]\n\tds_read_b64_tr_b16 v[100:101], %[ap] offset:32768\n\tds_read_b64_tr_b16 v[102:103], %[ap] offset:33280\n\tds_read_b128 v[104:107], %[bp] offset:512\n\ts_waitcnt lgkmcnt(9)\n\tv_mfma_f32_32x32x16_bf16 v[32:47], v[108:111], v[112:115], v[32:47]\n\tds_read_b64_tr_b16 v[108:109], %[ap] offset:34816\n\tds_read_b64_tr_b16 v[110:111], %[ap] offset:35328\n\tds_read_b128 v[112:115], %[bp] offset:544\n\ts_waitcnt lgkmcnt(9)\n\tv_mfma_f32_32x32x16_bf16 v[32:47], v[116:119], v[120:123], v[32:47]\n\tds_read_b64_tr_b16 v[116:117], %[ap] offset:36864\n\tds_read_b64_tr_b16 v[118:119], %[ap] offset:37376\n\tds_read_b128 v[120:123], %[bp] offset:576\n\ts_waitcnt lgkmcnt(9)\n\tv_mfma_f32_32x32x16_bf16 v[32:47], v[124:127], v[128:131], v[32:47]\n\tds_read_b64_tr_b16 v[124:125], %[ap] offset:38912\n\tds_read_b64_tr_b16 v[126:127], %[ap] offset:39424\n\tds_read_b128 v[128:131], %[bp] offset:608\n\ts_waitcnt lgkmcnt(9)\n\tv_mfma_f32_32x32x16_bf16 v[32:47], v[100:103], v[104:107], v[32:47]\n\tds_read_b64_tr_b16 v[100:101], %[ap] offset:40960\n\tds_read_b64_tr_b16 v[102:103], %[ap] offset:41472\n\tds_read_b128 v[104:107], %[bp] offset:640\n\ts_waitcnt lgkmcnt(9)\n\tv_mfma_f32_32x32x16_bf16 v[32:47], v[108:111], v[112:115], v[32:47]\n\tds_read_b64_tr_b16 v[108:109], %[ap] offset:43008\n\tds_read_b64_tr_b16 v[110:111], %[ap] offset:43520\n\tds_read_b128 v[112:115], %[bp] offset:672\n\ts_waitcnt lgkmcnt(9)\n\tv_mfma_f32_32x32x16_bf16 v[32:47], v[116:119], v[120:123], v[32:47]\n\tds_read_b64_tr_b16 v[116:117], %[ap] offset:45056\n\tds_read_b64_tr_b16 v[118:119], %[ap] offset:45568\n\tds_read_b128 v[120:123], %[bp] offset:704\n\ts_waitcnt lgkmcnt(9)\n\tv_mfma_f32_32x32x16_bf16 v[32:47], v[124:127], v[128:131], v[32:47]\n\tds_read_b64_tr_b16 v[124:125], %[ap] offset:47104\n\tds_read_b64_tr_b16 v[126:127], %[ap] offset:47616\n\tds_read_b128 v[128:131], %[bp] offset:736\n\ts_waitcnt lgkmcnt(9)\n\tv_mfma_f32_32x32x16_bf16 v[32:47], v[100:103], v[104:107], v[32:47]\n\tds_read_b64_tr_b16 v[100:101], %[ap] offset:49152\n\tds_read_b64_tr_b16 v[102:103], %[ap] offset:49664\n\tds_read_b128 v[104:107], %[bp] offset:768\n\ts_waitcnt lgkmcnt(9)\n\tv_mfma_f32_32x32x16_bf16 v[32:47], v[108:111], v[112:115], v[32:47]\n\tds_read_b64_tr_b16 v[108:109], %[ap] offset:51200\n\tds_read_b64_tr_b16 v[110:111], %[ap] offset:51712\n\tds_read_b128 v[112:115], %[bp] offset:800\n\ts_waitcnt lgkmcnt(9)\n\tv_mfma_f32_32x32x16_bf16 v[32:47], v[116:119], v[120:123], v[32:47]\n\tds_read_b64_tr_b16 v[116:117], %[ap] offset:53248\n\tds_read_b64_tr_b16 v[118:119], %[ap] offset:53760\n\tds_read_b128 v[120:123], %[bp] offset:832\n\ts_waitcnt lgkmcnt(9)\n\tv_mfma_f32_32x32x16_bf16 v[32:47], v[124:127], v[128:131], v[32:47]\n\tds_read_b64_tr_b16 v[124:125], %[ap] offset:55296\n\tds_read_b64_tr_b16 v[126:127], %[ap] offset:55808\n\tds_read_b128 v[128:131], %[bp] offset:864\n\ts_waitcnt lgkmcnt(9)\n\tv_mfma_f32_32x32x16_bf16 v[32:47], v[100:103], v[104:107], v[32:47]\n\tds_read_b64_tr_b16 v[100:101], %[ap] offset:57344\n\tds_read_b64_tr_b16 v[102:103], %[ap] offset:57856\n\tds_read_b128 v[104:107], %[bp] offset:896\n\ts_waitcnt lgkmcnt(9)\n\tv_mfma_f32_32x32x16_bf16 v[32:47], v[108:111], v[112:115], v[32:47]\n\tds_read_b64_tr_b16 v[108:109], %[ap] offset:59392\n\tds_read_b64_tr_b16 v[110:111], %[ap] offset:59904\n\tds_read_b128 v[112:115], %[bp] offset:928\n\ts_waitcnt lgkmcnt(9)\n\tv_mfma_f32_32x32x16_bf16 v[32:47], v[116:119], v[120:123], v[32:47]\n\tds_read_b64_tr_b16 v[116:117], %[ap] offset:61440\n\tds_read_b64_tr_b16 v[118:119], %[ap] offset:61952\n\tds_read_b128 v[120:123], %[bp] offset:960\n\ts_waitcnt lgkmcnt(9)\n\tv_mfma_f32_32x32x16_bf16 v[32:47], v[124:127], v[128:131], v[32:47]\n\tds_read_b64_tr_b16 v[124:125], %[ap] offset:63488\n\tds_read_b64_tr_b16 v[126:127], %[ap] offset:64000\n\tds_read_b128 v[128:131], %[bp] offset:992\n\ts_waitcnt lgkmcnt(9)\n\tv_mfma_f32_32x32x16_bf16 v[32:47], v[100:103], v[104:107], v[32:47]\n\ts_waitcnt lgkmcnt(6)\n\tv_mfma_f32_32x32x16_bf16 v[32:47], v[108:111], v[112:115], v[32:47]\n\ts_waitcnt lgkmcnt(3)\n\tv_mfma_f32_32x32x16_bf16 v[32:47], v[116:119], v[120:123], v[32:47]\n\ts_waitcnt lgkmcnt(0)\n\tv_mfma_f32_32x32x16_bf16 v[32:47], v[124:127], v[128:131], v[32:47]\n\ts_nop 15\n\ts_nop 3" : "=&{v[32:47]}"(acc) : [ap] "v"(ap), [bp] "v"(bp) : "v100", "v101", "v102", "v103", "v104", "v105", "v106", "v107", "v108", "v109", "v110", "v111", "v112", "v113", "v114", "v115", "v116", "v117", "v118", "v119", "v120", "v121", "v122", "v123", "v124", "v125", "v126", "v127", "v128", "v129", "v130", "v131", "memory");
            const float sc = 0.25f * 1.4426950408889634f;
            u32x4 qo;
            #pragma unroll
            for (int i = 0; i < 4; ++i) { qf[i] = pk2(acc[2 * i] * sc, acc[2 * i + 1] * sc); qo[i] = pk2(acc[8 + 2 * i] * sc, acc[9 + 2 * i] * sc); }
            *(LAS u32x4*)(L + L_QX + w * 1024 + l * 16) = qo;
            ST(3); AP(3);
        }
    }

    const int rep = b & 7;
    if (w >= 4) {
        const int bw = w - 4;
        if (w >= 6) tag = get_tag(L);
        {
            GAS unsigned* fp = (GAS unsigned*)(ws + WS_F1 + rep * 1024) + bw * 64 + l;
            _Pragma("nounroll") for (int i = 0; i < SPIN_CAP; ++i) {
                unsigned v = __hip_atomic_load(fp, RLX_AGENT);
                if (__all(v == tag)) break;
                __builtin_amdgcn_s_sleep(1);
            }
            FENCE();
        }
        ST(4); AP(4);
        u32x4 kf[16], vf[16];
        #pragma unroll
        for (int i = 0; i < 16; ++i) {
            kf[i] = __builtin_amdgcn_raw_buffer_load_b128(rs, l * 16, WS_KF + (4 * i + bw) * 1024, 16);
            vf[i] = __builtin_amdgcn_raw_buffer_load_b128(rs, l * 16, WS_VF + (4 * i + bw) * 1024, 16);
        }
        lds_wait_ge(L, L_C5, 2u);
        LAS char* kd = L + bw * REC + l * 16;
        LAS char* vd = L + bw * REC + 1024 + (l >> 5) * 528 + (l & 31) * 16;
        const u32x4 ones = {0x3f803f80u, 0x3f803f80u, 0x3f803f80u, 0x3f803f80u};
        LAS char* od = L + ((l >> 1) * 4 + bw) * REC + 1024 + (l & 1) * 528 + 512;
        #pragma unroll
        for (int i = 0; i < 3; ++i) { *(LAS u32x4*)(kd + i * 4 * REC) = kf[i]; *(LAS u32x4*)(vd + i * 4 * REC) = vf[i]; }
        if (l < 6) *(LAS u32x4*)od = ones;
        ST(5); AP(5);
        BARRIER();
        ST(6); AP(6);
        qf = *(LAS u32x4*)(L + L_QX + bw * 1024 + l * 16);
        #pragma unroll
        for (int i = 3; i < 16; ++i) { *(LAS u32x4*)(kd + i * 4 * REC) = kf[i]; *(LAS u32x4*)(vd + i * 4 * REC) = vf[i]; }
        if (l >= 6 && l < 32) *(LAS u32x4*)od = ones;
        lds_signal(L, L_C4, l);
        if (b == 0 && w == 4 && l == 0) __hip_atomic_store((GAS unsigned*)(ws + WS_EPOCH), tag, RLX_AGENT);
    } else {
        BARRIER();
        ST(6); AP(6);
        tag = get_tag(L);
    }

    const int ft = xcd * 28 + j;
    if (j < 28) {
        const u16* wsrc = ((l & 1) ? w_up : w_gate) + (size_t)krow * 1792 + 8 * ft;
        #pragma unroll
        for (int i = 0; i < 2; ++i) { const int s = 2 * w + i; dma16p(wsrc + (size_t)s * 32 * 1792, LB + L_FW1 + s * 1024); }
    }
    if (w < 7) dma16(krow * 1024 + (l & 1) * 16, (const char*)(w_down + (size_t)(224 * xcd + 32 * w) * 512 + 16 * j), LB + L_FW2 + w * 1024);

    const int hl = 2 * (w & 1) + (w >> 2), tha = (w >> 1) & 1;
    unsigned kp = LB + l * 16;
    unsigned vp = LB + 1024 + ((l & 31) < 16 ? (l >> 5) * 256 + (l & 31) * 16 : 512);
    const int idxv = (int)0xDDDDDDDD;
    f32x16 X, Y, acc; u32x4 KR, V0, V1;
    for (int i = 0; i < 16; ++i) acc[i] = 0.f;
    if (w >= 4) __builtin_amdgcn_s_setprio(1);
    ST(7); STC(30); AP(7);
    asm volatile("s_branch MQA_OVER\n\tMQA_BODY:\n\tv_exp_f32 v32, v32\n\tv_exp_f32 v33, v33\n\ts_waitcnt lgkmcnt(2)\n\tv_smfmac_f32_32x32x32_bf16 v[64:79], v[88:91], v[56:63], v96\n\tds_read_b128 v[88:91], v98 offset:528\n\tv_exp_f32 v34, v34\n\tv_exp_f32 v35, v35\n\tv_exp_f32 v36, v36\n\tv_exp_f32 v37, v37\n\ts_waitcnt lgkmcnt(2)\n\tv_mfma_f32_32x32x16_bf16 v[48:63], v[80:83], v[92:95], 0\n\tds_read_b128 v[80:83], v97 offset:4160\n\tv_exp_f32 v38, v38\n\tv_exp_f32 v39, v39\n\tv_exp_f32 v40, v40\n\tv_exp_f32 v41, v41\n\ts_waitcnt lgkmcnt(2)\n\tv_smfmac_f32_32x32x32_bf16 v[64:79], v[84:87], v[32:39], v96\n\tds_read_b128 v[84:87], v98 offset:2080\n\tv_exp_f32 v42, v42\n\tv_exp_f32 v43, v43\n\tv_exp_f32 v44, v44\n\tv_exp_f32 v45, v45\n\tv_exp_f32 v46, v46\n\tv_exp_f32 v47, v47\n\tv_exp_f32 v48, v48\n\tv_exp_f32 v49, v49\n\ts_waitcnt lgkmcnt(2)\n\tv_smfmac_f32_32x32x32_bf16 v[64:79], v[88:91], v[40:47], v96\n\tds_read_b128 v[88:91], v98 offset:2608\n\tv_exp_f32 v50, v50\n\tv_exp_f32 v51, v51\n\tv_exp_f32 v52, v52\n\tv_exp_f32 v53, v53\n\ts_waitcnt lgkmcnt(2)\n\tv_mfma_f32_32x32x16_bf16 v[32:47], v[80:83], v[92:95], 0\n\tds_read_b128 v[80:83], v97 offset:6240\n\tv_exp_f32 v54, v54\n\tv_exp_f32 v55, v55\n\tv_exp_f32 v56, v56\n\tv_exp_f32 v57, v57\n\ts_waitcnt lgkmcnt(2)\n\tv_smfmac_f32_32x32x32_bf16 v[64:79], v[84:87], v[48:55], v96\n\tds_read_b128 v[84:87], v98 offset:4160\n\tv_exp_f32 v58, v58\n\tv_exp_f32 v59, v59\n\tv_exp_f32 v60, v60\n\tv_exp_f32 v61, v61\n\tv_exp_f32 v62, v62\n\tv_exp_f32 v63, v63\n\tv_exp_f32 v32, v32\n\tv_exp_f32 v33, v33\n\ts_waitcnt lgkmcnt(2)\n\tv_smfmac_f32_32x32x32_bf16 v[64:79], v[88:91], v[56:63], v96\n\tds_read_b128 v[88:91], v98 offset:4688\n\tv_exp_f32 v34, v34\n\tv_exp_f32 v35, v35\n\tv_exp_f32 v36, v36\n\tv_exp_f32 v37, v37\n\ts_waitcnt lgkmcnt(2)\n\tv_mfma_f32_32x32x16_bf16 v[48:63], v[80:83], v[92:95], 0\n\tds_read_b128 v[80:83], v97 offset:8320\n\tv_exp_f32 v38, v38\n\tv_exp_f32 v39, v39\n\tv_exp_f32 v40, v40\n\tv_exp_f32 v41, v41\n\ts_waitcnt lgkmcnt(2)\n\tv_smfmac_f32_32x32x32_bf16 v[64:79], v[84:87], v[32:39], v96\n\tds_read_b128 v[84:87], v98 offset:6240\n\tv_exp_f32 v42, v42\n\tv_exp_f32 v43, v43\n\tv_exp_f32 v44, v44\n\tv_exp_f32 v45, v45\n\tv_exp_f32 v46, v46\n\tv_exp_f32 v47, v47\n\tv_exp_f32 v48, v48\n\tv_exp_f32 v49, v49\n\ts_waitcnt lgkmcnt(2)\n\tv_smfmac_f32_32x32x32_bf16 v[64:79], v[88:91], v[40:47], v96\n\tds_read_b128 v[88:91], v98 offset:6768\n\tv_exp_f32 v50, v50\n\tv_exp_f32 v51, v51\n\tv_exp_f32 v52, v52\n\tv_exp_f32 v53, v53\n\ts_waitcnt lgkmcnt(2)\n\tv_mfma_f32_32x32x16_bf16 v[32:47], v[80:83], v[92:95], 0\n\tds_read_b128 v[80:83], v97 offset:10400\n\tv_exp_f32 v54, v54\n\tv_exp_f32 v55, v55\n\tv_exp_f32 v56, v56\n\tv_exp_f32 v57, v57\n\ts_waitcnt lgkmcnt(2)\n\tv_smfmac_f32_32x32x32_bf16 v[64:79], v[84:87], v[48:55], v96\n\tds_read_b128 v[84:87], v98 offset:8320\n\tv_exp_f32 v58, v58\n\tv_exp_f32 v59, v59\n\tv_exp_f32 v60, v60\n\tv_exp_f32 v61, v61\n\tv_exp_f32 v62, v62\n\tv_exp_f32 v63, v63\n\tv_exp_f32 v32, v32\n\tv_exp_f32 v33, v33\n\ts_waitcnt lgkmcnt(2)\n\tv_smfmac_f32_32x32x32_bf16 v[64:79], v[88:91], v[56:63], v96\n\tds_read_b128 v[88:91], v98 offset:8848\n\tv_exp_f32 v34, v34\n\tv_exp_f32 v35, v35\n\tv_exp_f32 v36, v36\n\tv_exp_f32 v37, v37\n\ts_waitcnt lgkmcnt(2)\n\tv_mfma_f32_32x32x16_bf16 v[48:63], v[80:83], v[92:95], 0\n\tds_read_b128 v[80:83], v97 offset:12480\n\tv_exp_f32 v38, v38\n\tv_exp_f32 v39, v39\n\tv_exp_f32 v40, v40\n\tv_exp_f32 v41, v41\n\ts_waitcnt lgkmcnt(2)\n\tv_smfmac_f32_32x32x32_bf16 v[64:79], v[84:87], v[32:39], v96\n\tds_read_b128 v[84:87], v98 offset:10400\n\tv_exp_f32 v42, v42\n\tv_exp_f32 v43, v43\n\tv_exp_f32 v44, v44\n\tv_exp_f32 v45, v45\n\tv_exp_f32 v46, v46\n\tv_exp_f32 v47, v47\n\tv_exp_f32 v48, v48\n\tv_exp_f32 v49, v49\n\ts_waitcnt lgkmcnt(2)\n\tv_smfmac_f32_32x32x32_bf16 v[64:79], v[88:91], v[40:47], v96\n\tds_read_b128 v[88:91], v98 offset:10928\n\tv_exp_f32 v50, v50\n\tv_exp_f32 v51, v51\n\tv_exp_f32 v52, v52\n\tv_exp_f32 v53, v53\n\ts_waitcnt lgkmcnt(2)\n\tv_mfma_f32_32x32x16_bf16 v[32:47], v[80:83], v[92:95], 0\n\tds_read_b128 v[80:83], v97 offset:14560\n\tv_exp_f32 v54, v54\n\tv_exp_f32 v55, v55\n\tv_exp_f32 v56, v56\n\tv_exp_f32 v57, v57\n\ts_waitcnt lgkmcnt(2)\n\tv_smfmac_f32_32x32x32_bf16 v[64:79], v[84:87], v[48:55], v96\n\tds_read_b128 v[84:87], v98 offset:12480\n\tv_exp_f32 v58, v58\n\tv_exp_f32 v59, v59\n\tv_exp_f32 v60, v60\n\tv_exp_f32 v61, v61\n\tv_exp_f32 v62, v62\n\tv_exp_f32 v63, v63\n\tv_exp_f32 v32, v32\n\tv_exp_f32 v33, v33\n\ts_waitcnt lgkmcnt(2)\n\tv_smfmac_f32_32x32x32_bf16 v[64:79], v[88:91], v[56:63], v96\n\tds_read_b128 v[88:91], v98 offset:13008\n\tv_exp_f32 v34, v34\n\tv_exp_f32 v35, v35\n\tv_exp_f32 v36, v36\n\tv_exp_f32 v37, v37\n\ts_waitcnt lgkmcnt(2)\n\tv_mfma_f32_32x32x16_bf16 v[48:63], v[80:83], v[92:95], 0\n\tds_read_b128 v[80:83], v97 offset:16640\n\tv_exp_f32 v38, v38\n\tv_exp_f32 v39, v39\n\tv_exp_f32 v40, v40\n\tv_exp_f32 v41, v41\n\ts_waitcnt lgkmcnt(2)\n\tv_smfmac_f32_32x32x32_bf16 v[64:79], v[84:87], v[32:39], v96\n\tds_read_b128 v[84:87], v98 offset:14560\n\tv_exp_f32 v42, v42\n\tv_exp_f32 v43, v43\n\tv_exp_f32 v44, v44\n\tv_exp_f32 v45, v45\n\tv_exp_f32 v46, v46\n\tv_exp_f32 v47, v47\n\tv_exp_f32 v48, v48\n\tv_exp_f32 v49, v49\n\ts_waitcnt lgkmcnt(2)\n\tv_smfmac_f32_32x32x32_bf16 v[64:79], v[88:91], v[40:47], v96\n\tds_read_b128 v[88:91], v98 offset:15088\n\tv_exp_f32 v50, v50\n\tv_exp_f32 v51, v51\n\tv_exp_f32 v52, v52\n\tv_exp_f32 v53, v53\n\ts_waitcnt lgkmcnt(2)\n\tv_mfma_f32_32x32x16_bf16 v[32:47], v[80:83], v[92:95], 0\n\tds_read_b128 v[80:83], v97 offset:18720\n\tv_exp_f32 v54, v54\n\tv_exp_f32 v55, v55\n\tv_exp_f32 v56, v56\n\tv_exp_f32 v57, v57\n\ts_waitcnt lgkmcnt(2)\n\tv_smfmac_f32_32x32x32_bf16 v[64:79], v[84:87], v[48:55], v96\n\tds_read_b128 v[84:87], v98 offset:16640\n\tv_exp_f32 v58, v58\n\tv_exp_f32 v59, v59\n\tv_exp_f32 v60, v60\n\tv_exp_f32 v61, v61\n\tv_exp_f32 v62, v62\n\tv_exp_f32 v63, v63\n\tv_add_u32 v97, 16640, v97\n\tv_add_u32 v98, 16640, v98\n\ts_sub_u32 s78, s78, 1\n\ts_cmp_lg_u32 s78, 0\n\ts_cbranch_scc1 MQA_BODY\n\ts_waitcnt lgkmcnt(0)\n\ts_nop 7\n\ts_setpc_b64 s[76:77]\n\tMQA_OVER:\n\tds_read_b128 v[80:83], v97 offset:0\n\tds_read_b128 v[84:87], v98 offset:0\n\ts_waitcnt lgkmcnt(1)\n\tv_mfma_f32_32x32x16_bf16 v[32:47], v[80:83], v[92:95], 0\n\tds_read_b128 v[80:83], v97 offset:2080\n\tds_read_b128 v[88:91], v98 offset:528\n\tv_mov_b32 v56, 0\n\tv_mov_b32 v57, 0\n\tv_mov_b32 v58, 0\n\tv_mov_b32 v59, 0\n\tv_mov_b32 v60, 0\n\tv_mov_b32 v61, 0\n\tv_mov_b32 v62, 0\n\tv_mov_b32 v63, 0\n\tv_mov_b32 v48, 0\n\tv_mov_b32 v49, 0\n\tv_mov_b32 v50, 0\n\tv_mov_b32 v51, 0\n\tv_mov_b32 v52, 0\n\tv_mov_b32 v53, 0\n\tv_mov_b32 v54, 0\n\tv_mov_b32 v55, 0\n\ts_nop 7\n\ts_nop 3\n\ts_waitcnt lgkmcnt(0)"
                 : "={v[32:47]}"(X), "={v[48:63]}"(Y), "={v[80:83]}"(KR), "={v[84:87]}"(V0), "={v[88:91]}"(V1)
                 : "{v[92:95]}"(qf), "{v97}"(kp), "{v98}"(vp) : "memory");
#define SEG(N) asm volatile("s_mov_b32 s78, %[n]\n\ts_getpc_b64 s[76:77]\n\ts_add_u32 s76, s76, 12\n\ts_addc_u32 s77, s77, 0\n\ts_branch MQA_BODY" \
                     : "+{v[32:47]}"(X), "+{v[48:63]}"(Y), "+{v[64:79]}"(acc), "+{v[80:83]}"(KR), "+{v[84:87]}"(V0), "+{v[88:91]}"(V1), \
                       "+{v97}"(kp), "+{v98}"(vp) \
                     : [n] "n"(N), "{v[92:95]}"(qf), "{v96}"(idxv) : "s76", "s77", "s78", "scc", "memory")
    const int tho = hg >> 2, cg = hg & 3;
    const int n0 = cg * 128 + 16 * w;
    u32x4 fl0, fl1;
    if (w == 0) {
        fl0 = __builtin_amdgcn_raw_buffer_load_b128(rs, l * 16, WS_F2 + rep * 2048, 16);
        fl1 = __builtin_amdgcn_raw_buffer_load_b128(rs, l * 16, WS_F2 + rep * 2048 + 1024, 16);
    }
    SEG(1);
    ST(12); AP(12);
    WAIT_VM0();
    lds_signal(L, L_C7, l);
    if (w == 0) {
        _Pragma("nounroll") for (int i = 0; i < SPIN_CAP; ++i) {
            if (__all(fl0[1] == tag && fl0[3] == tag && fl1[1] == tag && fl1[3] == tag)) break;
            FENCE();
            fl0 = __builtin_amdgcn_raw_buffer_load_b128(rs, l * 16, WS_F2 + rep * 2048, 16);
            fl1 = __builtin_amdgcn_raw_buffer_load_b128(rs, l * 16, WS_F2 + rep * 2048 + 1024, 16);
        }
        unsigned key = max(max(fl0[0], fl0[2]), max(fl1[0], fl1[2]));
        #pragma unroll
        for (int o = 1; o < 64; o <<= 1) { unsigned k2 = __shfl_xor(key, o); key = key > k2 ? key : k2; }
        key = __builtin_amdgcn_readfirstlane(key);
        if (l == 0) *(LAS unsigned*)(L + L_KEY) = key;
        lds_signal(L, L_C6, l);
        const int idx = 2047 - (int)(key & 0x7ffu);
        dma16(l * 16, (const char*)x + (size_t)idx * 1024, LB + L_XSEL);
    }
    lds_wait_ge(L, L_C4, 4u);
    SEG(1);
    ST(13); AP(13);
    if (w == 0 && j < 28) {
        WAIT_VM0();
        lds_wait_ge(L, L_C7, 8u);
        f32x4 a = {0.f, 0.f, 0.f, 0.f};
        u32x4 fa[8], fb[8];
        #pragma unroll
        for (int hh = 0; hh < 2; ++hh) {
            #pragma unroll
            for (int s = 0; s < 8; ++s) {
                fa[s] = tr_frag(L + L_FW1 + (8 * hh + s) * 1024 + 8 * l);
                fb[s] = *(LAS u32x4*)(L + L_XSEL + (8 * hh + s) * 64 + g4 * 16);
            }
            __builtin_amdgcn_sched_barrier(0);
            #pragma unroll
            for (int s = 0; s < 8; ++s) a = mfma16(fa[s], fb[s], a);
            __builtin_amdgcn_sched_barrier(0);
        }
        float hv[4];
        #pragma unroll
        for (int i = 0; i < 4; ++i) {
            float u = bfr(__shfl_xor(a[i], 32));
            float gq = bfr(a[i]);
            hv[i] = bfr(gq * sigmoidf_(gq)) * u;
        }
        u32x4 gr = {pk2(hv[0], hv[1]), tag, pk2(hv[2], hv[3]), tag};
        if (l == 0 || l == 16) __builtin_amdgcn_raw_buffer_store_b128(gr, rs, g4 * 16, WS_HG + ft * 32, 16);
    }
    SEG(1);
    ST(14); AP(14);
    lds_wait_ge(L, L_C6, 1u);
    const unsigned rkey = __builtin_amdgcn_readfirstlane(*(LAS unsigned*)(L + L_KEY));
    if (w == 1) fl0 = __builtin_amdgcn_raw_buffer_load_b128(rs, (l < 56 ? l : 55) * 16, WS_HG + xcd * 896, 16);
    u32x4 wof[16];
    u32x2 xres[2];
    float gres[2];
    #pragma unroll
    for (int s = 0; s < 16; ++s)
        wof[s] = __builtin_amdgcn_raw_buffer_load_b128(rs, l * 16, WS_WOF + ((cg * 8 + w) * 16 + s) * 1024, 16);
    #pragma unroll
    for (int t = 0; t < 2; ++t) {
        const int tok = tb * 64 + tho * 32 + 16 * t + l15;
        xres[t] = *(const GAS u32x2*)(x + (size_t)tok * 512 + n0 + 4 * g4);
        gres[t] = u2f(__hip_atomic_load((GAS unsigned*)(ws + WS_GATE) + tok, RLX_AGENT));
    }
    SEG(1);
    ST(15); AP(15);
    if (w == 1) {
        _Pragma("nounroll") for (int i = 0; i < SPIN_CAP; ++i) {
            if (__all(fl0[1] == tag && fl0[3] == tag)) break;
            FENCE();
            fl0 = __builtin_amdgcn_raw_buffer_load_b128(rs, (l < 56 ? l : 55) * 16, WS_HG + xcd * 896, 16);
        }
        u32x2 hv = {fl0[0], fl0[2]};
        if (l < 56) *(LAS u32x2*)(L + L_HBUF + l * 8) = hv;
        WAIT_LGKM0();
        f32x4 a = {0.f, 0.f, 0.f, 0.f};
        u32x4 fa[7], fb[7];
        #pragma unroll
        for (int s = 0; s < 7; ++s) {
            fa[s] = tr_frag(L + L_FW2 + s * 1024 + 8 * l);
            fb[s] = *(LAS u32x4*)(L + L_HBUF + s * 64 + g4 * 16);
        }
        __builtin_amdgcn_sched_barrier(0);
        #pragma unroll
        for (int s = 0; s < 7; ++s) a = mfma16(fa[s], fb[s], a);
        u32x4 g0 = {f2u(a[0]), tag, f2u(a[1]), tag};
        u32x4 g1 = {f2u(a[2]), tag, f2u(a[3]), tag};
        if (l15 == 0) {
            __builtin_amdgcn_raw_buffer_store_b128(g0, rs, g4 * 32, WS_YG + (xcd * 512 + 16 * j) * 8, 16);
            __builtin_amdgcn_raw_buffer_store_b128(g1, rs, g4 * 32 + 16, WS_YG + (xcd * 512 + 16 * j) * 8, 16);
        }
    }
    SEG(PRIO_N);
    ST(16); AP(16);
    __builtin_amdgcn_s_setprio(0);
    SEG(4 - PRIO_N);
    asm volatile("s_nop 1\n\tv_smfmac_f32_32x32x32_bf16 v[64:79], v[88:91], v[56:63], v96\n\ts_nop 15\n\ts_nop 7" : "+{v[64:79]}"(acc) : "{v[48:63]}"(Y), "{v[88:91]}"(V1), "{v96}"(idxv) : "memory");
    __builtin_amdgcn_s_setprio(0);
    ST(8); STC(31); AP(8);

    {
        const float ls = acc[8];
        bool ok = ls >= 8.6736174e-19f && ls <= 1.2676506e30f;
        #pragma unroll
        for (int i = 0; i < 8; ++i) ok = ok && (__builtin_fabsf(acc[i]) < __builtin_inff());
        if (!__all(ok)) {
            LAS char* kq = L + l * 16;
            LAS char* vq = L + 1024 + ((l & 31) < 16 ? (l >> 5) * 256 + (l & 31) * 16 : 512);
            acc = attn_true_max(kq, vq, qf);
        }
    }
    {
        const float inv = 1.f / acc[8];
        u32x4 o;
        #pragma unroll
        for (int i = 0; i < 4; ++i) { unsigned u = pk2(acc[2 * i] * inv, acc[2 * i + 1] * inv); o[i] = u < 0xFFFFFFFEu ? u : 0xFFFFFFFEu; }
        __builtin_amdgcn_raw_buffer_store_b128(o, rs, l * 16, WS_OF + ((tb * 2 + tha) * 32 + 4 * hg + hl) * 1024, 16);
    }
    const int idx = 2047 - (int)(rkey & 0x7ffu);
    const bool has_idx = (idx >> 5) == tb * 2 + tho;
    u32x4 yg = {0u, tag, 0u, tag};
    const unsigned yoff = ((l >> 3) * 512 + n0 + 2 * (l & 7)) * 8;
    if (has_idx) yg = __builtin_amdgcn_raw_buffer_load_b128(rs, yoff, WS_YG, 16);
    BARRIER();
    ST(9); AP(9);
    {
        u32x4 f[4];
        const unsigned src = WS_OF + ((tb * 2 + tho) * 32 + 4 * w) * 1024;
        _Pragma("nounroll") for (int i = 0; i < SPIN_CAP; ++i) {
            bool ok = true;
            #pragma unroll
            for (int k = 0; k < 4; ++k) {
                f[k] = __builtin_amdgcn_raw_buffer_load_b128(rs, l * 16, src + k * 1024, 16);
                ok = ok && f[k][0] != 0xFFFFFFFFu && f[k][1] != 0xFFFFFFFFu && f[k][2] != 0xFFFFFFFFu && f[k][3] != 0xFFFFFFFFu;
            }
            if (__all(ok)) break;
            FENCE();
        }
        #pragma unroll
        for (int k = 0; k < 4; ++k) *(LAS u32x4*)(L + (4 * w + k) * 1024 + l * 16) = f[k];
    }
    ST(10); AP(10);
    BARRIER();
    f32x4 oa[2];
    {
        const unsigned op = LB + (g4 >> 1) * 1024 + (g4 & 1) * 512 + l15 * 16;
        asm volatile("ds_read_b128 v[40:43], %[op] offset:0\n\tds_read_b128 v[44:47], %[op] offset:256\n\tds_read_b128 v[48:51], %[op] offset:2048\n\tds_read_b128 v[52:55], %[op] offset:2304\n\tds_read_b128 v[56:59], %[op] offset:4096\n\tds_read_b128 v[60:63], %[op] offset:4352\n\tds_read_b128 v[64:67], %[op] offset:6144\n\tds_read_b128 v[68:71], %[op] offset:6400\n\tds_read_b128 v[72:75], %[op] offset:8192\n\tds_read_b128 v[76:79], %[op] offset:8448\n\tds_read_b128 v[80:83], %[op] offset:10240\n\tds_read_b128 v[84:87], %[op] offset:10496\n\ts_waitcnt lgkmcnt(11)\n\tv_mfma_f32_16x16x32_bf16 v[32:35], %[w0], v[40:43], 0\n\ts_waitcnt lgkmcnt(10)\n\tv_mfma_f32_16x16x32_bf16 v[36:39], %[w0], v[44:47], 0\n\tds_read_b128 v[40:43], %[op] offset:12288\n\tds_read_b128 v[44:47], %[op] offset:12544\n\ts_waitcnt lgkmcnt(11)\n\tv_mfma_f32_16x16x32_bf16 v[32:35], %[w1], v[48:51], v[32:35]\n\ts_waitcnt lgkmcnt(10)\n\tv_mfma_f32_16x16x32_bf16 v[36:39], %[w1], v[52:55], v[36:39]\n\tds_read_b128 v[48:51], %[op] offset:14336\n\tds_read_b128 v[52:55], %[op] offset:14592\n\ts_waitcnt lgkmcnt(11)\n\tv_mfma_f32_16x16x32_bf16 v[32:35], %[w2], v[56:59], v[32:35]\n\ts_waitcnt lgkmcnt(10)\n\tv_mfma_f32_16x16x32_bf16 v[36:39], %[w2], v[60:63], v[36:39]\n\tds_read_b128 v[56:59], %[op] offset:16384\n\tds_read_b128 v[60:63], %[op] offset:16640\n\ts_waitcnt lgkmcnt(11)\n\tv_mfma_f32_16x16x32_bf16 v[32:35], %[w3], v[64:67], v[32:35]\n\ts_waitcnt lgkmcnt(10)\n\tv_mfma_f32_16x16x32_bf16 v[36:39], %[w3], v[68:71], v[36:39]\n\tds_read_b128 v[64:67], %[op] offset:18432\n\tds_read_b128 v[68:71], %[op] offset:18688\n\ts_waitcnt lgkmcnt(11)\n\tv_mfma_f32_16x16x32_bf16 v[32:35], %[w4], v[72:75], v[32:35]\n\ts_waitcnt lgkmcnt(10)\n\tv_mfma_f32_16x16x32_bf16 v[36:39], %[w4], v[76:79], v[36:39]\n\tds_read_b128 v[72:75], %[op] offset:20480\n\tds_read_b128 v[76:79], %[op] offset:20736\n\ts_waitcnt lgkmcnt(11)\n\tv_mfma_f32_16x16x32_bf16 v[32:35], %[w5], v[80:83], v[32:35]\n\ts_waitcnt lgkmcnt(10)\n\tv_mfma_f32_16x16x32_bf16 v[36:39], %[w5], v[84:87], v[36:39]\n\tds_read_b128 v[80:83], %[op] offset:22528\n\tds_read_b128 v[84:87], %[op] offset:22784\n\ts_waitcnt lgkmcnt(11)\n\tv_mfma_f32_16x16x32_bf16 v[32:35], %[w6], v[40:43], v[32:35]\n\ts_waitcnt lgkmcnt(10)\n\tv_mfma_f32_16x16x32_bf16 v[36:39], %[w6], v[44:47], v[36:39]\n\tds_read_b128 v[40:43], %[op] offset:24576\n\tds_read_b128 v[44:47], %[op] offset:24832\n\ts_waitcnt lgkmcnt(11)\n\tv_mfma_f32_16x16x32_bf16 v[32:35], %[w7], v[48:51], v[32:35]\n\ts_waitcnt lgkmcnt(10)\n\tv_mfma_f32_16x16x32_bf16 v[36:39], %[w7], v[52:55], v[36:39]\n\tds_read_b128 v[48:51], %[op] offset:26624\n\tds_read_b128 v[52:55], %[op] offset:26880\n\ts_waitcnt lgkmcnt(11)\n\tv_mfma_f32_16x16x32_bf16 v[32:35], %[w8], v[56:59], v[32:35]\n\ts_waitcnt lgkmcnt(10)\n\tv_mfma_f32_16x16x32_bf16 v[36:39], %[w8], v[60:63], v[36:39]\n\tds_read_b128 v[56:59], %[op] offset:28672\n\tds_read_b128 v[60:63], %[op] offset:28928\n\ts_waitcnt lgkmcnt(11)\n\tv_mfma_f32_16x16x32_bf16 v[32:35], %[w9], v[64:67], v[32:35]\n\ts_waitcnt lgkmcnt(10)\n\tv_mfma_f32_16x16x32_bf16 v[36:39], %[w9], v[68:71], v[36:39]\n\tds_read_b128 v[64:67], %[op] offset:30720\n\tds_read_b128 v[68:71], %[op] offset:30976\n\ts_waitcnt lgkmcnt(11)\n\tv_mfma_f32_16x16x32_bf16 v[32:35], %[w10], v[72:75], v[32:35]\n\ts_waitcnt lgkmcnt(10)\n\tv_mfma_f32_16x16x32_bf16 v[36:39], %[w10], v[76:79], v[36:39]\n\ts_waitcnt lgkmcnt(9)\n\tv_mfma_f32_16x16x32_bf16 v[32:35], %[w11], v[80:83], v[32:35]\n\ts_waitcnt lgkmcnt(8)\n\tv_mfma_f32_16x16x32_bf16 v[36:39], %[w11], v[84:87], v[36:39]\n\ts_waitcnt lgkmcnt(7)\n\tv_mfma_f32_16x16x32_bf16 v[32:35], %[w12], v[40:43], v[32:35]\n\ts_waitcnt lgkmcnt(6)\n\tv_mfma_f32_16x16x32_bf16 v[36:39], %[w12], v[44:47], v[36:39]\n\ts_waitcnt lgkmcnt(5)\n\tv_mfma_f32_16x16x32_bf16 v[32:35], %[w13], v[48:51], v[32:35]\n\ts_waitcnt lgkmcnt(4)\n\tv_mfma_f32_16x16x32_bf16 v[36:39], %[w13], v[52:55], v[36:39]\n\ts_waitcnt lgkmcnt(3)\n\tv_mfma_f32_16x16x32_bf16 v[32:35], %[w14], v[56:59], v[32:35]\n\ts_waitcnt lgkmcnt(2)\n\tv_mfma_f32_16x16x32_bf16 v[36:39], %[w14], v[60:63], v[36:39]\n\ts_waitcnt lgkmcnt(1)\n\tv_mfma_f32_16x16x32_bf16 v[32:35], %[w15], v[64:67], v[32:35]\n\ts_waitcnt lgkmcnt(0)\n\tv_mfma_f32_16x16x32_bf16 v[36:39], %[w15], v[68:71], v[36:39]\n\ts_nop 7\n\ts_nop 1" : "=&{v[32:35]}"(oa[0]), "=&{v[36:39]}"(oa[1]) : [op] "v"(op), [w0] "v"(wof[0]), [w1] "v"(wof[1]), [w2] "v"(wof[2]), [w3] "v"(wof[3]), [w4] "v"(wof[4]), [w5] "v"(wof[5]), [w6] "v"(wof[6]), [w7] "v"(wof[7]), [w8] "v"(wof[8]), [w9] "v"(wof[9]), [w10] "v"(wof[10]), [w11] "v"(wof[11]), [w12] "v"(wof[12]), [w13] "v"(wof[13]), [w14] "v"(wof[14]), [w15] "v"(wof[15]) : "v40", "v41", "v42", "v43", "v44", "v45", "v46", "v47", "v48", "v49", "v50", "v51", "v52", "v53", "v54", "v55", "v56", "v57", "v58", "v59", "v60", "v61", "v62", "v63", "v64", "v65", "v66", "v67", "v68", "v69", "v70", "v71", "v72", "v73", "v74", "v75", "v76", "v77", "v78", "v79", "v80", "v81", "v82", "v83", "v84", "v85", "v86", "v87", "memory");
    }
    float yv[4] = {0.f, 0.f, 0.f, 0.f};
    if (has_idx) {
        _Pragma("nounroll") for (int i = 0; i < SPIN_CAP; ++i) {
            if (__all(yg[1] == tag && yg[3] == tag)) break;
            FENCE();
            yg = __builtin_amdgcn_raw_buffer_load_b128(rs, yoff, WS_YG, 16);
        }
        float y0 = u2f(yg[0]), y1 = u2f(yg[2]);
        #pragma unroll
        for (int o = 8; o < 64; o <<= 1) { y0 += __shfl_xor(y0, o); y1 += __shfl_xor(y1, o); }
        unsigned ord = rkey >> 16;
        unsigned rb = (ord & 0x8000u) ? (ord & 0x7fffu) : (~ord & 0xffffu);
        const float rsel = bfr(sigmoidf_(bflo(rb)));
        yv[0] = bfr(bfr(__shfl(y0, 2 * g4)) * rsel);     yv[1] = bfr(bfr(__shfl(y1, 2 * g4)) * rsel);
        yv[2] = bfr(bfr(__shfl(y0, 2 * g4 + 1)) * rsel); yv[3] = bfr(bfr(__shfl(y1, 2 * g4 + 1)) * rsel);
    }
    #pragma unroll
    for (int t = 0; t < 2; ++t) {
        const int tok = tb * 64 + tho * 32 + 16 * t + l15;
        const float g = gres[t], omg = bfr(1.f - g);
        float bx[4] = {bflo(xres[t][0]), bfhi(xres[t][0]), bflo(xres[t][1]), bfhi(xres[t][1])};
        float r4[4];
        #pragma unroll
        for (int i = 0; i < 4; ++i) {
            float bb = (has_idx && tok == idx) ? bfr(bx[i] + yv[i]) : bx[i];
            r4[i] = bfr(g * bfr(oa[t][i])) + bfr(omg * bb);
        }
        u32x2 o2 = {pk2(r4[0], r4[1]), pk2(r4[2], r4[3])};
        *(GAS u32x2*)(out + (size_t)tok * 512 + n0 + 4 * g4) = o2;
    }
    ST(11); AP(11);
    { const unsigned long long t1 = __builtin_amdgcn_s_memrealtime(), d = ((AMP_W >> w) & 1) ? (tB - tA) * AMP_K / AMP_CYC : 0;
      _Pragma("nounroll") for (int i = 0; i < (1 << 20); ++i) { if (__builtin_amdgcn_s_memrealtime() - t1 >= d) break; __builtin_amdgcn_s_sleep(2); } }
}

extern "C" void kernel_launch(void* const* d_in, const int* in_sizes, int n_in, void* d_out, int out_size,
                              void* d_ws, size_t ws_size, hipStream_t stream) {
    (void)in_sizes; (void)n_in; (void)out_size; (void)ws_size;
    (void)hipFuncSetAttribute((const void*)mqa_moe_fused, hipFuncAttributeMaxDynamicSharedMemorySize, LDS_BYTES);
    mqa_moe_fused<<<256, 512, LDS_BYTES, stream>>>(
        (const u16*)d_in[0], (const u16*)d_in[1], (const u16*)d_in[2], (const u16*)d_in[3], (const u16*)d_in[4],
        (const u16*)d_in[6], (const u16*)d_in[7], (const u16*)d_in[8], (const u16*)d_in[9], (const u16*)d_in[10],
        (u16*)d_out, (char*)d_ws);
}
```

```cpp
#include <hip/hip_runtime.h>
#include <stdint.h>
#define OUTAUX 0
#define FREP 8
#define FSTR 16
#define ZROW 2
#define QWIN 2
#define RW -1
#define QD 4
#define TAILV 7
#define FLE 0
#define EPIV 7
#define FPOS 1
#define PSV 1
#define J2V 2
#define APR 1
#define BPR 2
#define PACE 2
#define FSPL 5
#define FPN 1
#define KSF 0
#define AWT 52
#define TPOS 3
#define FSLEEP 4
#define X2 3
#define KS2 2
#define KS3 2
#define P1 8
#define KS1 4
#define PROV 1
#define CKV 3
#define ACHK1 5,0,10

#define QROT 1
#define PSLEEP 2
#define NQA 2
#define SENTPOS 4
#define SIDEPOS 4
#define LAYOUT 1
#define WARM 1
#define WARMW 3
#define HOIST 1
#define LSLEEP 4
typedef unsigned short u16;
typedef __attribute__((ext_vector_type(8))) __bf16 bf16x8;
typedef __attribute__((ext_vector_type(16))) __bf16 bf16x16;
typedef __attribute__((ext_vector_type(2))) __bf16 bf16x2;
typedef __attribute__((ext_vector_type(4))) short s16x4;
typedef __attribute__((ext_vector_type(2))) float f32x2;
typedef __attribute__((ext_vector_type(4))) float f32x4;
typedef __attribute__((ext_vector_type(8))) float f32x8;
typedef __attribute__((ext_vector_type(16))) float f32x16;
typedef __attribute__((ext_vector_type(16))) unsigned u32x16;
typedef __attribute__((ext_vector_type(8))) unsigned u32x8;
typedef __attribute__((ext_vector_type(4))) unsigned u32x4;
typedef __attribute__((ext_vector_type(2))) unsigned u32x2;
#define LAS __attribute__((address_space(3)))
#define GAS __attribute__((address_space(1)))
#define RLX_AGENT __ATOMIC_RELAXED, __HIP_MEMORY_SCOPE_AGENT
#define RLX_WG __ATOMIC_RELAXED, __HIP_MEMORY_SCOPE_WORKGROUP

constexpr unsigned WS_EPOCH = 0;
constexpr unsigned WS_F1   = 2834432;
constexpr unsigned WS_F2   = 16384;
constexpr unsigned WS_KF   = 32768;
constexpr unsigned WS_VF   = 98304;
constexpr unsigned WS_WOF  = 163840;
constexpr unsigned WS_OF   = 688128;
constexpr unsigned WS_GATE = 2785280;
constexpr unsigned WS_HG   = 2793472;
constexpr unsigned WS_YG   = 2801664;
constexpr unsigned WS_TG   = WS_F1 + FREP * 256 * FSTR;
constexpr unsigned WS_END  = WS_TG + 32768;
constexpr unsigned WS_DBG  = 4194304;

constexpr unsigned REC     = 2080;
constexpr unsigned L_WKV   = 0;
constexpr unsigned L_WOST  = 16640;
constexpr unsigned L_GW    = 18688;
constexpr unsigned L_XT    = 25088;
constexpr unsigned L_WQ    = 91520;
constexpr unsigned L_FW1   = 133120;
constexpr unsigned L_FW2   = 149504;
constexpr unsigned L_QX    = 157056;
constexpr unsigned L_MISC  = 161152;
constexpr unsigned L_C4 = L_MISC + 4, L_C5 = L_MISC + 8, L_C6 = L_MISC + 12, L_C7 = L_MISC + 16,
                   L_C8 = L_MISC + 20, L_C9 = L_MISC + 24, L_C3 = L_MISC + 28, L_KEY = L_MISC + 32,
                   L_AKEY = L_MISC + 36, L_CA = L_MISC + 40, L_CQ = L_MISC + 44, L_GT = L_MISC + 64;
constexpr unsigned L_XSEL  = L_MISC + 128;
constexpr unsigned L_HBUF  = L_XSEL + 1024;
constexpr unsigned LDS_BYTES = 163840;
constexpr int SPIN_CAP = 1 << 18;

__device__ __forceinline__ unsigned pk2(float a, float b) {
    f32x2 v = {a, b};
    return __builtin_bit_cast(unsigned, __builtin_convertvector(v, bf16x2));
}
__device__ __forceinline__ float bfr(float a) {
    return __builtin_bit_cast(float, pk2(a, a) & 0xffff0000u);
}
__device__ __forceinline__ unsigned f2u(float f) { return __builtin_bit_cast(unsigned, f); }
__device__ __forceinline__ float u2f(unsigned u) { return __builtin_bit_cast(float, u); }
__device__ __forceinline__ float bflo(unsigned u) { return __builtin_bit_cast(float, u << 16); }
__device__ __forceinline__ float bfhi(unsigned u) { return __builtin_bit_cast(float, u & 0xffff0000u); }
__device__ __forceinline__ float sigmoidf_(float z) { return __builtin_amdgcn_rcpf(1.f + __builtin_amdgcn_exp2f(-1.4426950408889634f * z)); }

__device__ __forceinline__ void dma16(unsigned voff, const void* sbase, unsigned lds) {
    unsigned keep;
    asm volatile("s_mov_b32 %0, m0\n\ts_mov_b32 m0, %3\n\ts_nop 0\n\tglobal_load_lds_dwordx4 %1, %2\n\ts_mov_b32 m0, %0"
                 : "=&s"(keep) : "v"(voff), "s"(sbase), "s"(lds) : "memory");
}
__device__ __forceinline__ void dma16p(const void* gsrc, unsigned lds) {
    unsigned keep;
    asm volatile("s_mov_b32 %0, m0\n\ts_mov_b32 m0, %2\n\ts_nop 0\n\tglobal_load_lds_dwordx4 %1, off\n\ts_mov_b32 m0, %0"
                 : "=&s"(keep) : "v"(gsrc), "s"(lds) : "memory");
}
__device__ __forceinline__ void dma16n(unsigned voff, const void* sbase, unsigned lds) {
    asm volatile("s_mov_b32 m0, %2\n\ts_nop 0\n\tglobal_load_lds_dwordx4 %0, %1" :: "v"(voff), "s"(sbase), "s"(lds) : "m0", "memory");
}
__device__ __forceinline__ void touch(unsigned voff, const void* sbase, unsigned lds) {
    asm volatile("s_mov_b32 m0, %2\n\ts_nop 0\n\tglobal_load_lds_dword %0, %1 sc1" :: "v"(voff), "s"(sbase), "s"(lds) : "m0", "memory");
}
template <int DL> __device__ __forceinline__ void dma16x2(unsigned voff, const void* sbase, unsigned lds) {
    if (DL == 0) asm volatile("s_mov_b32 m0, %2\n\ts_nop 0\n\tglobal_load_lds_dwordx4 %0, %1\n\tglobal_load_lds_dwordx4 %0, %1 offset:1024"
                              :: "v"(voff), "s"(sbase), "s"(lds) : "m0", "memory");
    else         asm volatile("s_mov_b32 m0, %2\n\ts_nop 0\n\tglobal_load_lds_dwordx4 %0, %1\n\ts_add_u32 m0, m0, 16\n\ts_nop 0\n\tglobal_load_lds_dwordx4 %0, %1 offset:1024"
                              :: "v"(voff), "s"(sbase), "s"(lds) : "m0", "scc", "memory");
}
template <int N> __device__ __forceinline__ void wq_issue(unsigned& qvo, unsigned& dst, const char* src, unsigned lds) {
    if (N > 0) asm volatile(".rept %4\n\ts_add_u32 m0, %1, %3\n\ts_add_u32 %1, %1, 4096\n\tglobal_load_lds_dwordx4 %0, %2\n\t"
                            "v_add_u32 %0, 0x8000, %0\n\ts_and_b32 %1, %1, 0xffff\n\tv_and_b32 %0, 0x7ffff, %0\n\t.endr"
                            : "+v"(qvo), "+s"(dst) : "s"(src), "s"(lds), "n"(N) : "m0", "scc", "memory");
}
template <int N> __device__ __forceinline__ void kv_issue(unsigned voff, u32x4 srd, unsigned so, unsigned dst) {
    asm volatile(".rept %4\n\ts_mov_b32 m0, %1\n\ts_add_u32 %1, %1, 4160\n\tbuffer_load_dwordx4 %2, %3, %0 offen sc1 lds\n\ts_add_u32 %0, %0, 2048\n\t.endr"
                 : "+s"(so), "+s"(dst) : "v"(voff), "s"(srd), "n"(N) : "m0", "scc", "memory");
}
template <int N, int SL> __device__ __forceinline__ void kv_paced(unsigned voff, u32x4 srd, unsigned so, unsigned dst) {
    if (N > 0)
    asm volatile(".rept %4\n\ts_sleep %5\n\ts_mov_b32 m0, %1\n\ts_add_u32 %1, %1, 4160\n\tbuffer_load_dwordx4 %2, %3, %0 offen sc1 lds\n\ts_add_u32 %0, %0, 2048\n\t.endr"
                 : "+s"(so), "+s"(dst) : "v"(voff), "s"(srd), "n"(N), "n"(SL) : "m0", "scc", "memory");
}
template <int TOT> __device__ __forceinline__ void wait_vm(int i) {
    if (TOT - 4 * i == 12) asm volatile("s_waitcnt vmcnt(12)" ::: "memory");
    if (TOT - 4 * i == 8)  asm volatile("s_waitcnt vmcnt(8)" ::: "memory");
    if (TOT - 4 * i == 4)  asm volatile("s_waitcnt vmcnt(4)" ::: "memory");
    if (TOT - 4 * i <= 0)  asm volatile("s_waitcnt vmcnt(0)" ::: "memory");
}
template <int N> __device__ __forceinline__ void wait_vmn() { asm volatile("s_waitcnt vmcnt(%0)" :: "n"(N) : "memory"); }
#define WAIT_VM0()   asm volatile("s_waitcnt vmcnt(0)" ::: "memory")
#define WAIT_LGKM0() asm volatile("s_waitcnt lgkmcnt(0)" ::: "memory")
#define BARRIER()    asm volatile("s_waitcnt lgkmcnt(0)\n\ts_barrier" ::: "memory")
#define FENCE()      asm volatile("" ::: "memory")
#define STORE128(d, r, vo, so) do { const u32x4 d_ = (d); __builtin_amdgcn_raw_buffer_store_b128(d_, r, vo, so, 16); \
        asm volatile("s_nop 1" :: "v"(d_) : "memory"); } while (0)

__device__ __forceinline__ u32x4 tr_frag(LAS char* p) {
    s16x4 a0 = __builtin_amdgcn_ds_read_tr16_b64_v4i16((LAS s16x4*)p);
    s16x4 a1 = __builtin_amdgcn_ds_read_tr16_b64_v4i16((LAS s16x4*)(p + 512));
    u32x2 p0 = __builtin_bit_cast(u32x2, a0), p1 = __builtin_bit_cast(u32x2, a1);
    u32x4 a = {p0[0], p0[1], p1[0], p1[1]};
    return a;
}
__device__ __forceinline__ f32x4 mfma16(u32x4 a, u32x4 b, f32x4 c) {
    return __builtin_amdgcn_mfma_f32_16x16x32_bf16(__builtin_bit_cast(bf16x8, a), __builtin_bit_cast(bf16x8, b), c, 0, 0, 0);
}
__device__ __forceinline__ f32x16 mfma32(u32x4 a, u32x4 b, f32x16 c) {
    return __builtin_amdgcn_mfma_f32_32x32x16_bf16(__builtin_bit_cast(bf16x8, a), __builtin_bit_cast(bf16x8, b), c, 0, 0, 0);
}
__device__ __forceinline__ f32x16 smf(u32x4 v, f32x8 p, f32x16 acc) {
    return __builtin_amdgcn_smfmac_f32_32x32x32_bf16(__builtin_bit_cast(bf16x8, v), __builtin_bit_cast(bf16x16, p), acc, (int)0xDDDDDDDD, 0, 0);
}
template <unsigned OFF, unsigned WANT> __device__ __forceinline__ void lds_wait(unsigned mb) {
    unsigned v, sv, n;
    asm volatile("ds_read_b32 %0, %3 offset:%4\n\ts_waitcnt lgkmcnt(0)\n\tv_readfirstlane_b32 %1, %0\n\ts_cmp_ge_u32 %1, %5\n\ts_cbranch_scc1 MQA_WD%=\n\t"
                 "s_mov_b32 %2, 0x40000\n\tMQA_WL%=:\n\ts_sleep 1\n\tds_read_b32 %0, %3 offset:%4\n\ts_sub_u32 %2, %2, 1\n\ts_waitcnt lgkmcnt(0)\n\t"
                 "v_readfirstlane_b32 %1, %0\n\ts_cmp_eq_u32 %2, 0\n\ts_cbranch_scc1 MQA_WD%=\n\ts_cmp_ge_u32 %1, %5\n\ts_cbranch_scc0 MQA_WL%=\n\tMQA_WD%=:"
                 : "=&v"(v), "=&s"(sv), "=&s"(n) : "v"(mb), "n"(OFF - L_MISC), "n"(WANT) : "scc", "memory");
}
template <unsigned OFF, unsigned WANT> __device__ __forceinline__ void lds_wait_slow(unsigned mb) {
    unsigned v, sv, n;
    asm volatile("s_mov_b32 %2, 0x40000\n\tMQA_VL%=:\n\tds_read_b32 %0, %3 offset:%4\n\ts_sub_u32 %2, %2, 1\n\ts_waitcnt lgkmcnt(0)\n\t"
                 "v_readfirstlane_b32 %1, %0\n\ts_cmp_eq_u32 %2, 0\n\ts_cbranch_scc1 MQA_VD%=\n\ts_cmp_ge_u32 %1, %5\n\ts_cbranch_scc1 MQA_VD%=\n\t"
                 "s_sleep %6\n\ts_branch MQA_VL%=\n\tMQA_VD%=:"
                 : "=&v"(v), "=&s"(sv), "=&s"(n) : "v"(mb), "n"(OFF - L_MISC), "n"(WANT), "n"(LSLEEP) : "scc", "memory");
}
template <unsigned OFF> __device__ __forceinline__ void lds_sig(unsigned mb, unsigned one) {
    asm volatile("s_waitcnt lgkmcnt(0)\n\ts_mov_b64 exec, 1\n\tds_add_u32 %0, %1 offset:%2\n\ts_mov_b64 exec, -1"
                 :: "v"(mb), "v"(one), "n"(OFF - L_MISC) : "memory");
}
#define lds_wait_ge(L, off, want) lds_wait<off, want>(LB + L_MISC)
#define lds_signal(L, off, l)     lds_sig<off>(LB + L_MISC, 1u)
#define ST(i) do {} while (0)
#define STC(i) do {} while (0)
#define AP(i) do {} while (0)
#define SC(i) do {} while (0)

__device__ __forceinline__ unsigned wave_max(unsigned v) {
    v = max(v, (unsigned)__builtin_amdgcn_update_dpp((int)v, (int)v, 0xB1, 0xf, 0xf, false));
    v = max(v, (unsigned)__builtin_amdgcn_update_dpp((int)v, (int)v, 0x4E, 0xf, 0xf, false));
    v = max(v, (unsigned)__builtin_amdgcn_update_dpp((int)v, (int)v, 0x124, 0xf, 0xf, false));
    v = max(v, (unsigned)__builtin_amdgcn_update_dpp((int)v, (int)v, 0x128, 0xf, 0xf, false));
    u32x2 s = __builtin_amdgcn_permlane16_swap(v, v, false, false); v = max(s[0], s[1]);
    s = __builtin_amdgcn_permlane32_swap(v, v, false, false);
    return max(s[0], s[1]);
}
__device__ __forceinline__ f32x16 attn_true_max(LAS char* kp, LAS char* vp, LAS char* vp1, u32x4 q) {
    f32x16 z; for (int i = 0; i < 16; ++i) z[i] = 0.f;
    float m = -__builtin_inff();
    #pragma unroll 1
    for (int t = 0; t < 64; ++t) {
        f32x16 s = mfma32(*(LAS u32x4*)(kp + t * REC), q, z);
        #pragma unroll
        for (int i = 0; i < 16; ++i) m = fmaxf(m, s[i]);
    }
    m = fmaxf(m, __shfl_xor(m, 32));
    f32x16 acc = z;
    #pragma unroll 1
    for (int t = 0; t < 64; ++t) {
        f32x16 s = mfma32(*(LAS u32x4*)(kp + t * REC), q, z);
        f32x8 p0, p1;
        #pragma unroll
        for (int i = 0; i < 8; ++i) { p0[i] = __builtin_amdgcn_exp2f(s[i] - m); p1[i] = __builtin_amdgcn_exp2f(s[8 + i] - m); }
        acc = smf(*(LAS u32x4*)(vp + t * REC), p0, acc);
        acc = smf(*(LAS u32x4*)(vp1 + t * REC), p1, acc);
    }
    return acc;
}

__global__ void __launch_bounds__(512, 2) mqa_moe_fused(
        const u16* __restrict__ a_x, const u16* __restrict__ a_wq, const u16* __restrict__ a_wk, const u16* __restrict__ a_wv,
        const u16* __restrict__ a_wo, const u16* __restrict__ a_w_r, const u16* __restrict__ a_w_gate, const u16* __restrict__ a_w_up,
        const u16* __restrict__ a_w_down, const u16* __restrict__ a_gate_w, u16* __restrict__ a_out, char* a_ws) {
    u32x16 ka; u32x8 kb;
    asm volatile("s_load_dwordx16 %0, %2, 0x0\n\ts_load_dwordx8 %1, %2, 0x40"
                 : "={s[36:51]}"(ka), "={s[52:59]}"(kb) : "s"(__builtin_amdgcn_kernarg_segment_ptr()));
    extern __shared__ __attribute__((aligned(256))) char smem[];
    LAS char* L = (LAS char*)smem;
    const unsigned LB = (unsigned)(size_t)L;
    ST(0); STC(29); SC(43); AP(0);
#define HGX 0
#define NOPF 0
#define APRIO 3
#define VMCL
#define JOBV 0
#define SENTW ((JOBV & 2) ? 0 : 3)
#define J1W_ 3
#define J1W J1W_
#define MQA_STR2(x) #x
#define MQA_STR(x) MQA_STR2(x)
    int l = threadIdx.x & 63, w = __builtin_amdgcn_readfirstlane(threadIdx.x >> 6), b = blockIdx.x;
    const u16 *x, *wq, *wk, *wv, *wo, *w_r, *w_gate, *w_up, *w_down, *gate_w; u16* out; char* ws;
    unsigned E;
    {
        asm volatile("s_cmp_lg_u32 %[w], 7\n\ts_cbranch_scc1 MQA_T7\n\ts_getpc_b64 s[94:95]\n\tMQA_PC:\n\t"
                     "s_cmp_ge_u32 %[j], (MQA_END - MQA_PC) / 1024\n\ts_cselect_b32 s93, 0, %[j]\n\ts_lshl_b32 s93, s93, 10\n\t"
                     "s_add_u32 s94, s94, s93\n\ts_addc_u32 s95, s95, 0\n\ts_mov_b32 m0, %[junk]\n\ts_nop 0\n\t"
                     "global_load_lds_dwordx4 %[l16], s[94:95]\n\t"
                     "MQA_T7:"
                     :: [w] "s"(w), [j] "s"(b >> 3), [l16] "v"(l * 16), [junk] "s"(LB + L_XSEL) : "s93", "s94", "s95", "m0", "scc", "memory");
        const int hg0 = (b >> 5) ^ HGX, row = (hg0 >> 1) * 16 + 4 * w, tb0 = (b & 7) * 4 + ((b >> 3) & 3);
        const unsigned woff = (w & 3) * 4096, xoff = (tb0 * 64 + (row & 63)) * 1024, mw = LB + L_WKV + woff, mx = LB + L_XT + row * 256;
        const unsigned wvo = (8 * ((l >> 3) & 3) + 4 * (l >> 5) + ((l >> 1) & 3)) * 32 + (l & 1) * 16;
        const unsigned xvo = (l >> 4) * 1024 + (((l & 15) ^ (4 * (w & 3) + (l >> 4))) * 16);
        asm volatile("s_cmp_lg_u32 %[w], 6\n\ts_cbranch_scc1 MQA_T6\n\tds_write_b32 %[ca], %[z]\n\tMQA_T6:"
                     :: [w] "s"(w), [ca] "v"(LB + L_MISC + 4 * (l & 15)), [z] "v"(0u), "s"(woff), "s"(xoff), "s"(mw), "s"(mx), "v"(wvo), "v"(xvo) : "scc", "memory");
        asm volatile("s_waitcnt lgkmcnt(0)" : "+{s[36:51]}"(ka), "+{s[52:59]}"(kb) :: "memory");
#define KARG(v, i) ((unsigned long long)v[2 * (i)] | ((unsigned long long)v[2 * (i) + 1] << 32))
        x = (const u16*)KARG(ka, 0); wq = (const u16*)KARG(ka, 1); wk = (const u16*)KARG(ka, 2); wv = (const u16*)KARG(ka, 3);
        wo = (const u16*)KARG(ka, 4); w_r = (const u16*)KARG(ka, 5); w_gate = (const u16*)KARG(ka, 6); w_up = (const u16*)KARG(ka, 7);
        w_down = (const u16*)KARG(kb, 0); gate_w = (const u16*)KARG(kb, 1); out = (u16*)KARG(kb, 2); ws = (char*)KARG(kb, 3);
        ST(22); AP(22);
        const char* wsrc = (const char*)((hg0 & 1) ? wv : wk) + woff;
        const char* xsrc = (const char*)x + xoff;
        asm volatile("global_load_dword %[E], %[z], %[ws] sc1\n\t"
                     "s_cmp_ge_u32 %[w], 4\n\ts_cbranch_scc1 MQA_TB\n\ts_setprio " MQA_STR(APRIO) "\n\t"
                     "s_mov_b32 m0, %[mw]\n\ts_nop 0\n\tglobal_load_lds_dwordx4 %[wvo], %[sw]\n\tglobal_load_lds_dwordx4 %[wvo], %[sw] offset:1024\n\t"
                     "s_mov_b32 m0, %[mx]\n\ts_nop 0\n\tglobal_load_lds_dwordx4 %[xvo], %[sx]\n\t"
                     "s_add_u32 m0, m0, 16128\n\ts_nop 0\n\tglobal_load_lds_dwordx4 %[xvo], %[sx] offset:256\n\t"
                     "s_mov_b32 m0, %[mw]\n\ts_nop 0\n\tglobal_load_lds_dwordx4 %[wvo], %[sw] offset:2048\n\tglobal_load_lds_dwordx4 %[wvo], %[sw] offset:3072\n\t"
                     "s_add_u32 m0, %[mx], 32256\n\ts_nop 0\n\tglobal_load_lds_dwordx4 %[xvo], %[sx] offset:512\n\t"
                     "s_add_u32 m0, m0, 16128\n\ts_nop 0\n\tglobal_load_lds_dwordx4 %[xvo], %[sx] offset:768\n\t"
                     "s_cmp_lg_u32 %[w], 1\n\ts_cbranch_scc1 MQA_TB\n\t"
                     "s_mov_b32 m0, %[mg]\n\ts_nop 0\n\tglobal_load_lds_dwordx4 %[l16], %[wr]\n\t"
                     "s_add_u32 m0, m0, 1024\n\ts_nop 0\n\tglobal_load_lds_dwordx4 %[l16], %[gw]\n\t"
                     "MQA_TB:\n\ts_waitcnt lgkmcnt(0)\n\ts_barrier"
                     : [E] "=&v"(E)
                     : [w] "s"(w), [z] "v"(0u), [ws] "s"(ws + WS_EPOCH), [mw] "s"(mw), [mx] "s"(mx),
                       [wvo] "v"(wvo), [xvo] "v"(xvo), [sw] "s"(wsrc), [sx] "s"(xsrc),
                       [mg] "s"(LB + L_GW), [l16] "v"(l * 16), [wr] "s"(w_r), [gw] "s"(gate_w)
                     : "m0", "scc", "memory");
        asm volatile("" : "+s"(w));
        asm volatile("" : "+s"(b));
        asm volatile("" : "+v"(l));
    }
    const int xcd = b & 7, j = b >> 3;
    const int tb = xcd * 4 + (j & 3), hg = (j >> 2) ^ HGX;
    const int g4 = l >> 4, l15 = l & 15;
    const __amdgpu_buffer_rsrc_t rs = __builtin_amdgcn_make_buffer_rsrc((void*)ws, 0, WS_END, 0x00020000);
    const __amdgpu_buffer_rsrc_t ro = __builtin_amdgcn_make_buffer_rsrc((void*)out, 0, 2048 * 1024, 0x00020000);
    const int sg = (l >> 3) & 3, sjh = l >> 5, sq = (l >> 1) & 3;
    const int krow = 8 * sg + 4 * sjh + sq;
    const u32x4 ones = {0x3f803f80u, 0x3f803f80u, 0x3f803f80u, 0x3f803f80u};
    const int type = hg & 1, tg = hg >> 1;
    const char* xt = (const char*)x + (size_t)tb * 65536;
    unsigned tag;
    const u32x4 srd = {(unsigned)(size_t)ws, (unsigned)((size_t)ws >> 32) & 0xffffu, WS_END, 0x00020000u};
    const unsigned kso0 = ((w & 1) ? WS_VF : WS_KF) + ((w >> 1) & 1) * 1024;
    const unsigned kdst0 = LB + ((w >> 1) & 1) * REC + (w & 1) * 1024;
    GAS unsigned* const fp0 = (GAS unsigned*)(ws + WS_F1 + (b & (FREP - 1)) * (256 * FSTR) + ((w & 3) * 64 + l) * FSTR);
    const int l31 = l & 31;
    const bool vone = l31 >= 16 && (l31 < 20 || l31 >= 28);
    const unsigned vdat = 1024 + (l >> 5) * 256 + (l31 < 16 ? l31 : l31 - 16) * 16;
    const bool vzero = ZROW == 9 || (ZROW >= 1 && l31 >= 20 && l31 < 28) || (ZROW == 2 && l31 > 16 && l31 != 28);
    const unsigned vrel0 = vzero ? 2064 : vone ? 2048 : vdat, vrel1 = vzero ? 2064 : vone ? 2048 : vdat + 512;
    unsigned kp, vp, vp1;
#define PRELOOP() do { kp = LB + l * 16; vp = LB + vrel0; vp1 = LB + vrel1; \
        if (HOIST) asm volatile("" : "+{v193}"(kp), "+{v194}"(vp), "+{v195}"(vp1)); } while (0)
#define ROUTER() do { \
            lds_wait_ge(L, L_C3, 4u); \
            f32x4 acc = {0.f, 0.f, 0.f, 0.f}; \
            const unsigned xp = L_XT + (tg * 16 + l15) * 256 + (g4 ^ l15) * 16; \
            LAS char* gp = L + L_GW + (l & 1) * 1024 + g4 * 16; \
            _Pragma("unroll") \
            for (int hh = 0; hh < 2; ++hh) { \
                u32x4 af[8], bx[8]; \
                _Pragma("unroll") \
                for (int s = 0; s < 8; ++s) { af[s] = *(LAS u32x4*)(gp + (8 * hh + s) * 64); bx[s] = *(LAS u32x4*)(L + (xp ^ ((s & 3) * 64)) + (2 * hh + (s >> 2)) * 16384); } \
                __builtin_amdgcn_sched_barrier(0); \
                _Pragma("unroll") \
                for (int s = 0; s < 8; ++s) acc = mfma16(af[s], bx[s], acc); \
                __builtin_amdgcn_sched_barrier(0); \
            } \
              \
            const int tok = tb * 64 + tg * 16 + l15; \
            const float gt = bfr(sigmoidf_(bfr(acc[1]))); \
            unsigned uu = pk2(acc[0], acc[0]) & 0xffffu; \
            unsigned ord = (uu & 0x8000u) ? (~uu & 0xffffu) : (uu | 0x8000u); \
            if ((uu & 0x7fffu) > 0x7f80u) ord = 0u;     \
            unsigned key = (l < 16) ? ((ord << 16) | (unsigned)(2047 - tok)) : 0u; \
            key = wave_max(key); \
            if (l < 16) *(LAS unsigned*)(L + L_GT + 4 * l) = f2u(gt); \
            if (l == 0) *(LAS unsigned*)(L + L_AKEY) = key; \
        } while (0)
    u32x2 xres[2];
    float gres[2];
    f32x16 W0, W1, W2, W3;
#define JSLEEP 8
#define ASTV
#define ASTC
    if (w < 4) {
        ST(20); AP(20);
#define SENTINELS() do { _Pragma("unroll") for (int i = 0; i < 8; ++i) \
            __builtin_amdgcn_raw_buffer_store_b128(sent, rs, l * 16, WS_OF + ((tb * 2 + (i >> 2)) * 32 + 4 * hg + (i & 3)) * 1024, 16); \
            asm volatile("s_nop 1" :: "v"(sent) : "memory"); } while (0)
        if (w == 2) {
            const int wrow = 16 * (sg >> 1) + 8 * sjh + 4 * (sg & 1) + sq;
            const char* wsrc = (const char*)(wo + (size_t)(64 * (b & 7)) * 512 + 16 * (b >> 3));
            const unsigned wovo = wrow * 1024 + (l & 1) * 16;
            dma16n(wovo, wsrc, LB + L_WOST);
            dma16n(wovo, wsrc + 32 * 1024, LB + L_WOST + 1024);
        }
        const int cp = w & 1, th = w >> 1, h = l >> 5, nhi = g4 & 1, q = (l >> 2) & 3, p = l & 3;
        const unsigned ap = LB + L_WQ + h * 1024 + q * 128 + ((64 * cp + 32 * nhi + 8 * p) ^ (((q >> 1) & 1) << 6));
        const unsigned bp = LB + L_XT + (32 * th + (l & 31)) * 256 + ((l & 15) ^ h) * 16;
        const unsigned q0 = ((j & 3) * QROT) & 3, q1 = (q0 + 1) & 3, q2 = (q0 + 2) & 3, q3 = (q0 + 3) & 3;
        const unsigned a0 = ap + q0 * 16384, a1 = ap + q1 * 16384, a2 = ap + q2 * 16384, a3 = ap + q3 * 16384;
        const unsigned b0 = bp + q0 * 16384, b1 = bp + q1 * 16384, b2 = bp + q2 * 16384, b3 = bp + q3 * 16384;
        const int kt = tb * 2 + (tg >> 1);
        const int pf = ((kt & 1) * 2 + type) * 64 + (kt >> 1) * 2 + (tg & 1);
        GAS unsigned* const fa = (GAS unsigned*)(ws + WS_F1 + (l & (FREP - 1)) * (256 * FSTR) + pf * FSTR);
        const unsigned df = __builtin_amdgcn_readfirstlane(w == 0 ? (unsigned)tb >> 2 : 0u);
        const unsigned wp = LB + L_WKV + 8 * l;
        const unsigned xp = LB + L_XT + (tg * 16 + l15) * 256 + (g4 ^ l15) * 16;
        asm volatile("" :: "v"(a0), "v"(a1), "v"(a2), "v"(a3), "v"(b0), "v"(b1), "v"(b2), "v"(b3), "v"(fa), "s"(df), "v"(LB + L_CQ),
                           "v"(wp), "v"(xp), "v"(xp ^ 64), "v"(xp ^ 128), "v"(xp ^ 192));
        PRELOOP();
        if (w == 2 || (RW < 0 && w == 1)) asm volatile("s_waitcnt vmcnt(2)" ::: "memory");
        else WAIT_VM0();
        asm volatile("v_readfirstlane_b32 %0, %1" : "=s"(tag) : "v"(E) : "memory");
        tag += 1u;
        lds_signal(L, L_C3, l);
        ST(1); AP(1);
        if (w == 0) {
            lds_wait_ge(L, L_C3, 4u);
            f32x4 acc;
            if (type == 0) asm volatile("ds_read_b64_tr_b16 v[36:37], %[wp] offset:0\n\tds_read_b64_tr_b16 v[38:39], %[wp] offset:512\n\tds_read_b128 v[40:43], %[xp0] offset:0\n\tds_read_b64_tr_b16 v[44:45], %[wp] offset:1024\n\tds_read_b64_tr_b16 v[46:47], %[wp] offset:1536\n\tds_read_b128 v[48:51], %[xp1] offset:0\n\tds_read_b64_tr_b16 v[52:53], %[wp] offset:2048\n\tds_read_b64_tr_b16 v[54:55], %[wp] offset:2560\n\tds_read_b128 v[56:59], %[xp2] offset:0\n\tds_read_b64_tr_b16 v[60:61], %[wp] offset:3072\n\tds_read_b64_tr_b16 v[62:63], %[wp] offset:3584\n\tds_read_b128 v[64:67], %[xp3] offset:0\n\tds_read_b64_tr_b16 v[68:69], %[wp] offset:4096\n\tds_read_b64_tr_b16 v[70:71], %[wp] offset:4608\n\tds_read_b128 v[72:75], %[xp0] offset:16384\n\ts_waitcnt lgkmcnt(12)\n\tv_mfma_f32_16x16x32_bf16 v[32:35], v[36:39], v[40:43], 0\n\tds_read_b64_tr_b16 v[36:37], %[wp] offset:5120\n\tds_read_b64_tr_b16 v[38:39], %[wp] offset:5632\n\tds_read_b128 v[40:43], %[xp1] offset:16384\n\ts_waitcnt lgkmcnt(12)\n\tv_mfma_f32_16x16x32_bf16 v[32:35], v[44:47], v[48:51], v[32:35]\n\tds_read_b64_tr_b16 v[44:45], %[wp] offset:6144\n\tds_read_b64_tr_b16 v[46:47], %[wp] offset:6656\n\tds_read_b128 v[48:51], %[xp2] offset:16384\n\ts_waitcnt lgkmcnt(12)\n\tv_mfma_f32_16x16x32_bf16 v[32:35], v[52:55], v[56:59], v[32:35]\n\tds_read_b64_tr_b16 v[52:53], %[wp] offset:7168\n\tds_read_b64_tr_b16 v[54:55], %[wp] offset:7680\n\tds_read_b128 v[56:59], %[xp3] offset:16384\n\ts_waitcnt lgkmcnt(12)\n\tv_mfma_f32_16x16x32_bf16 v[32:35], v[60:63], v[64:67], v[32:35]\n\tds_read_b64_tr_b16 v[60:61], %[wp] offset:8192\n\tds_read_b64_tr_b16 v[62:63], %[wp] offset:8704\n\tds_read_b128 v[64:67], %[xp0] offset:32768\n\ts_waitcnt lgkmcnt(12)\n\tv_mfma_f32_16x16x32_bf16 v[32:35], v[68:71], v[72:75], v[32:35]\n\tds_read_b64_tr_b16 v[68:69], %[wp] offset:9216\n\tds_read_b64_tr_b16 v[70:71], %[wp] offset:9728\n\tds_read_b128 v[72:75], %[xp1] offset:32768\n\ts_waitcnt lgkmcnt(12)\n\tv_mfma_f32_16x16x32_bf16 v[32:35], v[36:39], v[40:43], v[32:35]\n\tds_read_b64_tr_b16 v[36:37], %[wp] offset:10240\n\tds_read_b64_tr_b16 v[38:39], %[wp] offset:10752\n\tds_read_b128 v[40:43], %[xp2] offset:32768\n\ts_waitcnt lgkmcnt(12)\n\tv_mfma_f32_16x16x32_bf16 v[32:35], v[44:47], v[48:51], v[32:35]\n\tds_read_b64_tr_b16 v[44:45], %[wp] offset:11264\n\tds_read_b64_tr_b16 v[46:47], %[wp] offset:11776\n\tds_read_b128 v[48:51], %[xp3] offset:32768\n\ts_waitcnt lgkmcnt(12)\n\tv_mfma_f32_16x16x32_bf16 v[32:35], v[52:55], v[56:59], v[32:35]\n\tds_read_b64_tr_b16 v[52:53], %[wp] offset:12288\n\tds_read_b64_tr_b16 v[54:55], %[wp] offset:12800\n\tds_read_b128 v[56:59], %[xp0] offset:49152\n\ts_waitcnt lgkmcnt(12)\n\tv_mfma_f32_16x16x32_bf16 v[32:35], v[60:63], v[64:67], v[32:35]\n\tds_read_b64_tr_b16 v[60:61], %[wp] offset:13312\n\tds_read_b64_tr_b16 v[62:63], %[wp] offset:13824\n\tds_read_b128 v[64:67], %[xp1] offset:49152\n\ts_waitcnt lgkmcnt(12)\n\tv_mfma_f32_16x16x32_bf16 v[32:35], v[68:71], v[72:75], v[32:35]\n\tds_read_b64_tr_b16 v[68:69], %[wp] offset:14336\n\tds_read_b64_tr_b16 v[70:71], %[wp] offset:14848\n\tds_read_b128 v[72:75], %[xp2] offset:49152\n\ts_waitcnt lgkmcnt(12)\n\tv_mfma_f32_16x16x32_bf16 v[32:35], v[36:39], v[40:43], v[32:35]\n\tds_read_b64_tr_b16 v[36:37], %[wp] offset:15360\n\tds_read_b64_tr_b16 v[38:39], %[wp] offset:15872\n\tds_read_b128 v[40:43], %[xp3] offset:49152\n\ts_waitcnt lgkmcnt(12)\n\tv_mfma_f32_16x16x32_bf16 v[32:35], v[44:47], v[48:51], v[32:35]\n\ts_waitcnt lgkmcnt(9)\n\tv_mfma_f32_16x16x32_bf16 v[32:35], v[52:55], v[56:59], v[32:35]\n\ts_waitcnt lgkmcnt(6)\n\tv_mfma_f32_16x16x32_bf16 v[32:35], v[60:63], v[64:67], v[32:35]\n\ts_waitcnt lgkmcnt(3)\n\tv_mfma_f32_16x16x32_bf16 v[32:35], v[68:71], v[72:75], v[32:35]\n\ts_waitcnt lgkmcnt(0)\n\tv_mfma_f32_16x16x32_bf16 v[32:35], v[36:39], v[40:43], v[32:35]\n\ts_nop 7\n\ts_nop 1" : "=&{v[32:35]}"(acc) : [wp] "v"(wp), [xp0] "v"(xp), [xp1] "v"(xp ^ 64), [xp2] "v"(xp ^ 128), [xp3] "v"(xp ^ 192) : "v36", "v37", "v38", "v39", "v40", "v41", "v42", "v43", "v44", "v45", "v46", "v47", "v48", "v49", "v50", "v51", "v52", "v53", "v54", "v55", "v56", "v57", "v58", "v59", "v60", "v61", "v62", "v63", "v64", "v65", "v66", "v67", "v68", "v69", "v70", "v71", "v72", "v73", "v74", "v75", "memory");
            else           asm volatile("ds_read_b64_tr_b16 v[36:37], %[wp] offset:0\n\tds_read_b64_tr_b16 v[38:39], %[wp] offset:512\n\tds_read_b128 v[40:43], %[xp0] offset:0\n\tds_read_b64_tr_b16 v[44:45], %[wp] offset:1024\n\tds_read_b64_tr_b16 v[46:47], %[wp] offset:1536\n\tds_read_b128 v[48:51], %[xp1] offset:0\n\tds_read_b64_tr_b16 v[52:53], %[wp] offset:2048\n\tds_read_b64_tr_b16 v[54:55], %[wp] offset:2560\n\tds_read_b128 v[56:59], %[xp2] offset:0\n\tds_read_b64_tr_b16 v[60:61], %[wp] offset:3072\n\tds_read_b64_tr_b16 v[62:63], %[wp] offset:3584\n\tds_read_b128 v[64:67], %[xp3] offset:0\n\tds_read_b64_tr_b16 v[68:69], %[wp] offset:4096\n\tds_read_b64_tr_b16 v[70:71], %[wp] offset:4608\n\tds_read_b128 v[72:75], %[xp0] offset:16384\n\ts_waitcnt lgkmcnt(12)\n\tv_mfma_f32_16x16x32_bf16 v[32:35], v[40:43], v[36:39], 0\n\tds_read_b64_tr_b16 v[36:37], %[wp] offset:5120\n\tds_read_b64_tr_b16 v[38:39], %[wp] offset:5632\n\tds_read_b128 v[40:43], %[xp1] offset:16384\n\ts_waitcnt lgkmcnt(12)\n\tv_mfma_f32_16x16x32_bf16 v[32:35], v[48:51], v[44:47], v[32:35]\n\tds_read_b64_tr_b16 v[44:45], %[wp] offset:6144\n\tds_read_b64_tr_b16 v[46:47], %[wp] offset:6656\n\tds_read_b128 v[48:51], %[xp2] offset:16384\n\ts_waitcnt lgkmcnt(12)\n\tv_mfma_f32_16x16x32_bf16 v[32:35], v[56:59], v[52:55], v[32:35]\n\tds_read_b64_tr_b16 v[52:53], %[wp] offset:7168\n\tds_read_b64_tr_b16 v[54:55], %[wp] offset:7680\n\tds_read_b128 v[56:59], %[xp3] offset:16384\n\ts_waitcnt lgkmcnt(12)\n\tv_mfma_f32_16x16x32_bf16 v[32:35], v[64:67], v[60:63], v[32:35]\n\tds_read_b64_tr_b16 v[60:61], %[wp] offset:8192\n\tds_read_b64_tr_b16 v[62:63], %[wp] offset:8704\n\tds_read_b128 v[64:67], %[xp0] offset:32768\n\ts_waitcnt lgkmcnt(12)\n\tv_mfma_f32_16x16x32_bf16 v[32:35], v[72:75], v[68:71], v[32:35]\n\tds_read_b64_tr_b16 v[68:69], %[wp] offset:9216\n\tds_read_b64_tr_b16 v[70:71], %[wp] offset:9728\n\tds_read_b128 v[72:75], %[xp1] offset:32768\n\ts_waitcnt lgkmcnt(12)\n\tv_mfma_f32_16x16x32_bf16 v[32:35], v[40:43], v[36:39], v[32:35]\n\tds_read_b64_tr_b16 v[36:37], %[wp] offset:10240\n\tds_read_b64_tr_b16 v[38:39], %[wp] offset:10752\n\tds_read_b128 v[40:43], %[xp2] offset:32768\n\ts_waitcnt lgkmcnt(12)\n\tv_mfma_f32_16x16x32_bf16 v[32:35], v[48:51], v[44:47], v[32:35]\n\tds_read_b64_tr_b16 v[44:45], %[wp] offset:11264\n\tds_read_b64_tr_b16 v[46:47], %[wp] offset:11776\n\tds_read_b128 v[48:51], %[xp3] offset:32768\n\ts_waitcnt lgkmcnt(12)\n\tv_mfma_f32_16x16x32_bf16 v[32:35], v[56:59], v[52:55], v[32:35]\n\tds_read_b64_tr_b16 v[52:53], %[wp] offset:12288\n\tds_read_b64_tr_b16 v[54:55], %[wp] offset:12800\n\tds_read_b128 v[56:59], %[xp0] offset:49152\n\ts_waitcnt lgkmcnt(12)\n\tv_mfma_f32_16x16x32_bf16 v[32:35], v[64:67], v[60:63], v[32:35]\n\tds_read_b64_tr_b16 v[60:61], %[wp] offset:13312\n\tds_read_b64_tr_b16 v[62:63], %[wp] offset:13824\n\tds_read_b128 v[64:67], %[xp1] offset:49152\n\ts_waitcnt lgkmcnt(12)\n\tv_mfma_f32_16x16x32_bf16 v[32:35], v[72:75], v[68:71], v[32:35]\n\tds_read_b64_tr_b16 v[68:69], %[wp] offset:14336\n\tds_read_b64_tr_b16 v[70:71], %[wp] offset:14848\n\tds_read_b128 v[72:75], %[xp2] offset:49152\n\ts_waitcnt lgkmcnt(12)\n\tv_mfma_f32_16x16x32_bf16 v[32:35], v[40:43], v[36:39], v[32:35]\n\tds_read_b64_tr_b16 v[36:37], %[wp] offset:15360\n\tds_read_b64_tr_b16 v[38:39], %[wp] offset:15872\n\tds_read_b128 v[40:43], %[xp3] offset:49152\n\ts_waitcnt lgkmcnt(12)\n\tv_mfma_f32_16x16x32_bf16 v[32:35], v[48:51], v[44:47], v[32:35]\n\ts_waitcnt lgkmcnt(9)\n\tv_mfma_f32_16x16x32_bf16 v[32:35], v[56:59], v[52:55], v[32:35]\n\ts_waitcnt lgkmcnt(6)\n\tv_mfma_f32_16x16x32_bf16 v[32:35], v[64:67], v[60:63], v[32:35]\n\ts_waitcnt lgkmcnt(3)\n\tv_mfma_f32_16x16x32_bf16 v[32:35], v[72:75], v[68:71], v[32:35]\n\ts_waitcnt lgkmcnt(0)\n\tv_mfma_f32_16x16x32_bf16 v[32:35], v[40:43], v[36:39], v[32:35]\n\ts_nop 7\n\ts_nop 1" : "=&{v[32:35]}"(acc) : [wp] "v"(wp), [xp0] "v"(xp), [xp1] "v"(xp ^ 64), [xp2] "v"(xp ^ 128), [xp3] "v"(xp ^ 192) : "v36", "v37", "v38", "v39", "v40", "v41", "v42", "v43", "v44", "v45", "v46", "v47", "v48", "v49", "v50", "v51", "v52", "v53", "v54", "v55", "v56", "v57", "v58", "v59", "v60", "v61", "v62", "v63", "v64", "v65", "v66", "v67", "v68", "v69", "v70", "v71", "v72", "v73", "v74", "v75", "memory");
            const unsigned p0 = pk2(acc[0], acc[1]), p1 = pk2(acc[2], acc[3]);
            if (tb < 4) {
                const u32x4 pv = {p0, tag, p1, tag};
                STORE128(pv, rs, l * 16, WS_TG + ((tb * 2 + (tg >> 1)) * 4 + type * 2 + (tg & 1)) * 1024);
            } else if (type == 0) {
                const u32x2 a = __builtin_amdgcn_permlane32_swap(p0, p0, false, false), c = __builtin_amdgcn_permlane32_swap(p1, p1, false, false);
                const u32x4 pv = {a[0], c[0], a[1], c[1]};
                if (l < 32) STORE128(pv, rs, ((tg & 1) * 16 + l15 + 32 * (g4 & 1)) * 16, WS_KF + (tb * 2 + (tg >> 1)) * 1024);
            } else {
                const u32x2 a = __builtin_amdgcn_permlane16_swap(p0, p0, false, false), c = __builtin_amdgcn_permlane16_swap(p1, p1, false, false);
                const u32x4 pv = {a[0], c[0], a[1], c[1]};
                if (!(g4 & 1)) STORE128(pv, rs, (g4 >> 1) * 256 + l15 * 16, WS_VF + (tb * 4 + tg) * 512);
            }
            ST(2); AP(2);
            lds_signal(L, L_C5, l);
            ST(3); AP(3);
        }
        if (RW < 0 && w == 1 && type == 0) { WAIT_VM0(); ROUTER(); ST(2); }
        lds_wait_ge(L, L_C3, 4u);
        f32x16 acc;
        u32x4 f0, f1;
        {
            ST(4); AP(4);
#define QSTOP
#define QSTCL
            asm volatile("ds_read_b32 v48, %[cq] offset:0\n\ts_waitcnt lgkmcnt(0)\n\tv_readfirstlane_b32 s92, v48\n\ts_cmp_ge_u32 s92, 4\n\ts_cbranch_scc1 MQA_QGO0\n\ts_mov_b32 s93, 0x40000\n\tMQA_QSP0:\n\ts_sleep 1\n\tds_read_b32 v48, %[cq] offset:0\n\ts_sub_u32 s93, s93, 1\n\ts_waitcnt lgkmcnt(0)\n\tv_readfirstlane_b32 s92, v48\n\ts_cmp_eq_u32 s93, 0\n\ts_cbranch_scc1 MQA_QGO0\n\ts_cmp_ge_u32 s92, 4\n\ts_cbranch_scc0 MQA_QSP0\n\tMQA_QGO0:\n\tds_read_b64_tr_b16 v[100:101], %[a0] offset:0\n\tds_read_b64_tr_b16 v[102:103], %[a0] offset:512\n\tds_read_b128 v[104:107], %[b0]\n\tv_xor_b32 v49, 32, %[b0]\n\tds_read_b64_tr_b16 v[108:109], %[a0] offset:2048\n\tds_read_b64_tr_b16 v[110:111], %[a0] offset:2560\n\tds_read_b128 v[112:115], v49\n\tv_xor_b32 v49, 64, %[b0]\n\tds_read_b64_tr_b16 v[116:117], %[a0] offset:4096\n\tds_read_b64_tr_b16 v[118:119], %[a0] offset:4608\n\tds_read_b128 v[120:123], v49\n\tv_xor_b32 v49, 96, %[b0]\n\tds_read_b64_tr_b16 v[124:125], %[a0] offset:6144\n\tds_read_b64_tr_b16 v[126:127], %[a0] offset:6656\n\tds_read_b128 v[128:131], v49\n\tv_xor_b32 v49, 128, %[b0]\n\tds_read_b64_tr_b16 v[132:133], %[a0] offset:8192\n\tds_read_b64_tr_b16 v[134:135], %[a0] offset:8704\n\tds_read_b128 v[136:139], v49\n\ts_waitcnt lgkmcnt(14)\n\tds_read_b32 v48, %[cq] offset:4\n\ts_waitcnt lgkmcnt(13)\n\tv_mfma_f32_32x32x16_bf16 v[32:47], v[100:103], v[104:107], 0\n\ts_waitcnt lgkmcnt(12)\n\tv_xor_b32 v49, 160, %[b0]\n\tds_read_b64_tr_b16 v[100:101], %[a0] offset:10240\n\tds_read_b64_tr_b16 v[102:103], %[a0] offset:10752\n\tds_read_b128 v[104:107], v49\n\ts_waitcnt lgkmcnt(13)\n\tv_mfma_f32_32x32x16_bf16 v[32:47], v[108:111], v[112:115], v[32:47]\n\ts_waitcnt lgkmcnt(12)\n\tv_xor_b32 v49, 192, %[b0]\n\tds_read_b64_tr_b16 v[108:109], %[a0] offset:12288\n\tds_read_b64_tr_b16 v[110:111], %[a0] offset:12800\n\tds_read_b128 v[112:115], v49\n\ts_waitcnt lgkmcnt(13)\n\tv_mfma_f32_32x32x16_bf16 v[32:47], v[116:119], v[120:123], v[32:47]\n\ts_waitcnt lgkmcnt(12)\n\tv_xor_b32 v49, 224, %[b0]\n\tds_read_b64_tr_b16 v[116:117], %[a0] offset:14336\n\tds_read_b64_tr_b16 v[118:119], %[a0] offset:14848\n\tds_read_b128 v[120:123], v49\n\ts_waitcnt lgkmcnt(13)\n\tv_mfma_f32_32x32x16_bf16 v[32:47], v[124:127], v[128:131], v[32:47]\n\ts_cmp_eq_u32 %[df], 0\n\ts_cbranch_scc1 MQA_QNF\n\ts_waitcnt vmcnt(0)\n\ts_mov_b64 exec, %[fm]\n\tglobal_store_dword %[fa], %[ft], off sc1\n\ts_mov_b64 exec, -1\n\tMQA_QNF:\n\ts_waitcnt lgkmcnt(9)\n\tv_readfirstlane_b32 s92, v48\n\ts_cmp_ge_u32 s92, 4\n\ts_cbranch_scc1 MQA_QGO1\n\ts_mov_b32 s93, 0x40000\n\tMQA_QSP1:\n\ts_sleep 1\n\tds_read_b32 v48, %[cq] offset:4\n\ts_sub_u32 s93, s93, 1\n\ts_waitcnt lgkmcnt(0)\n\tv_readfirstlane_b32 s92, v48\n\ts_cmp_eq_u32 s93, 0\n\ts_cbranch_scc1 MQA_QGO1\n\ts_cmp_ge_u32 s92, 4\n\ts_cbranch_scc0 MQA_QSP1\n\tMQA_QGO1:\n\tds_read_b64_tr_b16 v[124:125], %[a1] offset:0\n\tds_read_b64_tr_b16 v[126:127], %[a1] offset:512\n\tds_read_b128 v[128:131], %[b1]\n\ts_waitcnt lgkmcnt(13)\n\tv_mfma_f32_32x32x16_bf16 v[32:47], v[132:135], v[136:139], v[32:47]\n\tv_xor_b32 v49, 32, %[b1]\n\tds_read_b64_tr_b16 v[132:133], %[a1] offset:2048\n\tds_read_b64_tr_b16 v[134:135], %[a1] offset:2560\n\tds_read_b128 v[136:139], v49\n\ts_waitcnt lgkmcnt(12)\n\tv_mfma_f32_32x32x16_bf16 v[32:47], v[100:103], v[104:107], v[32:47]\n\tv_xor_b32 v49, 64, %[b1]\n\tds_read_b64_tr_b16 v[100:101], %[a1] offset:4096\n\tds_read_b64_tr_b16 v[102:103], %[a1] offset:4608\n\tds_read_b128 v[104:107], v49\n\ts_waitcnt lgkmcnt(12)\n\tv_mfma_f32_32x32x16_bf16 v[32:47], v[108:111], v[112:115], v[32:47]\n\tv_xor_b32 v49, 96, %[b1]\n\tds_read_b64_tr_b16 v[108:109], %[a1] offset:6144\n\tds_read_b64_tr_b16 v[110:111], %[a1] offset:6656\n\tds_read_b128 v[112:115], v49\n\ts_waitcnt lgkmcnt(12)\n\tv_mfma_f32_32x32x16_bf16 v[32:47], v[116:119], v[120:123], v[32:47]\n\tv_xor_b32 v49, 128, %[b1]\n\tds_read_b64_tr_b16 v[116:117], %[a1] offset:8192\n\tds_read_b64_tr_b16 v[118:119], %[a1] offset:8704\n\tds_read_b128 v[120:123], v49\n\ts_waitcnt lgkmcnt(14)\n\tds_read_b32 v48, %[cq] offset:8\n\ts_waitcnt lgkmcnt(13)\n\tv_mfma_f32_32x32x16_bf16 v[32:47], v[124:127], v[128:131], v[32:47]\n\ts_waitcnt lgkmcnt(12)\n\tv_xor_b32 v49, 160, %[b1]\n\tds_read_b64_tr_b16 v[124:125], %[a1] offset:10240\n\tds_read_b64_tr_b16 v[126:127], %[a1] offset:10752\n\tds_read_b128 v[128:131], v49\n\ts_waitcnt lgkmcnt(13)\n\tv_mfma_f32_32x32x16_bf16 v[32:47], v[132:135], v[136:139], v[32:47]\n\ts_waitcnt lgkmcnt(12)\n\tv_xor_b32 v49, 192, %[b1]\n\tds_read_b64_tr_b16 v[132:133], %[a1] offset:12288\n\tds_read_b64_tr_b16 v[134:135], %[a1] offset:12800\n\tds_read_b128 v[136:139], v49\n\ts_waitcnt lgkmcnt(13)\n\tv_mfma_f32_32x32x16_bf16 v[32:47], v[100:103], v[104:107], v[32:47]\n\ts_waitcnt lgkmcnt(12)\n\tv_xor_b32 v49, 224, %[b1]\n\tds_read_b64_tr_b16 v[100:101], %[a1] offset:14336\n\tds_read_b64_tr_b16 v[102:103], %[a1] offset:14848\n\tds_read_b128 v[104:107], v49\n\ts_waitcnt lgkmcnt(13)\n\tv_mfma_f32_32x32x16_bf16 v[32:47], v[108:111], v[112:115], v[32:47]\n\ts_waitcnt lgkmcnt(9)\n\tv_readfirstlane_b32 s92, v48\n\ts_cmp_ge_u32 s92, 4\n\ts_cbranch_scc1 MQA_QGO2\n\ts_mov_b32 s93, 0x40000\n\tMQA_QSP2:\n\ts_sleep 1\n\tds_read_b32 v48, %[cq] offset:8\n\ts_sub_u32 s93, s93, 1\n\ts_waitcnt lgkmcnt(0)\n\tv_readfirstlane_b32 s92, v48\n\ts_cmp_eq_u32 s93, 0\n\ts_cbranch_scc1 MQA_QGO2\n\ts_cmp_ge_u32 s92, 4\n\ts_cbranch_scc0 MQA_QSP2\n\tMQA_QGO2:\n\tds_read_b64_tr_b16 v[108:109], %[a2] offset:0\n\tds_read_b64_tr_b16 v[110:111], %[a2] offset:512\n\tds_read_b128 v[112:115], %[b2]\n\ts_waitcnt lgkmcnt(13)\n\tv_mfma_f32_32x32x16_bf16 v[32:47], v[116:119], v[120:123], v[32:47]\n\tv_xor_b32 v49, 32, %[b2]\n\tds_read_b64_tr_b16 v[116:117], %[a2] offset:2048\n\tds_read_b64_tr_b16 v[118:119], %[a2] offset:2560\n\tds_read_b128 v[120:123], v49\n\ts_waitcnt lgkmcnt(12)\n\tv_mfma_f32_32x32x16_bf16 v[32:47], v[124:127], v[128:131], v[32:47]\n\tv_xor_b32 v49, 64, %[b2]\n\tds_read_b64_tr_b16 v[124:125], %[a2] offset:4096\n\tds_read_b64_tr_b16 v[126:127], %[a2] offset:4608\n\tds_read_b128 v[128:131], v49\n\ts_waitcnt lgkmcnt(12)\n\tv_mfma_f32_32x32x16_bf16 v[32:47], v[132:135], v[136:139], v[32:47]\n\tv_xor_b32 v49, 96, %[b2]\n\tds_read_b64_tr_b16 v[132:133], %[a2] offset:6144\n\tds_read_b64_tr_b16 v[134:135], %[a2] offset:6656\n\tds_read_b128 v[136:139], v49\n\ts_waitcnt lgkmcnt(12)\n\tv_mfma_f32_32x32x16_bf16 v[32:47], v[100:103], v[104:107], v[32:47]\n\tv_xor_b32 v49, 128, %[b2]\n\tds_read_b64_tr_b16 v[100:101], %[a2] offset:8192\n\tds_read_b64_tr_b16 v[102:103], %[a2] offset:8704\n\tds_read_b128 v[104:107], v49\n\ts_waitcnt lgkmcnt(14)\n\tds_read_b32 v48, %[cq] offset:12\n\ts_waitcnt lgkmcnt(13)\n\tv_mfma_f32_32x32x16_bf16 v[32:47], v[108:111], v[112:115], v[32:47]\n\ts_waitcnt lgkmcnt(12)\n\tv_xor_b32 v49, 160, %[b2]\n\tds_read_b64_tr_b16 v[108:109], %[a2] offset:10240\n\tds_read_b64_tr_b16 v[110:111], %[a2] offset:10752\n\tds_read_b128 v[112:115], v49\n\ts_waitcnt lgkmcnt(13)\n\tv_mfma_f32_32x32x16_bf16 v[32:47], v[116:119], v[120:123], v[32:47]\n\ts_waitcnt lgkmcnt(12)\n\tv_xor_b32 v49, 192, %[b2]\n\tds_read_b64_tr_b16 v[116:117], %[a2] offset:12288\n\tds_read_b64_tr_b16 v[118:119], %[a2] offset:12800\n\tds_read_b128 v[120:123], v49\n\ts_waitcnt lgkmcnt(13)\n\tv_mfma_f32_32x32x16_bf16 v[32:47], v[124:127], v[128:131], v[32:47]\n\ts_waitcnt lgkmcnt(12)\n\tv_xor_b32 v49, 224, %[b2]\n\tds_read_b64_tr_b16 v[124:125], %[a2] offset:14336\n\tds_read_b64_tr_b16 v[126:127], %[a2] offset:14848\n\tds_read_b128 v[128:131], v49\n\ts_waitcnt lgkmcnt(13)\n\tv_mfma_f32_32x32x16_bf16 v[32:47], v[132:135], v[136:139], v[32:47]\n\ts_waitcnt lgkmcnt(9)\n\tv_readfirstlane_b32 s92, v48\n\ts_cmp_ge_u32 s92, 4\n\ts_cbranch_scc1 MQA_QGO3\n\ts_mov_b32 s93, 0x40000\n\tMQA_QSP3:\n\ts_sleep 1\n\tds_read_b32 v48, %[cq] offset:12\n\ts_sub_u32 s93, s93, 1\n\ts_waitcnt lgkmcnt(0)\n\tv_readfirstlane_b32 s92, v48\n\ts_cmp_eq_u32 s93, 0\n\ts_cbranch_scc1 MQA_QGO3\n\ts_cmp_ge_u32 s92, 4\n\ts_cbranch_scc0 MQA_QSP3\n\tMQA_QGO3:\n\tds_read_b64_tr_b16 v[132:133], %[a3] offset:0\n\tds_read_b64_tr_b16 v[134:135], %[a3] offset:512\n\tds_read_b128 v[136:139], %[b3]\n\ts_waitcnt lgkmcnt(13)\n\tv_mfma_f32_32x32x16_bf16 v[32:47], v[100:103], v[104:107], v[32:47]\n\tv_xor_b32 v49, 32, %[b3]\n\tds_read_b64_tr_b16 v[100:101], %[a3] offset:2048\n\tds_read_b64_tr_b16 v[102:103], %[a3] offset:2560\n\tds_read_b128 v[104:107], v49\n\ts_waitcnt lgkmcnt(12)\n\tv_mfma_f32_32x32x16_bf16 v[32:47], v[108:111], v[112:115], v[32:47]\n\tv_xor_b32 v49, 64, %[b3]\n\tds_read_b64_tr_b16 v[108:109], %[a3] offset:4096\n\tds_read_b64_tr_b16 v[110:111], %[a3] offset:4608\n\tds_read_b128 v[112:115], v49\n\ts_waitcnt lgkmcnt(12)\n\tv_mfma_f32_32x32x16_bf16 v[32:47], v[116:119], v[120:123], v[32:47]\n\tv_xor_b32 v49, 96, %[b3]\n\tds_read_b64_tr_b16 v[116:117], %[a3] offset:6144\n\tds_read_b64_tr_b16 v[118:119], %[a3] offset:6656\n\tds_read_b128 v[120:123], v49\n\ts_waitcnt lgkmcnt(12)\n\tv_mfma_f32_32x32x16_bf16 v[32:47], v[124:127], v[128:131], v[32:47]\n\tv_xor_b32 v49, 128, %[b3]\n\tds_read_b64_tr_b16 v[124:125], %[a3] offset:8192\n\tds_read_b64_tr_b16 v[126:127], %[a3] offset:8704\n\tds_read_b128 v[128:131], v49\n\ts_waitcnt lgkmcnt(12)\n\tv_mfma_f32_32x32x16_bf16 v[32:47], v[132:135], v[136:139], v[32:47]\n\tv_xor_b32 v49, 160, %[b3]\n\tds_read_b64_tr_b16 v[132:133], %[a3] offset:10240\n\tds_read_b64_tr_b16 v[134:135], %[a3] offset:10752\n\tds_read_b128 v[136:139], v49\n\ts_waitcnt lgkmcnt(12)\n\tv_mfma_f32_32x32x16_bf16 v[32:47], v[100:103], v[104:107], v[32:47]\n\tv_xor_b32 v49, 192, %[b3]\n\tds_read_b64_tr_b16 v[100:101], %[a3] offset:12288\n\tds_read_b64_tr_b16 v[102:103], %[a3] offset:12800\n\tds_read_b128 v[104:107], v49\n\ts_waitcnt lgkmcnt(12)\n\tv_mfma_f32_32x32x16_bf16 v[32:47], v[108:111], v[112:115], v[32:47]\n\tv_xor_b32 v49, 224, %[b3]\n\tds_read_b64_tr_b16 v[108:109], %[a3] offset:14336\n\tds_read_b64_tr_b16 v[110:111], %[a3] offset:14848\n\tds_read_b128 v[112:115], v49\n\ts_waitcnt lgkmcnt(12)\n\tv_mfma_f32_32x32x16_bf16 v[32:47], v[116:119], v[120:123], v[32:47]\n\ts_waitcnt lgkmcnt(9)\n\tv_mfma_f32_32x32x16_bf16 v[32:47], v[124:127], v[128:131], v[32:47]\n\ts_waitcnt lgkmcnt(6)\n\tv_mfma_f32_32x32x16_bf16 v[32:47], v[132:135], v[136:139], v[32:47]\n\ts_waitcnt lgkmcnt(3)\n\tv_mfma_f32_32x32x16_bf16 v[32:47], v[100:103], v[104:107], v[32:47]\n\ts_waitcnt lgkmcnt(0)\n\tv_mfma_f32_32x32x16_bf16 v[32:47], v[108:111], v[112:115], v[32:47]\n\ts_nop 15\n\ts_nop 3" : "=&{v[32:47]}"(acc) QSTOP
                         : [a0] "v"(a0), [b0] "v"(b0), [a1] "v"(a1), [b1] "v"(b1), [a2] "v"(a2), [b2] "v"(b2), [a3] "v"(a3), [b3] "v"(b3),
                           [cq] "v"(LB + L_CQ), [df] "s"(df), [fa] "v"(fa), [ft] "v"(tag), [fm] "s"((1ull << FREP) - 1) : "v100", "v101", "v102", "v103", "v104", "v105", "v106", "v107", "v108", "v109", "v110", "v111", "v112", "v113", "v114", "v115", "v116", "v117", "v118", "v119", "v120", "v121", "v122", "v123", "v124", "v125", "v126", "v127", "v128", "v129", "v130", "v131", "v132", "v133", "v134", "v135", "v136", "v137", "v138", "v139", "v48", "v49", "s92", "s93", "scc", "memory" QSTCL);
        }
        if (w == 2) { WAIT_VM0(); f0 = tr_frag(L + L_WOST + 8 * l); f1 = tr_frag(L + L_WOST + 1024 + 8 * l); }
        const float sc = 0.25f * 1.4426950408889634f;
        u32x4 qa, qb;
        #pragma unroll
        for (int i = 0; i < 4; ++i) { qa[i] = pk2(acc[2 * i] * sc, acc[2 * i + 1] * sc); qb[i] = pk2(acc[8 + 2 * i] * sc, acc[9 + 2 * i] * sc); }
        const int cg = hg & 3, n0 = cg * 128 + 16 * w, tok0 = tb * 64 + (hg >> 2) * 32 + l15;
        const unsigned xo = tok0 * 1024 + (n0 + 4 * g4) * 2, go = tok0 * 4;
        unsigned wso = WS_WOF + (cg * 8 + w) * 16 * 1024;
        asm volatile("" : "+{v[184:187]}"(qa), "+{v[188:191]}"(qb) : "v"(xo), "v"(go), "s"(wso));
        ST(5); AP(5);
        BARRIER();
        ST(6); AP(6);
        if (w == 2) {
            STORE128(f0, rs, l * 16, WS_WOF + (2 * b) * 1024);
            STORE128(f1, rs, l * 16, WS_WOF + (2 * b + 1) * 1024);
        }
        __builtin_amdgcn_s_setprio(APR);
        ST(7); STC(30); AP(7);
        f32x16 o0, o1;
        asm volatile("ds_read_b128 v[96:99], v193 offset:0\n\tds_read_b128 v[168:171], v194 offset:0\n\tds_read_b128 v[100:103], v193 offset:2080\n\tds_read_b128 v[176:179], v195 offset:0\n\tv_mov_b64 v[80:81], 0\n\tv_mov_b64 v[82:83], 0\n\tv_mov_b64 v[84:85], 0\n\tv_mov_b64 v[86:87], 0\n\tv_mov_b64 v[88:89], 0\n\tv_mov_b64 v[90:91], 0\n\tv_mov_b64 v[92:93], 0\n\tv_mov_b64 v[94:95], 0\n\ts_waitcnt lgkmcnt(3)\n\tv_mfma_f32_32x32x16_bf16 v[32:47], v[96:99], v[184:187], 0\n\tv_mov_b64 v[64:65], 0\n\tv_mov_b64 v[66:67], 0\n\tv_mov_b64 v[68:69], 0\n\tv_mov_b64 v[70:71], 0\n\tv_mov_b64 v[72:73], 0\n\tv_mov_b64 v[74:75], 0\n\tv_mov_b64 v[76:77], 0\n\tv_mov_b64 v[78:79], 0\n\tv_mov_b64 v[56:57], 0\n\tv_mov_b64 v[58:59], 0\n\tv_mov_b64 v[60:61], 0\n\tv_mov_b64 v[62:63], 0\n\tv_mov_b64 v[180:181], 0\n\tv_mov_b64 v[182:183], 0\n\tv_mov_b32 v192, 0xdddddddd\n\tv_mov_b32 v199, 1\n\tv_exp_f32 v32, v32\n\tv_exp_f32 v33, v33\n\tv_smfmac_f32_32x32x32_bf16 v[80:95], v[180:183], v[56:63], v192\n\tv_exp_f32 v34, v34\n\tv_exp_f32 v35, v35\n\tv_exp_f32 v36, v36\n\tv_exp_f32 v37, v37\n\tv_mfma_f32_32x32x16_bf16 v[48:63], v[96:99], v[188:191], 0\n\tds_read_b128 v[96:99], v193 offset:4160\n\tv_exp_f32 v38, v38\n\tv_exp_f32 v39, v39\n\tv_exp_f32 v40, v40\n\tv_exp_f32 v41, v41\n\ts_waitcnt lgkmcnt(1)\n\tv_smfmac_f32_32x32x32_bf16 v[64:79], v[168:171], v[32:39], v192\n\tds_read_b128 v[172:175], v194 offset:2080\n\tv_exp_f32 v42, v42\n\tv_exp_f32 v43, v43\n\tv_exp_f32 v44, v44\n\tv_exp_f32 v45, v45\n\tv_exp_f32 v46, v46\n\tv_exp_f32 v47, v47\n\tv_exp_f32 v48, v48\n\tv_exp_f32 v49, v49\n\tv_smfmac_f32_32x32x32_bf16 v[64:79], v[176:179], v[40:47], v192\n\tv_exp_f32 v50, v50\n\tv_exp_f32 v51, v51\n\tv_exp_f32 v52, v52\n\tv_exp_f32 v53, v53\n\tv_mfma_f32_32x32x16_bf16 v[32:47], v[100:103], v[184:187], 0\n\tds_read_b128 v[180:183], v195 offset:2080\n\tv_exp_f32 v54, v54\n\tv_exp_f32 v55, v55\n\tv_exp_f32 v56, v56\n\tv_exp_f32 v57, v57\n\tv_smfmac_f32_32x32x32_bf16 v[80:95], v[168:171], v[48:55], v192\n\tv_exp_f32 v58, v58\n\tv_exp_f32 v59, v59\n\tv_exp_f32 v60, v60\n\tv_exp_f32 v61, v61\n\tv_exp_f32 v62, v62\n\tv_exp_f32 v63, v63\n\tv_exp_f32 v32, v32\n\tv_exp_f32 v33, v33\n\tv_smfmac_f32_32x32x32_bf16 v[80:95], v[176:179], v[56:63], v192\n\tv_exp_f32 v34, v34\n\tv_exp_f32 v35, v35\n\tv_exp_f32 v36, v36\n\tv_exp_f32 v37, v37\n\tv_mfma_f32_32x32x16_bf16 v[48:63], v[100:103], v[188:191], 0\n\tds_read_b128 v[100:103], v193 offset:6240\n\tv_exp_f32 v38, v38\n\tv_exp_f32 v39, v39\n\tv_exp_f32 v40, v40\n\tv_exp_f32 v41, v41\n\ts_waitcnt lgkmcnt(1)\n\tv_smfmac_f32_32x32x32_bf16 v[64:79], v[172:175], v[32:39], v192\n\tds_read_b128 v[168:171], v194 offset:4160\n\tv_exp_f32 v42, v42\n\tv_exp_f32 v43, v43\n\tv_exp_f32 v44, v44\n\tv_exp_f32 v45, v45\n\tv_exp_f32 v46, v46\n\tv_exp_f32 v47, v47\n\tv_exp_f32 v48, v48\n\tv_exp_f32 v49, v49\n\tv_smfmac_f32_32x32x32_bf16 v[64:79], v[180:183], v[40:47], v192\n\tv_exp_f32 v50, v50\n\tv_exp_f32 v51, v51\n\tv_exp_f32 v52, v52\n\tv_exp_f32 v53, v53\n\tv_mfma_f32_32x32x16_bf16 v[32:47], v[96:99], v[184:187], 0\n\tds_read_b128 v[176:179], v195 offset:4160\n\tv_exp_f32 v54, v54\n\tv_exp_f32 v55, v55\n\tv_exp_f32 v56, v56\n\tv_exp_f32 v57, v57\n\tv_smfmac_f32_32x32x32_bf16 v[80:95], v[172:175], v[48:55], v192\n\tv_exp_f32 v58, v58\n\tv_exp_f32 v59, v59\n\tv_exp_f32 v60, v60\n\tv_exp_f32 v61, v61\n\tv_exp_f32 v62, v62\n\tv_exp_f32 v63, v63\n\tv_exp_f32 v32, v32\n\tv_exp_f32 v33, v33\n\tv_smfmac_f32_32x32x32_bf16 v[80:95], v[180:183], v[56:63], v192\n\tv_exp_f32 v34, v34\n\tv_exp_f32 v35, v35\n\tv_exp_f32 v36, v36\n\tv_exp_f32 v37, v37\n\tv_mfma_f32_32x32x16_bf16 v[48:63], v[96:99], v[188:191], 0\n\tds_read_b128 v[96:99], v193 offset:8320\n\tv_exp_f32 v38, v38\n\tv_exp_f32 v39, v39\n\tv_exp_f32 v40, v40\n\tv_exp_f32 v41, v41\n\ts_waitcnt lgkmcnt(1)\n\tv_smfmac_f32_32x32x32_bf16 v[64:79], v[168:171], v[32:39], v192\n\tds_read_b128 v[172:175], v194 offset:6240\n\tv_exp_f32 v42, v42\n\tv_exp_f32 v43, v43\n\tv_exp_f32 v44, v44\n\tv_exp_f32 v45, v45\n\tv_exp_f32 v46, v46\n\tv_exp_f32 v47, v47\n\tv_exp_f32 v48, v48\n\tv_exp_f32 v49, v49\n\tv_smfmac_f32_32x32x32_bf16 v[64:79], v[176:179], v[40:47], v192\n\tv_exp_f32 v50, v50\n\tv_exp_f32 v51, v51\n\tv_exp_f32 v52, v52\n\tv_exp_f32 v53, v53\n\tv_mfma_f32_32x32x16_bf16 v[32:47], v[100:103], v[184:187], 0\n\tds_read_b128 v[180:183], v195 offset:6240\n\tv_exp_f32 v54, v54\n\tv_exp_f32 v55, v55\n\tv_exp_f32 v56, v56\n\tv_exp_f32 v57, v57\n\tv_smfmac_f32_32x32x32_bf16 v[80:95], v[168:171], v[48:55], v192\n\tv_exp_f32 v58, v58\n\tv_exp_f32 v59, v59\n\tv_exp_f32 v60, v60\n\tv_exp_f32 v61, v61\n\tv_exp_f32 v62, v62\n\tv_exp_f32 v63, v63\n\tv_exp_f32 v32, v32\n\tv_exp_f32 v33, v33\n\tv_smfmac_f32_32x32x32_bf16 v[80:95], v[176:179], v[56:63], v192\n\tv_exp_f32 v34, v34\n\tv_exp_f32 v35, v35\n\tv_exp_f32 v36, v36\n\tv_exp_f32 v37, v37\n\tv_mfma_f32_32x32x16_bf16 v[48:63], v[100:103], v[188:191], 0\n\tds_read_b128 v[100:103], v193 offset:10400\n\tv_exp_f32 v38, v38\n\tv_exp_f32 v39, v39\n\tv_exp_f32 v40, v40\n\tv_exp_f32 v41, v41\n\ts_waitcnt lgkmcnt(1)\n\tv_smfmac_f32_32x32x32_bf16 v[64:79], v[172:175], v[32:39], v192\n\tds_read_b128 v[168:171], v194 offset:8320\n\tv_exp_f32 v42, v42\n\tv_exp_f32 v43, v43\n\tv_exp_f32 v44, v44\n\tv_exp_f32 v45, v45\n\tv_exp_f32 v46, v46\n\tv_exp_f32 v47, v47\n\tv_exp_f32 v48, v48\n\tv_exp_f32 v49, v49\n\tv_smfmac_f32_32x32x32_bf16 v[64:79], v[180:183], v[40:47], v192\n\tv_exp_f32 v50, v50\n\tv_exp_f32 v51, v51\n\tv_exp_f32 v52, v52\n\tv_exp_f32 v53, v53\n\tv_mfma_f32_32x32x16_bf16 v[32:47], v[96:99], v[184:187], 0\n\tds_read_b128 v[176:179], v195 offset:8320\n\tv_exp_f32 v54, v54\n\tv_exp_f32 v55, v55\n\tv_exp_f32 v56, v56\n\tv_exp_f32 v57, v57\n\tv_smfmac_f32_32x32x32_bf16 v[80:95], v[172:175], v[48:55], v192\n\tv_exp_f32 v58, v58\n\tv_exp_f32 v59, v59\n\tv_exp_f32 v60, v60\n\tv_exp_f32 v61, v61\n\tv_exp_f32 v62, v62\n\tv_exp_f32 v63, v63\n\tv_exp_f32 v32, v32\n\tv_exp_f32 v33, v33\n\tv_smfmac_f32_32x32x32_bf16 v[80:95], v[180:183], v[56:63], v192\n\tv_exp_f32 v34, v34\n\tv_exp_f32 v35, v35\n\tv_exp_f32 v36, v36\n\tv_exp_f32 v37, v37\n\tv_mfma_f32_32x32x16_bf16 v[48:63], v[96:99], v[188:191], 0\n\tds_read_b128 v[96:99], v193 offset:12480\n\tv_exp_f32 v38, v38\n\tv_exp_f32 v39, v39\n\tv_exp_f32 v40, v40\n\tv_exp_f32 v41, v41\n\ts_waitcnt lgkmcnt(1)\n\tv_smfmac_f32_32x32x32_bf16 v[64:79], v[168:171], v[32:39], v192\n\tds_read_b128 v[172:175], v194 offset:10400\n\tv_exp_f32 v42, v42\n\tv_exp_f32 v43, v43\n\tv_exp_f32 v44, v44\n\tv_exp_f32 v45, v45\n\tv_exp_f32 v46, v46\n\tv_exp_f32 v47, v47\n\tv_exp_f32 v48, v48\n\tv_exp_f32 v49, v49\n\tv_smfmac_f32_32x32x32_bf16 v[64:79], v[176:179], v[40:47], v192\n\tv_exp_f32 v50, v50\n\tv_exp_f32 v51, v51\n\tv_exp_f32 v52, v52\n\tv_exp_f32 v53, v53\n\tv_mfma_f32_32x32x16_bf16 v[32:47], v[100:103], v[184:187], 0\n\tds_read_b128 v[180:183], v195 offset:10400\n\tv_exp_f32 v54, v54\n\tv_exp_f32 v55, v55\n\tv_exp_f32 v56, v56\n\tv_exp_f32 v57, v57\n\tv_smfmac_f32_32x32x32_bf16 v[80:95], v[168:171], v[48:55], v192\n\tv_exp_f32 v58, v58\n\tv_exp_f32 v59, v59\n\tv_exp_f32 v60, v60\n\tv_exp_f32 v61, v61\n\tv_exp_f32 v62, v62\n\tv_exp_f32 v63, v63\n\tv_exp_f32 v32, v32\n\tv_exp_f32 v33, v33\n\tv_smfmac_f32_32x32x32_bf16 v[80:95], v[176:179], v[56:63], v192\n\tv_exp_f32 v34, v34\n\tv_exp_f32 v35, v35\n\tv_exp_f32 v36, v36\n\tv_exp_f32 v37, v37\n\tv_mfma_f32_32x32x16_bf16 v[48:63], v[100:103], v[188:191], 0\n\tds_read_b128 v[100:103], v193 offset:14560\n\tv_exp_f32 v38, v38\n\tv_exp_f32 v39, v39\n\tv_exp_f32 v40, v40\n\tv_exp_f32 v41, v41\n\ts_waitcnt lgkmcnt(1)\n\tv_smfmac_f32_32x32x32_bf16 v[64:79], v[172:175], v[32:39], v192\n\tds_read_b128 v[168:171], v194 offset:12480\n\tds_read_b32 v197, v198 offset:4\n\tv_exp_f32 v42, v42\n\tv_exp_f32 v43, v43\n\tv_exp_f32 v44, v44\n\tv_exp_f32 v45, v45\n\tv_exp_f32 v46, v46\n\tv_exp_f32 v47, v47\n\tv_exp_f32 v48, v48\n\tv_exp_f32 v49, v49\n\tv_smfmac_f32_32x32x32_bf16 v[64:79], v[180:183], v[40:47], v192\n\tv_exp_f32 v50, v50\n\tv_exp_f32 v51, v51\n\tv_exp_f32 v52, v52\n\tv_exp_f32 v53, v53\n\tv_mfma_f32_32x32x16_bf16 v[32:47], v[96:99], v[184:187], 0\n\tds_read_b128 v[176:179], v195 offset:12480\n\tv_exp_f32 v54, v54\n\tv_exp_f32 v55, v55\n\tv_exp_f32 v56, v56\n\tv_exp_f32 v57, v57\n\tv_smfmac_f32_32x32x32_bf16 v[80:95], v[172:175], v[48:55], v192\n\tv_exp_f32 v58, v58\n\tv_exp_f32 v59, v59\n\tv_exp_f32 v60, v60\n\tv_exp_f32 v61, v61\n\tv_exp_f32 v62, v62\n\tv_exp_f32 v63, v63\n\tv_exp_f32 v32, v32\n\tv_exp_f32 v33, v33\n\tv_smfmac_f32_32x32x32_bf16 v[80:95], v[180:183], v[56:63], v192\n\ts_waitcnt lgkmcnt(1)\n\tv_readfirstlane_b32 s78, v197\n\tv_exp_f32 v34, v34\n\tv_exp_f32 v35, v35\n\tv_exp_f32 v36, v36\n\ts_cmp_ge_u32 s78, 4\n\ts_cbranch_scc0 MQA_SP1\n\tMQA_BK1:\n\tv_exp_f32 v37, v37\n\tv_mfma_f32_32x32x16_bf16 v[48:63], v[96:99], v[188:191], 0\n\tds_read_b128 v[96:99], v193 offset:16640\n\tv_exp_f32 v38, v38\n\tv_exp_f32 v39, v39\n\tv_exp_f32 v40, v40\n\tv_exp_f32 v41, v41\n\ts_waitcnt lgkmcnt(1)\n\tv_smfmac_f32_32x32x32_bf16 v[64:79], v[168:171], v[32:39], v192\n\tds_read_b128 v[172:175], v194 offset:14560\n\tv_exp_f32 v42, v42\n\tv_exp_f32 v43, v43\n\tv_exp_f32 v44, v44\n\tv_exp_f32 v45, v45\n\tv_exp_f32 v46, v46\n\tv_exp_f32 v47, v47\n\tv_exp_f32 v48, v48\n\tv_exp_f32 v49, v49\n\tv_smfmac_f32_32x32x32_bf16 v[64:79], v[176:179], v[40:47], v192\n\tv_exp_f32 v50, v50\n\tv_exp_f32 v51, v51\n\tv_exp_f32 v52, v52\n\tv_exp_f32 v53, v53\n\tv_mfma_f32_32x32x16_bf16 v[32:47], v[100:103], v[184:187], 0\n\tds_read_b128 v[180:183], v195 offset:14560\n\tv_exp_f32 v54, v54\n\tv_exp_f32 v55, v55\n\tv_exp_f32 v56, v56\n\tv_exp_f32 v57, v57\n\tv_smfmac_f32_32x32x32_bf16 v[80:95], v[168:171], v[48:55], v192\n\tv_exp_f32 v58, v58\n\tv_exp_f32 v59, v59\n\tv_exp_f32 v60, v60\n\tv_exp_f32 v61, v61\n\tv_exp_f32 v62, v62\n\tv_exp_f32 v63, v63\n\tv_exp_f32 v32, v32\n\tv_exp_f32 v33, v33\n\tv_smfmac_f32_32x32x32_bf16 v[80:95], v[176:179], v[56:63], v192\n\tv_exp_f32 v34, v34\n\tv_exp_f32 v35, v35\n\tv_exp_f32 v36, v36\n\tv_exp_f32 v37, v37\n\tv_mfma_f32_32x32x16_bf16 v[48:63], v[100:103], v[188:191], 0\n\tds_read_b128 v[100:103], v193 offset:18720\n\tv_exp_f32 v38, v38\n\tv_exp_f32 v39, v39\n\tv_exp_f32 v40, v40\n\tv_exp_f32 v41, v41\n\ts_waitcnt lgkmcnt(1)\n\tv_smfmac_f32_32x32x32_bf16 v[64:79], v[172:175], v[32:39], v192\n\tds_read_b128 v[168:171], v194 offset:16640\n\tv_exp_f32 v42, v42\n\tv_exp_f32 v43, v43\n\tv_exp_f32 v44, v44\n\tv_exp_f32 v45, v45\n\tv_exp_f32 v46, v46\n\tv_exp_f32 v47, v47\n\tv_exp_f32 v48, v48\n\tv_exp_f32 v49, v49\n\tv_smfmac_f32_32x32x32_bf16 v[64:79], v[180:183], v[40:47], v192\n\tv_exp_f32 v50, v50\n\tv_exp_f32 v51, v51\n\tv_exp_f32 v52, v52\n\tv_exp_f32 v53, v53\n\tv_mfma_f32_32x32x16_bf16 v[32:47], v[96:99], v[184:187], 0\n\tds_read_b128 v[176:179], v195 offset:16640\n\tv_exp_f32 v54, v54\n\tv_exp_f32 v55, v55\n\tv_exp_f32 v56, v56\n\tv_exp_f32 v57, v57\n\tv_smfmac_f32_32x32x32_bf16 v[80:95], v[172:175], v[48:55], v192\n\tv_exp_f32 v58, v58\n\tv_exp_f32 v59, v59\n\tv_exp_f32 v60, v60\n\tv_exp_f32 v61, v61\n\tv_exp_f32 v62, v62\n\tv_exp_f32 v63, v63\n\tv_exp_f32 v32, v32\n\tv_exp_f32 v33, v33\n\tv_smfmac_f32_32x32x32_bf16 v[80:95], v[180:183], v[56:63], v192\n\tv_exp_f32 v34, v34\n\tv_exp_f32 v35, v35\n\tv_exp_f32 v36, v36\n\tv_exp_f32 v37, v37\n\tv_mfma_f32_32x32x16_bf16 v[48:63], v[96:99], v[188:191], 0\n\tds_read_b128 v[96:99], v193 offset:20800\n\tv_exp_f32 v38, v38\n\tv_exp_f32 v39, v39\n\tv_exp_f32 v40, v40\n\tv_exp_f32 v41, v41\n\ts_waitcnt lgkmcnt(1)\n\tv_smfmac_f32_32x32x32_bf16 v[64:79], v[168:171], v[32:39], v192\n\tds_read_b128 v[172:175], v194 offset:18720\n\tv_exp_f32 v42, v42\n\tv_exp_f32 v43, v43\n\tv_exp_f32 v44, v44\n\tv_exp_f32 v45, v45\n\tv_exp_f32 v46, v46\n\tv_exp_f32 v47, v47\n\tv_exp_f32 v48, v48\n\tv_exp_f32 v49, v49\n\tv_smfmac_f32_32x32x32_bf16 v[64:79], v[176:179], v[40:47], v192\n\tv_exp_f32 v50, v50\n\tv_exp_f32 v51, v51\n\tv_exp_f32 v52, v52\n\tv_exp_f32 v53, v53\n\tv_mfma_f32_32x32x16_bf16 v[32:47], v[100:103], v[184:187], 0\n\tds_read_b128 v[180:183], v195 offset:18720\n\tv_exp_f32 v54, v54\n\tv_exp_f32 v55, v55\n\tv_exp_f32 v56, v56\n\tv_exp_f32 v57, v57\n\tv_smfmac_f32_32x32x32_bf16 v[80:95], v[168:171], v[48:55], v192\n\ts_waitcnt vmcnt(0)\n\ts_mov_b64 exec, 1\n\tds_add_u32 v198, v199 offset:20\n\ts_mov_b64 exec, -1\n\tv_exp_f32 v58, v58\n\tv_exp_f32 v59, v59\n\tv_exp_f32 v60, v60\n\tv_exp_f32 v61, v61\n\tv_exp_f32 v62, v62\n\tv_exp_f32 v63, v63\n\tv_exp_f32 v32, v32\n\tv_exp_f32 v33, v33\n\tv_smfmac_f32_32x32x32_bf16 v[80:95], v[176:179], v[56:63], v192\n\tv_exp_f32 v34, v34\n\tv_exp_f32 v35, v35\n\tv_exp_f32 v36, v36\n\tv_exp_f32 v37, v37\n\tv_mfma_f32_32x32x16_bf16 v[48:63], v[100:103], v[188:191], 0\n\tds_read_b128 v[100:103], v193 offset:22880\n\tv_exp_f32 v38, v38\n\tv_exp_f32 v39, v39\n\tv_exp_f32 v40, v40\n\tv_exp_f32 v41, v41\n\ts_waitcnt lgkmcnt(2)\n\tv_smfmac_f32_32x32x32_bf16 v[64:79], v[172:175], v[32:39], v192\n\tds_read_b128 v[168:171], v194 offset:20800\n\tv_exp_f32 v42, v42\n\tv_exp_f32 v43, v43\n\tv_exp_f32 v44, v44\n\tv_exp_f32 v45, v45\n\tv_exp_f32 v46, v46\n\tv_exp_f32 v47, v47\n\tv_exp_f32 v48, v48\n\tv_exp_f32 v49, v49\n\tv_smfmac_f32_32x32x32_bf16 v[64:79], v[180:183], v[40:47], v192\n\tv_exp_f32 v50, v50\n\tv_exp_f32 v51, v51\n\tv_exp_f32 v52, v52\n\tv_exp_f32 v53, v53\n\tv_mfma_f32_32x32x16_bf16 v[32:47], v[96:99], v[184:187], 0\n\tds_read_b128 v[176:179], v195 offset:20800\n\tv_exp_f32 v54, v54\n\tv_exp_f32 v55, v55\n\tv_exp_f32 v56, v56\n\tv_exp_f32 v57, v57\n\tv_smfmac_f32_32x32x32_bf16 v[80:95], v[172:175], v[48:55], v192\n\tv_exp_f32 v58, v58\n\tv_exp_f32 v59, v59\n\tv_exp_f32 v60, v60\n\tv_exp_f32 v61, v61\n\tv_exp_f32 v62, v62\n\tv_exp_f32 v63, v63\n\tv_exp_f32 v32, v32\n\tv_exp_f32 v33, v33\n\tv_smfmac_f32_32x32x32_bf16 v[80:95], v[180:183], v[56:63], v192\n\tv_exp_f32 v34, v34\n\tv_exp_f32 v35, v35\n\tv_exp_f32 v36, v36\n\tv_exp_f32 v37, v37\n\tv_mfma_f32_32x32x16_bf16 v[48:63], v[96:99], v[188:191], 0\n\tds_read_b128 v[96:99], v193 offset:24960\n\tv_exp_f32 v38, v38\n\tv_exp_f32 v39, v39\n\tv_exp_f32 v40, v40\n\tv_exp_f32 v41, v41\n\ts_waitcnt lgkmcnt(1)\n\tv_smfmac_f32_32x32x32_bf16 v[64:79], v[168:171], v[32:39], v192\n\tds_read_b128 v[172:175], v194 offset:22880\n\tv_exp_f32 v42, v42\n\tv_exp_f32 v43, v43\n\tv_exp_f32 v44, v44\n\tv_exp_f32 v45, v45\n\tv_exp_f32 v46, v46\n\tv_exp_f32 v47, v47\n\tv_exp_f32 v48, v48\n\tv_exp_f32 v49, v49\n\tv_smfmac_f32_32x32x32_bf16 v[64:79], v[176:179], v[40:47], v192\n\tv_exp_f32 v50, v50\n\tv_exp_f32 v51, v51\n\tv_exp_f32 v52, v52\n\tv_exp_f32 v53, v53\n\tv_mfma_f32_32x32x16_bf16 v[32:47], v[100:103], v[184:187], 0\n\tds_read_b128 v[180:183], v195 offset:22880\n\tv_exp_f32 v54, v54\n\tv_exp_f32 v55, v55\n\tv_exp_f32 v56, v56\n\tv_exp_f32 v57, v57\n\tv_smfmac_f32_32x32x32_bf16 v[80:95], v[168:171], v[48:55], v192\n\tv_exp_f32 v58, v58\n\tv_exp_f32 v59, v59\n\tv_exp_f32 v60, v60\n\tv_exp_f32 v61, v61\n\tv_exp_f32 v62, v62\n\tv_exp_f32 v63, v63\n\tv_exp_f32 v32, v32\n\tv_exp_f32 v33, v33\n\tv_smfmac_f32_32x32x32_bf16 v[80:95], v[176:179], v[56:63], v192\n\tv_exp_f32 v34, v34\n\tv_exp_f32 v35, v35\n\tv_exp_f32 v36, v36\n\tv_exp_f32 v37, v37\n\tv_mfma_f32_32x32x16_bf16 v[48:63], v[100:103], v[188:191], 0\n\tds_read_b128 v[100:103], v193 offset:27040\n\tv_exp_f32 v38, v38\n\tv_exp_f32 v39, v39\n\tv_exp_f32 v40, v40\n\tv_exp_f32 v41, v41\n\ts_waitcnt lgkmcnt(1)\n\tv_smfmac_f32_32x32x32_bf16 v[64:79], v[172:175], v[32:39], v192\n\tds_read_b128 v[168:171], v194 offset:24960\n\tv_exp_f32 v42, v42\n\tv_exp_f32 v43, v43\n\tv_exp_f32 v44, v44\n\tv_exp_f32 v45, v45\n\tv_exp_f32 v46, v46\n\tv_exp_f32 v47, v47\n\tv_exp_f32 v48, v48\n\tv_exp_f32 v49, v49\n\tv_smfmac_f32_32x32x32_bf16 v[64:79], v[180:183], v[40:47], v192\n\tv_exp_f32 v50, v50\n\tv_exp_f32 v51, v51\n\tv_exp_f32 v52, v52\n\tv_exp_f32 v53, v53\n\tv_mfma_f32_32x32x16_bf16 v[32:47], v[96:99], v[184:187], 0\n\tds_read_b128 v[176:179], v195 offset:24960\n\tv_exp_f32 v54, v54\n\tv_exp_f32 v55, v55\n\tv_exp_f32 v56, v56\n\tv_exp_f32 v57, v57\n\tv_smfmac_f32_32x32x32_bf16 v[80:95], v[172:175], v[48:55], v192\n\tv_exp_f32 v58, v58\n\tv_exp_f32 v59, v59\n\tv_exp_f32 v60, v60\n\tv_exp_f32 v61, v61\n\tv_exp_f32 v62, v62\n\tv_exp_f32 v63, v63\n\tv_exp_f32 v32, v32\n\tv_exp_f32 v33, v33\n\tv_smfmac_f32_32x32x32_bf16 v[80:95], v[180:183], v[56:63], v192\n\tv_exp_f32 v34, v34\n\tv_exp_f32 v35, v35\n\tv_exp_f32 v36, v36\n\tv_exp_f32 v37, v37\n\tv_mfma_f32_32x32x16_bf16 v[48:63], v[96:99], v[188:191], 0\n\tds_read_b128 v[96:99], v193 offset:29120\n\tv_exp_f32 v38, v38\n\tv_exp_f32 v39, v39\n\tv_exp_f32 v40, v40\n\tv_exp_f32 v41, v41\n\ts_waitcnt lgkmcnt(1)\n\tv_smfmac_f32_32x32x32_bf16 v[64:79], v[168:171], v[32:39], v192\n\tds_read_b128 v[172:175], v194 offset:27040\n\tv_exp_f32 v42, v42\n\tv_exp_f32 v43, v43\n\tv_exp_f32 v44, v44\n\tv_exp_f32 v45, v45\n\tv_exp_f32 v46, v46\n\tv_exp_f32 v47, v47\n\tv_exp_f32 v48, v48\n\tv_exp_f32 v49, v49\n\tv_smfmac_f32_32x32x32_bf16 v[64:79], v[176:179], v[40:47], v192\n\tv_exp_f32 v50, v50\n\tv_exp_f32 v51, v51\n\tv_exp_f32 v52, v52\n\tv_exp_f32 v53, v53\n\tv_mfma_f32_32x32x16_bf16 v[32:47], v[100:103], v[184:187], 0\n\tds_read_b128 v[180:183], v195 offset:27040\n\tv_exp_f32 v54, v54\n\tv_exp_f32 v55, v55\n\tv_exp_f32 v56, v56\n\tv_exp_f32 v57, v57\n\tv_smfmac_f32_32x32x32_bf16 v[80:95], v[168:171], v[48:55], v192\n\tv_exp_f32 v58, v58\n\tv_exp_f32 v59, v59\n\tv_exp_f32 v60, v60\n\tv_exp_f32 v61, v61\n\tv_exp_f32 v62, v62\n\tv_exp_f32 v63, v63\n\tv_exp_f32 v32, v32\n\tv_exp_f32 v33, v33\n\tv_smfmac_f32_32x32x32_bf16 v[80:95], v[176:179], v[56:63], v192\n\tv_exp_f32 v34, v34\n\tv_exp_f32 v35, v35\n\tv_exp_f32 v36, v36\n\tv_exp_f32 v37, v37\n\tv_mfma_f32_32x32x16_bf16 v[48:63], v[100:103], v[188:191], 0\n\tds_read_b128 v[100:103], v193 offset:31200\n\tv_exp_f32 v38, v38\n\tv_exp_f32 v39, v39\n\tv_exp_f32 v40, v40\n\tv_exp_f32 v41, v41\n\ts_waitcnt lgkmcnt(1)\n\tv_smfmac_f32_32x32x32_bf16 v[64:79], v[172:175], v[32:39], v192\n\tds_read_b128 v[168:171], v194 offset:29120\n\tv_exp_f32 v42, v42\n\tv_exp_f32 v43, v43\n\tv_exp_f32 v44, v44\n\tv_exp_f32 v45, v45\n\tv_exp_f32 v46, v46\n\tv_exp_f32 v47, v47\n\tv_exp_f32 v48, v48\n\tv_exp_f32 v49, v49\n\tv_smfmac_f32_32x32x32_bf16 v[64:79], v[180:183], v[40:47], v192\n\tv_exp_f32 v50, v50\n\tv_exp_f32 v51, v51\n\tv_exp_f32 v52, v52\n\tv_exp_f32 v53, v53\n\tv_mfma_f32_32x32x16_bf16 v[32:47], v[96:99], v[184:187], 0\n\tds_read_b128 v[176:179], v195 offset:29120\n\tv_exp_f32 v54, v54\n\tv_exp_f32 v55, v55\n\tv_exp_f32 v56, v56\n\tv_exp_f32 v57, v57\n\tv_smfmac_f32_32x32x32_bf16 v[80:95], v[172:175], v[48:55], v192\n\tv_exp_f32 v58, v58\n\tv_exp_f32 v59, v59\n\tv_exp_f32 v60, v60\n\tv_exp_f32 v61, v61\n\tv_exp_f32 v62, v62\n\tv_exp_f32 v63, v63\n\tv_exp_f32 v32, v32\n\tv_exp_f32 v33, v33\n\tv_smfmac_f32_32x32x32_bf16 v[80:95], v[180:183], v[56:63], v192\n\tv_exp_f32 v34, v34\n\tv_exp_f32 v35, v35\n\tv_exp_f32 v36, v36\n\tv_exp_f32 v37, v37\n\tv_mfma_f32_32x32x16_bf16 v[48:63], v[96:99], v[188:191], 0\n\tds_read_b128 v[96:99], v193 offset:33280\n\tv_exp_f32 v38, v38\n\tv_exp_f32 v39, v39\n\tv_exp_f32 v40, v40\n\tv_exp_f32 v41, v41\n\ts_waitcnt lgkmcnt(1)\n\tv_smfmac_f32_32x32x32_bf16 v[64:79], v[168:171], v[32:39], v192\n\tds_read_b128 v[172:175], v194 offset:31200\n\tv_exp_f32 v42, v42\n\tv_exp_f32 v43, v43\n\tv_exp_f32 v44, v44\n\tv_exp_f32 v45, v45\n\tv_exp_f32 v46, v46\n\tv_exp_f32 v47, v47\n\tv_exp_f32 v48, v48\n\tv_exp_f32 v49, v49\n\tv_smfmac_f32_32x32x32_bf16 v[64:79], v[176:179], v[40:47], v192\n\tv_exp_f32 v50, v50\n\tv_exp_f32 v51, v51\n\tv_exp_f32 v52, v52\n\tv_exp_f32 v53, v53\n\tv_mfma_f32_32x32x16_bf16 v[32:47], v[100:103], v[184:187], 0\n\tds_read_b128 v[180:183], v195 offset:31200\n\tv_exp_f32 v54, v54\n\tv_exp_f32 v55, v55\n\tv_exp_f32 v56, v56\n\tv_exp_f32 v57, v57\n\tv_smfmac_f32_32x32x32_bf16 v[80:95], v[168:171], v[48:55], v192\n\tv_exp_f32 v58, v58\n\tv_exp_f32 v59, v59\n\tv_exp_f32 v60, v60\n\tv_exp_f32 v61, v61\n\tv_exp_f32 v62, v62\n\tv_exp_f32 v63, v63\n\tv_exp_f32 v32, v32\n\tv_exp_f32 v33, v33\n\tv_smfmac_f32_32x32x32_bf16 v[80:95], v[176:179], v[56:63], v192\n\tv_exp_f32 v34, v34\n\tv_exp_f32 v35, v35\n\tv_exp_f32 v36, v36\n\tv_exp_f32 v37, v37\n\tv_mfma_f32_32x32x16_bf16 v[48:63], v[100:103], v[188:191], 0\n\tds_read_b128 v[100:103], v193 offset:35360\n\tv_exp_f32 v38, v38\n\tv_exp_f32 v39, v39\n\tv_exp_f32 v40, v40\n\tv_exp_f32 v41, v41\n\ts_waitcnt lgkmcnt(1)\n\tv_smfmac_f32_32x32x32_bf16 v[64:79], v[172:175], v[32:39], v192\n\tds_read_b128 v[168:171], v194 offset:33280\n\tds_read_b32 v197, v198 offset:24\n\tv_exp_f32 v42, v42\n\tv_exp_f32 v43, v43\n\tv_exp_f32 v44, v44\n\tv_exp_f32 v45, v45\n\tv_exp_f32 v46, v46\n\tv_exp_f32 v47, v47\n\tv_exp_f32 v48, v48\n\tv_exp_f32 v49, v49\n\tv_smfmac_f32_32x32x32_bf16 v[64:79], v[180:183], v[40:47], v192\n\tv_exp_f32 v50, v50\n\tv_exp_f32 v51, v51\n\tv_exp_f32 v52, v52\n\tv_exp_f32 v53, v53\n\tv_mfma_f32_32x32x16_bf16 v[32:47], v[96:99], v[184:187], 0\n\tds_read_b128 v[176:179], v195 offset:33280\n\tv_exp_f32 v54, v54\n\tv_exp_f32 v55, v55\n\tv_exp_f32 v56, v56\n\tv_exp_f32 v57, v57\n\tv_smfmac_f32_32x32x32_bf16 v[80:95], v[172:175], v[48:55], v192\n\tv_exp_f32 v58, v58\n\tv_exp_f32 v59, v59\n\tv_exp_f32 v60, v60\n\tv_exp_f32 v61, v61\n\tv_exp_f32 v62, v62\n\tv_exp_f32 v63, v63\n\tv_exp_f32 v32, v32\n\tv_exp_f32 v33, v33\n\tv_smfmac_f32_32x32x32_bf16 v[80:95], v[180:183], v[56:63], v192\n\ts_waitcnt lgkmcnt(1)\n\tv_readfirstlane_b32 s78, v197\n\tv_exp_f32 v34, v34\n\tv_exp_f32 v35, v35\n\tv_exp_f32 v36, v36\n\ts_cmp_ge_u32 s78, 4\n\ts_cbranch_scc0 MQA_SP2\n\tMQA_BK2:\n\tv_exp_f32 v37, v37\n\tv_mfma_f32_32x32x16_bf16 v[48:63], v[96:99], v[188:191], 0\n\tds_read_b128 v[96:99], v193 offset:37440\n\tv_exp_f32 v38, v38\n\tv_exp_f32 v39, v39\n\tv_exp_f32 v40, v40\n\tv_exp_f32 v41, v41\n\ts_waitcnt lgkmcnt(1)\n\tv_smfmac_f32_32x32x32_bf16 v[64:79], v[168:171], v[32:39], v192\n\tds_read_b128 v[172:175], v194 offset:35360\n\tv_exp_f32 v42, v42\n\tv_exp_f32 v43, v43\n\tv_exp_f32 v44, v44\n\tv_exp_f32 v45, v45\n\tv_exp_f32 v46, v46\n\tv_exp_f32 v47, v47\n\tv_exp_f32 v48, v48\n\tv_exp_f32 v49, v49\n\tv_smfmac_f32_32x32x32_bf16 v[64:79], v[176:179], v[40:47], v192\n\tv_exp_f32 v50, v50\n\tv_exp_f32 v51, v51\n\tv_exp_f32 v52, v52\n\tv_exp_f32 v53, v53\n\tv_mfma_f32_32x32x16_bf16 v[32:47], v[100:103], v[184:187], 0\n\tds_read_b128 v[180:183], v195 offset:35360\n\tv_exp_f32 v54, v54\n\tv_exp_f32 v55, v55\n\tv_exp_f32 v56, v56\n\tv_exp_f32 v57, v57\n\tv_smfmac_f32_32x32x32_bf16 v[80:95], v[168:171], v[48:55], v192\n\tv_exp_f32 v58, v58\n\tv_exp_f32 v59, v59\n\tv_exp_f32 v60, v60\n\tv_exp_f32 v61, v61\n\tv_exp_f32 v62, v62\n\tv_exp_f32 v63, v63\n\tv_exp_f32 v32, v32\n\tv_exp_f32 v33, v33\n\tv_smfmac_f32_32x32x32_bf16 v[80:95], v[176:179], v[56:63], v192\n\tv_exp_f32 v34, v34\n\tv_exp_f32 v35, v35\n\tv_exp_f32 v36, v36\n\tv_exp_f32 v37, v37\n\tv_mfma_f32_32x32x16_bf16 v[48:63], v[100:103], v[188:191], 0\n\tds_read_b128 v[100:103], v193 offset:39520\n\tv_exp_f32 v38, v38\n\tv_exp_f32 v39, v39\n\tv_exp_f32 v40, v40\n\tv_exp_f32 v41, v41\n\ts_waitcnt lgkmcnt(1)\n\tv_smfmac_f32_32x32x32_bf16 v[64:79], v[172:175], v[32:39], v192\n\tds_read_b128 v[168:171], v194 offset:37440\n\tv_exp_f32 v42, v42\n\tv_exp_f32 v43, v43\n\tv_exp_f32 v44, v44\n\tv_exp_f32 v45, v45\n\tv_exp_f32 v46, v46\n\tv_exp_f32 v47, v47\n\tv_exp_f32 v48, v48\n\tv_exp_f32 v49, v49\n\tv_smfmac_f32_32x32x32_bf16 v[64:79], v[180:183], v[40:47], v192\n\tv_exp_f32 v50, v50\n\tv_exp_f32 v51, v51\n\tv_exp_f32 v52, v52\n\tv_exp_f32 v53, v53\n\tv_mfma_f32_32x32x16_bf16 v[32:47], v[96:99], v[184:187], 0\n\tds_read_b128 v[176:179], v195 offset:37440\n\tv_exp_f32 v54, v54\n\tv_exp_f32 v55, v55\n\tv_exp_f32 v56, v56\n\tv_exp_f32 v57, v57\n\tv_smfmac_f32_32x32x32_bf16 v[80:95], v[172:175], v[48:55], v192\n\tv_exp_f32 v58, v58\n\tv_exp_f32 v59, v59\n\tv_exp_f32 v60, v60\n\tv_exp_f32 v61, v61\n\tv_exp_f32 v62, v62\n\tv_exp_f32 v63, v63\n\tv_exp_f32 v32, v32\n\tv_exp_f32 v33, v33\n\tv_smfmac_f32_32x32x32_bf16 v[80:95], v[180:183], v[56:63], v192\n\tv_exp_f32 v34, v34\n\tv_exp_f32 v35, v35\n\tv_exp_f32 v36, v36\n\tv_exp_f32 v37, v37\n\tv_mfma_f32_32x32x16_bf16 v[48:63], v[96:99], v[188:191], 0\n\tds_read_b128 v[96:99], v193 offset:41600\n\tv_exp_f32 v38, v38\n\tv_exp_f32 v39, v39\n\tv_exp_f32 v40, v40\n\tv_exp_f32 v41, v41\n\ts_waitcnt lgkmcnt(1)\n\tv_smfmac_f32_32x32x32_bf16 v[64:79], v[168:171], v[32:39], v192\n\tds_read_b128 v[172:175], v194 offset:39520\n\tv_exp_f32 v42, v42\n\tv_exp_f32 v43, v43\n\tv_exp_f32 v44, v44\n\tv_exp_f32 v45, v45\n\tv_exp_f32 v46, v46\n\tv_exp_f32 v47, v47\n\tv_exp_f32 v48, v48\n\tv_exp_f32 v49, v49\n\tv_smfmac_f32_32x32x32_bf16 v[64:79], v[176:179], v[40:47], v192\n\tv_exp_f32 v50, v50\n\tv_exp_f32 v51, v51\n\tv_exp_f32 v52, v52\n\tv_exp_f32 v53, v53\n\tv_mfma_f32_32x32x16_bf16 v[32:47], v[100:103], v[184:187], 0\n\tds_read_b128 v[180:183], v195 offset:39520\n\tv_exp_f32 v54, v54\n\tv_exp_f32 v55, v55\n\tv_exp_f32 v56, v56\n\tv_exp_f32 v57, v57\n\tv_smfmac_f32_32x32x32_bf16 v[80:95], v[168:171], v[48:55], v192\n\tv_exp_f32 v58, v58\n\tv_exp_f32 v59, v59\n\tv_exp_f32 v60, v60\n\tv_exp_f32 v61, v61\n\tv_exp_f32 v62, v62\n\tv_exp_f32 v63, v63\n\tv_exp_f32 v32, v32\n\tv_exp_f32 v33, v33\n\tv_smfmac_f32_32x32x32_bf16 v[80:95], v[176:179], v[56:63], v192\n\tv_exp_f32 v34, v34\n\tv_exp_f32 v35, v35\n\tv_exp_f32 v36, v36\n\tv_exp_f32 v37, v37\n\tv_mfma_f32_32x32x16_bf16 v[48:63], v[100:103], v[188:191], 0\n\tds_read_b128 v[100:103], v193 offset:43680\n\tv_exp_f32 v38, v38\n\tv_exp_f32 v39, v39\n\tv_exp_f32 v40, v40\n\tv_exp_f32 v41, v41\n\ts_waitcnt lgkmcnt(1)\n\tv_smfmac_f32_32x32x32_bf16 v[64:79], v[172:175], v[32:39], v192\n\tds_read_b128 v[168:171], v194 offset:41600\n\tv_exp_f32 v42, v42\n\tv_exp_f32 v43, v43\n\tv_exp_f32 v44, v44\n\tv_exp_f32 v45, v45\n\tv_exp_f32 v46, v46\n\tv_exp_f32 v47, v47\n\tv_exp_f32 v48, v48\n\tv_exp_f32 v49, v49\n\tv_smfmac_f32_32x32x32_bf16 v[64:79], v[180:183], v[40:47], v192\n\tv_exp_f32 v50, v50\n\tv_exp_f32 v51, v51\n\tv_exp_f32 v52, v52\n\tv_exp_f32 v53, v53\n\tv_mfma_f32_32x32x16_bf16 v[32:47], v[96:99], v[184:187], 0\n\tds_read_b128 v[176:179], v195 offset:41600\n\tv_exp_f32 v54, v54\n\tv_exp_f32 v55, v55\n\tv_exp_f32 v56, v56\n\tv_exp_f32 v57, v57\n\tv_smfmac_f32_32x32x32_bf16 v[80:95], v[172:175], v[48:55], v192\n\tv_exp_f32 v58, v58\n\tv_exp_f32 v59, v59\n\tv_exp_f32 v60, v60\n\tv_exp_f32 v61, v61\n\tv_exp_f32 v62, v62\n\tv_exp_f32 v63, v63\n\tv_exp_f32 v32, v32\n\tv_exp_f32 v33, v33\n\tv_smfmac_f32_32x32x32_bf16 v[80:95], v[180:183], v[56:63], v192\n\tv_exp_f32 v34, v34\n\tv_exp_f32 v35, v35\n\tv_exp_f32 v36, v36\n\tv_exp_f32 v37, v37\n\tv_mfma_f32_32x32x16_bf16 v[48:63], v[96:99], v[188:191], 0\n\tds_read_b128 v[96:99], v193 offset:45760\n\tv_exp_f32 v38, v38\n\tv_exp_f32 v39, v39\n\tv_exp_f32 v40, v40\n\tv_exp_f32 v41, v41\n\ts_waitcnt lgkmcnt(1)\n\tv_smfmac_f32_32x32x32_bf16 v[64:79], v[168:171], v[32:39], v192\n\tds_read_b128 v[172:175], v194 offset:43680\n\tv_exp_f32 v42, v42\n\tv_exp_f32 v43, v43\n\tv_exp_f32 v44, v44\n\tv_exp_f32 v45, v45\n\tv_exp_f32 v46, v46\n\tv_exp_f32 v47, v47\n\tv_exp_f32 v48, v48\n\tv_exp_f32 v49, v49\n\tv_smfmac_f32_32x32x32_bf16 v[64:79], v[176:179], v[40:47], v192\n\tv_exp_f32 v50, v50\n\tv_exp_f32 v51, v51\n\tv_exp_f32 v52, v52\n\tv_exp_f32 v53, v53\n\tv_mfma_f32_32x32x16_bf16 v[32:47], v[100:103], v[184:187], 0\n\tds_read_b128 v[180:183], v195 offset:43680\n\tv_exp_f32 v54, v54\n\tv_exp_f32 v55, v55\n\tv_exp_f32 v56, v56\n\tv_exp_f32 v57, v57\n\tv_smfmac_f32_32x32x32_bf16 v[80:95], v[168:171], v[48:55], v192\n\tv_exp_f32 v58, v58\n\tv_exp_f32 v59, v59\n\tv_exp_f32 v60, v60\n\tv_exp_f32 v61, v61\n\tv_exp_f32 v62, v62\n\tv_exp_f32 v63, v63\n\tv_exp_f32 v32, v32\n\tv_exp_f32 v33, v33\n\tv_smfmac_f32_32x32x32_bf16 v[80:95], v[176:179], v[56:63], v192\n\tv_exp_f32 v34, v34\n\tv_exp_f32 v35, v35\n\tv_exp_f32 v36, v36\n\tv_exp_f32 v37, v37\n\tv_mfma_f32_32x32x16_bf16 v[48:63], v[100:103], v[188:191], 0\n\tds_read_b128 v[100:103], v193 offset:47840\n\tv_exp_f32 v38, v38\n\tv_exp_f32 v39, v39\n\tv_exp_f32 v40, v40\n\tv_exp_f32 v41, v41\n\ts_waitcnt lgkmcnt(1)\n\tv_smfmac_f32_32x32x32_bf16 v[64:79], v[172:175], v[32:39], v192\n\tds_read_b128 v[168:171], v194 offset:45760\n\tv_exp_f32 v42, v42\n\tv_exp_f32 v43, v43\n\tv_exp_f32 v44, v44\n\tv_exp_f32 v45, v45\n\tv_exp_f32 v46, v46\n\tv_exp_f32 v47, v47\n\tv_exp_f32 v48, v48\n\tv_exp_f32 v49, v49\n\tv_smfmac_f32_32x32x32_bf16 v[64:79], v[180:183], v[40:47], v192\n\tv_exp_f32 v50, v50\n\tv_exp_f32 v51, v51\n\tv_exp_f32 v52, v52\n\tv_exp_f32 v53, v53\n\tv_mfma_f32_32x32x16_bf16 v[32:47], v[96:99], v[184:187], 0\n\tds_read_b128 v[176:179], v195 offset:45760\n\tv_exp_f32 v54, v54\n\tv_exp_f32 v55, v55\n\tv_exp_f32 v56, v56\n\tv_exp_f32 v57, v57\n\tv_smfmac_f32_32x32x32_bf16 v[80:95], v[172:175], v[48:55], v192\n\tv_exp_f32 v58, v58\n\tv_exp_f32 v59, v59\n\tv_exp_f32 v60, v60\n\tv_exp_f32 v61, v61\n\tv_exp_f32 v62, v62\n\tv_exp_f32 v63, v63\n\tv_exp_f32 v32, v32\n\tv_exp_f32 v33, v33\n\tv_smfmac_f32_32x32x32_bf16 v[80:95], v[180:183], v[56:63], v192\n\tv_exp_f32 v34, v34\n\tv_exp_f32 v35, v35\n\tv_exp_f32 v36, v36\n\tv_exp_f32 v37, v37\n\tv_mfma_f32_32x32x16_bf16 v[48:63], v[96:99], v[188:191], 0\n\tds_read_b128 v[96:99], v193 offset:49920\n\tv_exp_f32 v38, v38\n\tv_exp_f32 v39, v39\n\tv_exp_f32 v40, v40\n\tv_exp_f32 v41, v41\n\ts_waitcnt lgkmcnt(1)\n\tv_smfmac_f32_32x32x32_bf16 v[64:79], v[168:171], v[32:39], v192\n\tds_read_b128 v[172:175], v194 offset:47840\n\tv_exp_f32 v42, v42\n\tv_exp_f32 v43, v43\n\tv_exp_f32 v44, v44\n\tv_exp_f32 v45, v45\n\tv_exp_f32 v46, v46\n\tv_exp_f32 v47, v47\n\tv_exp_f32 v48, v48\n\tv_exp_f32 v49, v49\n\tv_smfmac_f32_32x32x32_bf16 v[64:79], v[176:179], v[40:47], v192\n\tv_exp_f32 v50, v50\n\tv_exp_f32 v51, v51\n\tv_exp_f32 v52, v52\n\tv_exp_f32 v53, v53\n\tv_mfma_f32_32x32x16_bf16 v[32:47], v[100:103], v[184:187], 0\n\tds_read_b128 v[180:183], v195 offset:47840\n\tv_exp_f32 v54, v54\n\tv_exp_f32 v55, v55\n\tv_exp_f32 v56, v56\n\tv_exp_f32 v57, v57\n\tv_smfmac_f32_32x32x32_bf16 v[80:95], v[168:171], v[48:55], v192\n\tv_exp_f32 v58, v58\n\tv_exp_f32 v59, v59\n\tv_exp_f32 v60, v60\n\tv_exp_f32 v61, v61\n\tv_exp_f32 v62, v62\n\tv_exp_f32 v63, v63\n\tv_exp_f32 v32, v32\n\tv_exp_f32 v33, v33\n\tv_smfmac_f32_32x32x32_bf16 v[80:95], v[176:179], v[56:63], v192\n\tv_exp_f32 v34, v34\n\tv_exp_f32 v35, v35\n\tv_exp_f32 v36, v36\n\tv_exp_f32 v37, v37\n\tv_mfma_f32_32x32x16_bf16 v[48:63], v[100:103], v[188:191], 0\n\tds_read_b128 v[100:103], v193 offset:52000\n\tv_exp_f32 v38, v38\n\tv_exp_f32 v39, v39\n\tv_exp_f32 v40, v40\n\tv_exp_f32 v41, v41\n\ts_waitcnt lgkmcnt(1)\n\tv_smfmac_f32_32x32x32_bf16 v[64:79], v[172:175], v[32:39], v192\n\tds_read_b128 v[168:171], v194 offset:49920\n\tv_exp_f32 v42, v42\n\tv_exp_f32 v43, v43\n\tv_exp_f32 v44, v44\n\tv_exp_f32 v45, v45\n\tv_exp_f32 v46, v46\n\tv_exp_f32 v47, v47\n\tv_exp_f32 v48, v48\n\tv_exp_f32 v49, v49\n\tv_smfmac_f32_32x32x32_bf16 v[64:79], v[180:183], v[40:47], v192\n\tv_exp_f32 v50, v50\n\tv_exp_f32 v51, v51\n\tv_exp_f32 v52, v52\n\tv_exp_f32 v53, v53\n\tv_mfma_f32_32x32x16_bf16 v[32:47], v[96:99], v[184:187], 0\n\tds_read_b128 v[176:179], v195 offset:49920\n\tv_exp_f32 v54, v54\n\tv_exp_f32 v55, v55\n\tv_exp_f32 v56, v56\n\tv_exp_f32 v57, v57\n\tv_smfmac_f32_32x32x32_bf16 v[80:95], v[172:175], v[48:55], v192\n\tv_exp_f32 v58, v58\n\tv_exp_f32 v59, v59\n\tv_exp_f32 v60, v60\n\tv_exp_f32 v61, v61\n\tv_exp_f32 v62, v62\n\tv_exp_f32 v63, v63\n\tv_exp_f32 v32, v32\n\tv_exp_f32 v33, v33\n\tv_smfmac_f32_32x32x32_bf16 v[80:95], v[180:183], v[56:63], v192\n\tv_exp_f32 v34, v34\n\tv_exp_f32 v35, v35\n\tv_exp_f32 v36, v36\n\tv_exp_f32 v37, v37\n\tv_mfma_f32_32x32x16_bf16 v[48:63], v[96:99], v[188:191], 0\n\tds_read_b128 v[96:99], v193 offset:54080\n\tv_exp_f32 v38, v38\n\tv_exp_f32 v39, v39\n\tv_exp_f32 v40, v40\n\tv_exp_f32 v41, v41\n\ts_waitcnt lgkmcnt(1)\n\tv_smfmac_f32_32x32x32_bf16 v[64:79], v[168:171], v[32:39], v192\n\tds_read_b128 v[172:175], v194 offset:52000\n\tv_exp_f32 v42, v42\n\tv_exp_f32 v43, v43\n\tv_exp_f32 v44, v44\n\tv_exp_f32 v45, v45\n\tv_exp_f32 v46, v46\n\tv_exp_f32 v47, v47\n\tv_exp_f32 v48, v48\n\tv_exp_f32 v49, v49\n\tv_smfmac_f32_32x32x32_bf16 v[64:79], v[176:179], v[40:47], v192\n\tv_exp_f32 v50, v50\n\tv_exp_f32 v51, v51\n\tv_exp_f32 v52, v52\n\tv_exp_f32 v53, v53\n\tv_mfma_f32_32x32x16_bf16 v[32:47], v[100:103], v[184:187], 0\n\tds_read_b128 v[180:183], v195 offset:52000\n\tv_exp_f32 v54, v54\n\tv_exp_f32 v55, v55\n\tv_exp_f32 v56, v56\n\tv_exp_f32 v57, v57\n\tv_smfmac_f32_32x32x32_bf16 v[80:95], v[168:171], v[48:55], v192\n\tv_exp_f32 v58, v58\n\tv_exp_f32 v59, v59\n\tv_exp_f32 v60, v60\n\tv_exp_f32 v61, v61\n\tv_exp_f32 v62, v62\n\tv_exp_f32 v63, v63\n\tv_exp_f32 v32, v32\n\tv_exp_f32 v33, v33\n\tv_smfmac_f32_32x32x32_bf16 v[80:95], v[176:179], v[56:63], v192\n\tv_exp_f32 v34, v34\n\tv_exp_f32 v35, v35\n\tv_exp_f32 v36, v36\n\tv_exp_f32 v37, v37\n\tv_mfma_f32_32x32x16_bf16 v[48:63], v[100:103], v[188:191], 0\n\tds_read_b128 v[100:103], v193 offset:56160\n\tv_exp_f32 v38, v38\n\tv_exp_f32 v39, v39\n\tv_exp_f32 v40, v40\n\tv_exp_f32 v41, v41\n\ts_waitcnt lgkmcnt(1)\n\tv_smfmac_f32_32x32x32_bf16 v[64:79], v[172:175], v[32:39], v192\n\tds_read_b128 v[168:171], v194 offset:54080\n\tv_exp_f32 v42, v42\n\tv_exp_f32 v43, v43\n\tv_exp_f32 v44, v44\n\tv_exp_f32 v45, v45\n\tv_exp_f32 v46, v46\n\tv_exp_f32 v47, v47\n\tv_exp_f32 v48, v48\n\tv_exp_f32 v49, v49\n\tv_smfmac_f32_32x32x32_bf16 v[64:79], v[180:183], v[40:47], v192\n\tv_exp_f32 v50, v50\n\tv_exp_f32 v51, v51\n\tv_exp_f32 v52, v52\n\tv_exp_f32 v53, v53\n\tv_mfma_f32_32x32x16_bf16 v[32:47], v[96:99], v[184:187], 0\n\tds_read_b128 v[176:179], v195 offset:54080\n\tv_exp_f32 v54, v54\n\tv_exp_f32 v55, v55\n\tv_exp_f32 v56, v56\n\tv_exp_f32 v57, v57\n\tv_smfmac_f32_32x32x32_bf16 v[80:95], v[172:175], v[48:55], v192\n\tv_exp_f32 v58, v58\n\tv_exp_f32 v59, v59\n\tv_exp_f32 v60, v60\n\tv_exp_f32 v61, v61\n\tv_exp_f32 v62, v62\n\tv_exp_f32 v63, v63\n\tv_exp_f32 v32, v32\n\tv_exp_f32 v33, v33\n\tv_smfmac_f32_32x32x32_bf16 v[80:95], v[180:183], v[56:63], v192\n\tv_exp_f32 v34, v34\n\tv_exp_f32 v35, v35\n\tv_exp_f32 v36, v36\n\tv_exp_f32 v37, v37\n\tv_mfma_f32_32x32x16_bf16 v[48:63], v[96:99], v[188:191], 0\n\tds_read_b128 v[96:99], v193 offset:58240\n\tv_exp_f32 v38, v38\n\tv_exp_f32 v39, v39\n\tv_exp_f32 v40, v40\n\tv_exp_f32 v41, v41\n\ts_waitcnt lgkmcnt(1)\n\tv_smfmac_f32_32x32x32_bf16 v[64:79], v[168:171], v[32:39], v192\n\tds_read_b128 v[172:175], v194 offset:56160\n\tv_exp_f32 v42, v42\n\tv_exp_f32 v43, v43\n\tv_exp_f32 v44, v44\n\tv_exp_f32 v45, v45\n\tv_exp_f32 v46, v46\n\tv_exp_f32 v47, v47\n\tv_exp_f32 v48, v48\n\tv_exp_f32 v49, v49\n\tv_smfmac_f32_32x32x32_bf16 v[64:79], v[176:179], v[40:47], v192\n\tv_exp_f32 v50, v50\n\tv_exp_f32 v51, v51\n\tv_exp_f32 v52, v52\n\tv_exp_f32 v53, v53\n\tv_mfma_f32_32x32x16_bf16 v[32:47], v[100:103], v[184:187], 0\n\tds_read_b128 v[180:183], v195 offset:56160\n\tv_exp_f32 v54, v54\n\tv_exp_f32 v55, v55\n\tv_exp_f32 v56, v56\n\tv_exp_f32 v57, v57\n\tv_smfmac_f32_32x32x32_bf16 v[80:95], v[168:171], v[48:55], v192\n\tv_exp_f32 v58, v58\n\tv_exp_f32 v59, v59\n\tv_exp_f32 v60, v60\n\tv_exp_f32 v61, v61\n\tv_exp_f32 v62, v62\n\tv_exp_f32 v63, v63\n\tv_exp_f32 v32, v32\n\tv_exp_f32 v33, v33\n\tv_smfmac_f32_32x32x32_bf16 v[80:95], v[176:179], v[56:63], v192\n\tv_exp_f32 v34, v34\n\tv_exp_f32 v35, v35\n\tv_exp_f32 v36, v36\n\tv_exp_f32 v37, v37\n\tv_mfma_f32_32x32x16_bf16 v[48:63], v[100:103], v[188:191], 0\n\tds_read_b128 v[100:103], v193 offset:60320\n\tv_exp_f32 v38, v38\n\tv_exp_f32 v39, v39\n\tv_exp_f32 v40, v40\n\tv_exp_f32 v41, v41\n\ts_waitcnt lgkmcnt(1)\n\tv_smfmac_f32_32x32x32_bf16 v[64:79], v[172:175], v[32:39], v192\n\tds_read_b128 v[168:171], v194 offset:58240\n\tv_exp_f32 v42, v42\n\tv_exp_f32 v43, v43\n\tv_exp_f32 v44, v44\n\tv_exp_f32 v45, v45\n\tv_exp_f32 v46, v46\n\tv_exp_f32 v47, v47\n\tv_exp_f32 v48, v48\n\tv_exp_f32 v49, v49\n\tv_smfmac_f32_32x32x32_bf16 v[64:79], v[180:183], v[40:47], v192\n\tv_exp_f32 v50, v50\n\tv_exp_f32 v51, v51\n\tv_exp_f32 v52, v52\n\tv_exp_f32 v53, v53\n\tv_mfma_f32_32x32x16_bf16 v[32:47], v[96:99], v[184:187], 0\n\tds_read_b128 v[176:179], v195 offset:58240\n\tv_exp_f32 v54, v54\n\tv_exp_f32 v55, v55\n\tv_exp_f32 v56, v56\n\tv_exp_f32 v57, v57\n\tv_smfmac_f32_32x32x32_bf16 v[80:95], v[172:175], v[48:55], v192\n\tv_exp_f32 v58, v58\n\tv_exp_f32 v59, v59\n\tv_exp_f32 v60, v60\n\tv_exp_f32 v61, v61\n\tv_exp_f32 v62, v62\n\tv_exp_f32 v63, v63\n\tv_exp_f32 v32, v32\n\tv_exp_f32 v33, v33\n\tv_smfmac_f32_32x32x32_bf16 v[80:95], v[180:183], v[56:63], v192\n\tv_exp_f32 v34, v34\n\tv_exp_f32 v35, v35\n\tv_exp_f32 v36, v36\n\tv_exp_f32 v37, v37\n\tv_mfma_f32_32x32x16_bf16 v[48:63], v[96:99], v[188:191], 0\n\tds_read_b128 v[96:99], v193 offset:62400\n\tv_exp_f32 v38, v38\n\tv_exp_f32 v39, v39\n\tv_exp_f32 v40, v40\n\tv_exp_f32 v41, v41\n\ts_waitcnt lgkmcnt(1)\n\tv_smfmac_f32_32x32x32_bf16 v[64:79], v[168:171], v[32:39], v192\n\tds_read_b128 v[172:175], v194 offset:60320\n\tv_exp_f32 v42, v42\n\tv_exp_f32 v43, v43\n\tv_exp_f32 v44, v44\n\tv_exp_f32 v45, v45\n\tv_exp_f32 v46, v46\n\tv_exp_f32 v47, v47\n\tv_exp_f32 v48, v48\n\tv_exp_f32 v49, v49\n\tv_smfmac_f32_32x32x32_bf16 v[64:79], v[176:179], v[40:47], v192\n\tv_exp_f32 v50, v50\n\tv_exp_f32 v51, v51\n\tv_exp_f32 v52, v52\n\tv_exp_f32 v53, v53\n\tv_mfma_f32_32x32x16_bf16 v[32:47], v[100:103], v[184:187], 0\n\tds_read_b128 v[180:183], v195 offset:60320\n\tv_exp_f32 v54, v54\n\tv_exp_f32 v55, v55\n\tv_exp_f32 v56, v56\n\tv_exp_f32 v57, v57\n\tv_smfmac_f32_32x32x32_bf16 v[80:95], v[168:171], v[48:55], v192\n\tv_exp_f32 v58, v58\n\tv_exp_f32 v59, v59\n\tv_exp_f32 v60, v60\n\tv_exp_f32 v61, v61\n\tv_exp_f32 v62, v62\n\tv_exp_f32 v63, v63\n\tv_exp_f32 v32, v32\n\tv_exp_f32 v33, v33\n\tv_smfmac_f32_32x32x32_bf16 v[80:95], v[176:179], v[56:63], v192\n\tv_exp_f32 v34, v34\n\tv_exp_f32 v35, v35\n\tv_exp_f32 v36, v36\n\tv_exp_f32 v37, v37\n\tv_mfma_f32_32x32x16_bf16 v[48:63], v[100:103], v[188:191], 0\n\tds_read_b128 v[100:103], v193 offset:64480\n\tv_exp_f32 v38, v38\n\tv_exp_f32 v39, v39\n\tv_exp_f32 v40, v40\n\tv_exp_f32 v41, v41\n\ts_waitcnt lgkmcnt(1)\n\tv_smfmac_f32_32x32x32_bf16 v[64:79], v[172:175], v[32:39], v192\n\tds_read_b128 v[168:171], v194 offset:62400\n\tv_exp_f32 v42, v42\n\tv_exp_f32 v43, v43\n\tv_exp_f32 v44, v44\n\tv_exp_f32 v45, v45\n\tv_exp_f32 v46, v46\n\tv_exp_f32 v47, v47\n\tv_exp_f32 v48, v48\n\tv_exp_f32 v49, v49\n\tv_smfmac_f32_32x32x32_bf16 v[64:79], v[180:183], v[40:47], v192\n\tv_exp_f32 v50, v50\n\tv_exp_f32 v51, v51\n\tv_exp_f32 v52, v52\n\tv_exp_f32 v53, v53\n\tv_mfma_f32_32x32x16_bf16 v[32:47], v[96:99], v[184:187], 0\n\tds_read_b128 v[176:179], v195 offset:62400\n\tv_exp_f32 v54, v54\n\tv_exp_f32 v55, v55\n\tv_exp_f32 v56, v56\n\tv_exp_f32 v57, v57\n\tv_smfmac_f32_32x32x32_bf16 v[80:95], v[172:175], v[48:55], v192\n\tv_exp_f32 v58, v58\n\tv_exp_f32 v59, v59\n\tv_exp_f32 v60, v60\n\tv_exp_f32 v61, v61\n\tv_exp_f32 v62, v62\n\tv_exp_f32 v63, v63\n\tv_exp_f32 v32, v32\n\tv_exp_f32 v33, v33\n\tv_smfmac_f32_32x32x32_bf16 v[80:95], v[180:183], v[56:63], v192\n\tv_exp_f32 v34, v34\n\tv_exp_f32 v35, v35\n\tv_exp_f32 v36, v36\n\tv_exp_f32 v37, v37\n\tv_mfma_f32_32x32x16_bf16 v[48:63], v[96:99], v[188:191], 0\n\tv_add_u32 v193, 66560, v193\n\tds_read_b128 v[96:99], v193 offset:0\n\tv_exp_f32 v38, v38\n\tv_exp_f32 v39, v39\n\tv_exp_f32 v40, v40\n\tv_exp_f32 v41, v41\n\ts_waitcnt lgkmcnt(1)\n\tv_smfmac_f32_32x32x32_bf16 v[64:79], v[168:171], v[32:39], v192\n\tds_read_b128 v[172:175], v194 offset:64480\n\tv_exp_f32 v42, v42\n\tv_exp_f32 v43, v43\n\tv_exp_f32 v44, v44\n\tv_exp_f32 v45, v45\n\tv_exp_f32 v46, v46\n\tv_exp_f32 v47, v47\n\tv_exp_f32 v48, v48\n\tv_exp_f32 v49, v49\n\tv_smfmac_f32_32x32x32_bf16 v[64:79], v[176:179], v[40:47], v192\n\tv_exp_f32 v50, v50\n\tv_exp_f32 v51, v51\n\tv_exp_f32 v52, v52\n\tv_exp_f32 v53, v53\n\tv_mfma_f32_32x32x16_bf16 v[32:47], v[100:103], v[184:187], 0\n\tds_read_b128 v[180:183], v195 offset:64480\n\tv_exp_f32 v54, v54\n\tv_exp_f32 v55, v55\n\tv_exp_f32 v56, v56\n\tv_exp_f32 v57, v57\n\tv_smfmac_f32_32x32x32_bf16 v[80:95], v[168:171], v[48:55], v192\n\tv_exp_f32 v58, v58\n\tv_exp_f32 v59, v59\n\tv_exp_f32 v60, v60\n\tv_exp_f32 v61, v61\n\tv_exp_f32 v62, v62\n\tv_exp_f32 v63, v63\n\tv_exp_f32 v32, v32\n\tv_exp_f32 v33, v33\n\tv_smfmac_f32_32x32x32_bf16 v[80:95], v[176:179], v[56:63], v192\n\tv_exp_f32 v34, v34\n\tv_exp_f32 v35, v35\n\tv_exp_f32 v36, v36\n\tv_exp_f32 v37, v37\n\tv_mfma_f32_32x32x16_bf16 v[48:63], v[100:103], v[188:191], 0\n\tds_read_b128 v[100:103], v193 offset:2080\n\tv_exp_f32 v38, v38\n\tv_exp_f32 v39, v39\n\tv_exp_f32 v40, v40\n\tv_exp_f32 v41, v41\n\ts_waitcnt lgkmcnt(1)\n\tv_smfmac_f32_32x32x32_bf16 v[64:79], v[172:175], v[32:39], v192\n\tv_add_u32 v194, 66560, v194\n\tds_read_b128 v[168:171], v194 offset:0\n\tv_exp_f32 v42, v42\n\tv_exp_f32 v43, v43\n\tv_exp_f32 v44, v44\n\tv_exp_f32 v45, v45\n\tv_exp_f32 v46, v46\n\tv_exp_f32 v47, v47\n\tv_exp_f32 v48, v48\n\tv_exp_f32 v49, v49\n\tv_smfmac_f32_32x32x32_bf16 v[64:79], v[180:183], v[40:47], v192\n\tv_exp_f32 v50, v50\n\tv_exp_f32 v51, v51\n\tv_exp_f32 v52, v52\n\tv_exp_f32 v53, v53\n\tv_mfma_f32_32x32x16_bf16 v[32:47], v[96:99], v[184:187], 0\n\tv_add_u32 v195, 66560, v195\n\tds_read_b128 v[176:179], v195 offset:0\n\tv_exp_f32 v54, v54\n\tv_exp_f32 v55, v55\n\tv_exp_f32 v56, v56\n\tv_exp_f32 v57, v57\n\tv_smfmac_f32_32x32x32_bf16 v[80:95], v[172:175], v[48:55], v192\n\tv_exp_f32 v58, v58\n\tv_exp_f32 v59, v59\n\tv_exp_f32 v60, v60\n\tv_exp_f32 v61, v61\n\tv_exp_f32 v62, v62\n\tv_exp_f32 v63, v63\n\tv_exp_f32 v32, v32\n\tv_exp_f32 v33, v33\n\tv_smfmac_f32_32x32x32_bf16 v[80:95], v[180:183], v[56:63], v192\n\tv_exp_f32 v34, v34\n\tv_exp_f32 v35, v35\n\tv_exp_f32 v36, v36\n\tv_exp_f32 v37, v37\n\tv_mfma_f32_32x32x16_bf16 v[48:63], v[96:99], v[188:191], 0\n\tds_read_b128 v[96:99], v193 offset:4160\n\tv_exp_f32 v38, v38\n\tv_exp_f32 v39, v39\n\tv_exp_f32 v40, v40\n\tv_exp_f32 v41, v41\n\ts_waitcnt lgkmcnt(1)\n\tv_smfmac_f32_32x32x32_bf16 v[64:79], v[168:171], v[32:39], v192\n\tds_read_b128 v[172:175], v194 offset:2080\n\tv_exp_f32 v42, v42\n\tv_exp_f32 v43, v43\n\tv_exp_f32 v44, v44\n\tv_exp_f32 v45, v45\n\tv_exp_f32 v46, v46\n\tv_exp_f32 v47, v47\n\tv_exp_f32 v48, v48\n\tv_exp_f32 v49, v49\n\tv_smfmac_f32_32x32x32_bf16 v[64:79], v[176:179], v[40:47], v192\n\tv_exp_f32 v50, v50\n\tv_exp_f32 v51, v51\n\tv_exp_f32 v52, v52\n\tv_exp_f32 v53, v53\n\tv_mfma_f32_32x32x16_bf16 v[32:47], v[100:103], v[184:187], 0\n\tds_read_b128 v[180:183], v195 offset:2080\n\tv_exp_f32 v54, v54\n\tv_exp_f32 v55, v55\n\tv_exp_f32 v56, v56\n\tv_exp_f32 v57, v57\n\tv_smfmac_f32_32x32x32_bf16 v[80:95], v[168:171], v[48:55], v192\n\tv_exp_f32 v58, v58\n\tv_exp_f32 v59, v59\n\tv_exp_f32 v60, v60\n\tv_exp_f32 v61, v61\n\tv_exp_f32 v62, v62\n\tv_exp_f32 v63, v63\n\tv_exp_f32 v32, v32\n\tv_exp_f32 v33, v33\n\tv_smfmac_f32_32x32x32_bf16 v[80:95], v[176:179], v[56:63], v192\n\tv_exp_f32 v34, v34\n\tv_exp_f32 v35, v35\n\tv_exp_f32 v36, v36\n\tv_exp_f32 v37, v37\n\tv_mfma_f32_32x32x16_bf16 v[48:63], v[100:103], v[188:191], 0\n\tds_read_b128 v[100:103], v193 offset:6240\n\tv_exp_f32 v38, v38\n\tv_exp_f32 v39, v39\n\tv_exp_f32 v40, v40\n\tv_exp_f32 v41, v41\n\ts_waitcnt lgkmcnt(1)\n\tv_smfmac_f32_32x32x32_bf16 v[64:79], v[172:175], v[32:39], v192\n\tds_read_b128 v[168:171], v194 offset:4160\n\tv_exp_f32 v42, v42\n\tv_exp_f32 v43, v43\n\tv_exp_f32 v44, v44\n\tv_exp_f32 v45, v45\n\tv_exp_f32 v46, v46\n\tv_exp_f32 v47, v47\n\tv_exp_f32 v48, v48\n\tv_exp_f32 v49, v49\n\tv_smfmac_f32_32x32x32_bf16 v[64:79], v[180:183], v[40:47], v192\n\tv_exp_f32 v50, v50\n\tv_exp_f32 v51, v51\n\tv_exp_f32 v52, v52\n\tv_exp_f32 v53, v53\n\tv_mfma_f32_32x32x16_bf16 v[32:47], v[96:99], v[184:187], 0\n\tds_read_b128 v[176:179], v195 offset:4160\n\tv_exp_f32 v54, v54\n\tv_exp_f32 v55, v55\n\tv_exp_f32 v56, v56\n\tv_exp_f32 v57, v57\n\tv_smfmac_f32_32x32x32_bf16 v[80:95], v[172:175], v[48:55], v192\n\tv_exp_f32 v58, v58\n\tv_exp_f32 v59, v59\n\tv_exp_f32 v60, v60\n\tv_exp_f32 v61, v61\n\tv_exp_f32 v62, v62\n\tv_exp_f32 v63, v63\n\tv_exp_f32 v32, v32\n\tv_exp_f32 v33, v33\n\tv_smfmac_f32_32x32x32_bf16 v[80:95], v[180:183], v[56:63], v192\n\tv_exp_f32 v34, v34\n\tv_exp_f32 v35, v35\n\tv_exp_f32 v36, v36\n\tv_exp_f32 v37, v37\n\tv_mfma_f32_32x32x16_bf16 v[48:63], v[96:99], v[188:191], 0\n\tds_read_b128 v[96:99], v193 offset:8320\n\tv_exp_f32 v38, v38\n\tv_exp_f32 v39, v39\n\tv_exp_f32 v40, v40\n\tv_exp_f32 v41, v41\n\ts_waitcnt lgkmcnt(1)\n\tv_smfmac_f32_32x32x32_bf16 v[64:79], v[168:171], v[32:39], v192\n\tds_read_b128 v[172:175], v194 offset:6240\n\tv_exp_f32 v42, v42\n\tv_exp_f32 v43, v43\n\tv_exp_f32 v44, v44\n\tv_exp_f32 v45, v45\n\tv_exp_f32 v46, v46\n\tv_exp_f32 v47, v47\n\tv_exp_f32 v48, v48\n\tv_exp_f32 v49, v49\n\tv_smfmac_f32_32x32x32_bf16 v[64:79], v[176:179], v[40:47], v192\n\tv_exp_f32 v50, v50\n\tv_exp_f32 v51, v51\n\tv_exp_f32 v52, v52\n\tv_exp_f32 v53, v53\n\tv_mfma_f32_32x32x16_bf16 v[32:47], v[100:103], v[184:187], 0\n\tds_read_b128 v[180:183], v195 offset:6240\n\tv_exp_f32 v54, v54\n\tv_exp_f32 v55, v55\n\tv_exp_f32 v56, v56\n\tv_exp_f32 v57, v57\n\tv_smfmac_f32_32x32x32_bf16 v[80:95], v[168:171], v[48:55], v192\n\tv_exp_f32 v58, v58\n\tv_exp_f32 v59, v59\n\tv_exp_f32 v60, v60\n\tv_exp_f32 v61, v61\n\tv_exp_f32 v62, v62\n\tv_exp_f32 v63, v63\n\tv_exp_f32 v32, v32\n\tv_exp_f32 v33, v33\n\tv_smfmac_f32_32x32x32_bf16 v[80:95], v[176:179], v[56:63], v192\n\tv_exp_f32 v34, v34\n\tv_exp_f32 v35, v35\n\tv_exp_f32 v36, v36\n\tv_exp_f32 v37, v37\n\tv_mfma_f32_32x32x16_bf16 v[48:63], v[100:103], v[188:191], 0\n\tds_read_b128 v[100:103], v193 offset:10400\n\tv_exp_f32 v38, v38\n\tv_exp_f32 v39, v39\n\tv_exp_f32 v40, v40\n\tv_exp_f32 v41, v41\n\ts_waitcnt lgkmcnt(1)\n\tv_smfmac_f32_32x32x32_bf16 v[64:79], v[172:175], v[32:39], v192\n\tds_read_b128 v[168:171], v194 offset:8320\n\tv_exp_f32 v42, v42\n\tv_exp_f32 v43, v43\n\tv_exp_f32 v44, v44\n\tv_exp_f32 v45, v45\n\tv_exp_f32 v46, v46\n\tv_exp_f32 v47, v47\n\tv_exp_f32 v48, v48\n\tv_exp_f32 v49, v49\n\tv_smfmac_f32_32x32x32_bf16 v[64:79], v[180:183], v[40:47], v192\n\tv_exp_f32 v50, v50\n\tv_exp_f32 v51, v51\n\tv_exp_f32 v52, v52\n\tv_exp_f32 v53, v53\n\tv_mfma_f32_32x32x16_bf16 v[32:47], v[96:99], v[184:187], 0\n\tds_read_b128 v[176:179], v195 offset:8320\n\tv_exp_f32 v54, v54\n\tv_exp_f32 v55, v55\n\tv_exp_f32 v56, v56\n\tv_exp_f32 v57, v57\n\tv_smfmac_f32_32x32x32_bf16 v[80:95], v[172:175], v[48:55], v192\n\tv_exp_f32 v58, v58\n\tv_exp_f32 v59, v59\n\tv_exp_f32 v60, v60\n\tv_exp_f32 v61, v61\n\tv_exp_f32 v62, v62\n\tv_exp_f32 v63, v63\n\tv_exp_f32 v32, v32\n\tv_exp_f32 v33, v33\n\tv_smfmac_f32_32x32x32_bf16 v[80:95], v[180:183], v[56:63], v192\n\tv_exp_f32 v34, v34\n\tv_exp_f32 v35, v35\n\tv_exp_f32 v36, v36\n\tv_exp_f32 v37, v37\n\tv_mfma_f32_32x32x16_bf16 v[48:63], v[96:99], v[188:191], 0\n\tds_read_b128 v[96:99], v193 offset:12480\n\tv_exp_f32 v38, v38\n\tv_exp_f32 v39, v39\n\tv_exp_f32 v40, v40\n\tv_exp_f32 v41, v41\n\ts_waitcnt lgkmcnt(1)\n\tv_smfmac_f32_32x32x32_bf16 v[64:79], v[168:171], v[32:39], v192\n\tds_read_b128 v[172:175], v194 offset:10400\n\tv_exp_f32 v42, v42\n\tv_exp_f32 v43, v43\n\tv_exp_f32 v44, v44\n\tv_exp_f32 v45, v45\n\tv_exp_f32 v46, v46\n\tv_exp_f32 v47, v47\n\tv_exp_f32 v48, v48\n\tv_exp_f32 v49, v49\n\tv_smfmac_f32_32x32x32_bf16 v[64:79], v[176:179], v[40:47], v192\n\tv_exp_f32 v50, v50\n\tv_exp_f32 v51, v51\n\tv_exp_f32 v52, v52\n\tv_exp_f32 v53, v53\n\tv_mfma_f32_32x32x16_bf16 v[32:47], v[100:103], v[184:187], 0\n\tds_read_b128 v[180:183], v195 offset:10400\n\tv_exp_f32 v54, v54\n\tv_exp_f32 v55, v55\n\tv_exp_f32 v56, v56\n\tv_exp_f32 v57, v57\n\tv_smfmac_f32_32x32x32_bf16 v[80:95], v[168:171], v[48:55], v192\n\tv_exp_f32 v58, v58\n\tv_exp_f32 v59, v59\n\tv_exp_f32 v60, v60\n\tv_exp_f32 v61, v61\n\tv_exp_f32 v62, v62\n\tv_exp_f32 v63, v63\n\tv_exp_f32 v32, v32\n\tv_exp_f32 v33, v33\n\tv_smfmac_f32_32x32x32_bf16 v[80:95], v[176:179], v[56:63], v192\n\tv_exp_f32 v34, v34\n\tv_exp_f32 v35, v35\n\tv_exp_f32 v36, v36\n\tv_exp_f32 v37, v37\n\tv_mfma_f32_32x32x16_bf16 v[48:63], v[100:103], v[188:191], 0\n\tds_read_b128 v[100:103], v193 offset:14560\n\tv_exp_f32 v38, v38\n\tv_exp_f32 v39, v39\n\tv_exp_f32 v40, v40\n\tv_exp_f32 v41, v41\n\ts_waitcnt lgkmcnt(1)\n\tv_smfmac_f32_32x32x32_bf16 v[64:79], v[172:175], v[32:39], v192\n\tds_read_b128 v[168:171], v194 offset:12480\n\tds_read_b32 v197, v198 offset:40\n\tv_exp_f32 v42, v42\n\tv_exp_f32 v43, v43\n\tv_exp_f32 v44, v44\n\tv_exp_f32 v45, v45\n\tv_exp_f32 v46, v46\n\tv_exp_f32 v47, v47\n\tv_exp_f32 v48, v48\n\tv_exp_f32 v49, v49\n\tv_smfmac_f32_32x32x32_bf16 v[64:79], v[180:183], v[40:47], v192\n\tv_exp_f32 v50, v50\n\tv_exp_f32 v51, v51\n\tv_exp_f32 v52, v52\n\tv_exp_f32 v53, v53\n\tv_mfma_f32_32x32x16_bf16 v[32:47], v[96:99], v[184:187], 0\n\tds_read_b128 v[176:179], v195 offset:12480\n\tv_exp_f32 v54, v54\n\tv_exp_f32 v55, v55\n\tv_exp_f32 v56, v56\n\tv_exp_f32 v57, v57\n\tv_smfmac_f32_32x32x32_bf16 v[80:95], v[172:175], v[48:55], v192\n\tv_exp_f32 v58, v58\n\tv_exp_f32 v59, v59\n\tv_exp_f32 v60, v60\n\tv_exp_f32 v61, v61\n\tv_exp_f32 v62, v62\n\tv_exp_f32 v63, v63\n\tv_exp_f32 v32, v32\n\tv_exp_f32 v33, v33\n\tv_smfmac_f32_32x32x32_bf16 v[80:95], v[180:183], v[56:63], v192\n\ts_waitcnt lgkmcnt(1)\n\tv_readfirstlane_b32 s78, v197\n\tv_exp_f32 v34, v34\n\tv_exp_f32 v35, v35\n\tv_exp_f32 v36, v36\n\ts_cmp_ge_u32 s78, 20\n\ts_cbranch_scc0 MQA_SP3\n\tMQA_BK3:\n\tv_exp_f32 v37, v37\n\tv_mfma_f32_32x32x16_bf16 v[48:63], v[96:99], v[188:191], 0\n\tds_read_b128 v[96:99], v193 offset:16640\n\tv_exp_f32 v38, v38\n\tv_exp_f32 v39, v39\n\tv_exp_f32 v40, v40\n\tv_exp_f32 v41, v41\n\ts_waitcnt lgkmcnt(1)\n\tv_smfmac_f32_32x32x32_bf16 v[64:79], v[168:171], v[32:39], v192\n\tds_read_b128 v[172:175], v194 offset:14560\n\tv_exp_f32 v42, v42\n\tv_exp_f32 v43, v43\n\tv_exp_f32 v44, v44\n\tv_exp_f32 v45, v45\n\tv_exp_f32 v46, v46\n\tv_exp_f32 v47, v47\n\tv_exp_f32 v48, v48\n\tv_exp_f32 v49, v49\n\tv_smfmac_f32_32x32x32_bf16 v[64:79], v[176:179], v[40:47], v192\n\tv_exp_f32 v50, v50\n\tv_exp_f32 v51, v51\n\tv_exp_f32 v52, v52\n\tv_exp_f32 v53, v53\n\tv_mfma_f32_32x32x16_bf16 v[32:47], v[100:103], v[184:187], 0\n\tds_read_b128 v[180:183], v195 offset:14560\n\tv_exp_f32 v54, v54\n\tv_exp_f32 v55, v55\n\tv_exp_f32 v56, v56\n\tv_exp_f32 v57, v57\n\tv_smfmac_f32_32x32x32_bf16 v[80:95], v[168:171], v[48:55], v192\n\tv_exp_f32 v58, v58\n\tv_exp_f32 v59, v59\n\tv_exp_f32 v60, v60\n\tv_exp_f32 v61, v61\n\tv_exp_f32 v62, v62\n\tv_exp_f32 v63, v63\n\tv_exp_f32 v32, v32\n\tv_exp_f32 v33, v33\n\tv_smfmac_f32_32x32x32_bf16 v[80:95], v[176:179], v[56:63], v192\n\tv_exp_f32 v34, v34\n\tv_exp_f32 v35, v35\n\tv_exp_f32 v36, v36\n\tv_exp_f32 v37, v37\n\tv_mfma_f32_32x32x16_bf16 v[48:63], v[100:103], v[188:191], 0\n\tds_read_b128 v[100:103], v193 offset:18720\n\tv_exp_f32 v38, v38\n\tv_exp_f32 v39, v39\n\tv_exp_f32 v40, v40\n\tv_exp_f32 v41, v41\n\ts_waitcnt lgkmcnt(1)\n\tv_smfmac_f32_32x32x32_bf16 v[64:79], v[172:175], v[32:39], v192\n\tds_read_b128 v[168:171], v194 offset:16640\n\tv_exp_f32 v42, v42\n\tv_exp_f32 v43, v43\n\tv_exp_f32 v44, v44\n\tv_exp_f32 v45, v45\n\tv_exp_f32 v46, v46\n\tv_exp_f32 v47, v47\n\tv_exp_f32 v48, v48\n\tv_exp_f32 v49, v49\n\tv_smfmac_f32_32x32x32_bf16 v[64:79], v[180:183], v[40:47], v192\n\tv_exp_f32 v50, v50\n\tv_exp_f32 v51, v51\n\tv_exp_f32 v52, v52\n\tv_exp_f32 v53, v53\n\tv_mfma_f32_32x32x16_bf16 v[32:47], v[96:99], v[184:187], 0\n\tds_read_b128 v[176:179], v195 offset:16640\n\tv_exp_f32 v54, v54\n\tv_exp_f32 v55, v55\n\tv_exp_f32 v56, v56\n\tv_exp_f32 v57, v57\n\tv_smfmac_f32_32x32x32_bf16 v[80:95], v[172:175], v[48:55], v192\n\tv_exp_f32 v58, v58\n\tv_exp_f32 v59, v59\n\tv_exp_f32 v60, v60\n\tv_exp_f32 v61, v61\n\tv_exp_f32 v62, v62\n\tv_exp_f32 v63, v63\n\tv_exp_f32 v32, v32\n\tv_exp_f32 v33, v33\n\tv_smfmac_f32_32x32x32_bf16 v[80:95], v[180:183], v[56:63], v192\n\tv_exp_f32 v34, v34\n\tv_exp_f32 v35, v35\n\tv_exp_f32 v36, v36\n\tv_exp_f32 v37, v37\n\tv_mfma_f32_32x32x16_bf16 v[48:63], v[96:99], v[188:191], 0\n\tds_read_b128 v[96:99], v193 offset:20800\n\tv_exp_f32 v38, v38\n\tv_exp_f32 v39, v39\n\tv_exp_f32 v40, v40\n\tv_exp_f32 v41, v41\n\ts_waitcnt lgkmcnt(1)\n\tv_smfmac_f32_32x32x32_bf16 v[64:79], v[168:171], v[32:39], v192\n\tds_read_b128 v[172:175], v194 offset:18720\n\tv_exp_f32 v42, v42\n\tv_exp_f32 v43, v43\n\tv_exp_f32 v44, v44\n\tv_exp_f32 v45, v45\n\tv_exp_f32 v46, v46\n\tv_exp_f32 v47, v47\n\tv_exp_f32 v48, v48\n\tv_exp_f32 v49, v49\n\tv_smfmac_f32_32x32x32_bf16 v[64:79], v[176:179], v[40:47], v192\n\tv_exp_f32 v50, v50\n\tv_exp_f32 v51, v51\n\tv_exp_f32 v52, v52\n\tv_exp_f32 v53, v53\n\tv_mfma_f32_32x32x16_bf16 v[32:47], v[100:103], v[184:187], 0\n\tds_read_b128 v[180:183], v195 offset:18720\n\tv_exp_f32 v54, v54\n\tv_exp_f32 v55, v55\n\tv_exp_f32 v56, v56\n\tv_exp_f32 v57, v57\n\tv_smfmac_f32_32x32x32_bf16 v[80:95], v[168:171], v[48:55], v192\n\tv_exp_f32 v58, v58\n\tv_exp_f32 v59, v59\n\tv_exp_f32 v60, v60\n\tv_exp_f32 v61, v61\n\tv_exp_f32 v62, v62\n\tv_exp_f32 v63, v63\n\tv_exp_f32 v32, v32\n\tv_exp_f32 v33, v33\n\tv_smfmac_f32_32x32x32_bf16 v[80:95], v[176:179], v[56:63], v192\n\tv_exp_f32 v34, v34\n\tv_exp_f32 v35, v35\n\tv_exp_f32 v36, v36\n\tv_exp_f32 v37, v37\n\tv_mfma_f32_32x32x16_bf16 v[48:63], v[100:103], v[188:191], 0\n\tds_read_b128 v[100:103], v193 offset:22880\n\tv_exp_f32 v38, v38\n\tv_exp_f32 v39, v39\n\tv_exp_f32 v40, v40\n\tv_exp_f32 v41, v41\n\ts_waitcnt lgkmcnt(1)\n\tv_smfmac_f32_32x32x32_bf16 v[64:79], v[172:175], v[32:39], v192\n\tds_read_b128 v[168:171], v194 offset:20800\n\tv_exp_f32 v42, v42\n\tv_exp_f32 v43, v43\n\tv_exp_f32 v44, v44\n\tv_exp_f32 v45, v45\n\tv_exp_f32 v46, v46\n\tv_exp_f32 v47, v47\n\tv_exp_f32 v48, v48\n\tv_exp_f32 v49, v49\n\tv_smfmac_f32_32x32x32_bf16 v[64:79], v[180:183], v[40:47], v192\n\tv_exp_f32 v50, v50\n\tv_exp_f32 v51, v51\n\tv_exp_f32 v52, v52\n\tv_exp_f32 v53, v53\n\tv_mfma_f32_32x32x16_bf16 v[32:47], v[96:99], v[184:187], 0\n\tds_read_b128 v[176:179], v195 offset:20800\n\tv_exp_f32 v54, v54\n\tv_exp_f32 v55, v55\n\tv_exp_f32 v56, v56\n\tv_exp_f32 v57, v57\n\tv_smfmac_f32_32x32x32_bf16 v[80:95], v[172:175], v[48:55], v192\n\tv_exp_f32 v58, v58\n\tv_exp_f32 v59, v59\n\tv_exp_f32 v60, v60\n\tv_exp_f32 v61, v61\n\tv_exp_f32 v62, v62\n\tv_exp_f32 v63, v63\n\tv_exp_f32 v32, v32\n\tv_exp_f32 v33, v33\n\tv_smfmac_f32_32x32x32_bf16 v[80:95], v[180:183], v[56:63], v192\n\tv_exp_f32 v34, v34\n\tv_exp_f32 v35, v35\n\tv_exp_f32 v36, v36\n\tv_exp_f32 v37, v37\n\tv_mfma_f32_32x32x16_bf16 v[48:63], v[96:99], v[188:191], 0\n\tds_read_b128 v[96:99], v193 offset:24960\n\tv_exp_f32 v38, v38\n\tv_exp_f32 v39, v39\n\tv_exp_f32 v40, v40\n\tv_exp_f32 v41, v41\n\ts_waitcnt lgkmcnt(1)\n\tv_smfmac_f32_32x32x32_bf16 v[64:79], v[168:171], v[32:39], v192\n\tds_read_b128 v[172:175], v194 offset:22880\n\tv_exp_f32 v42, v42\n\tv_exp_f32 v43, v43\n\tv_exp_f32 v44, v44\n\tv_exp_f32 v45, v45\n\tv_exp_f32 v46, v46\n\tv_exp_f32 v47, v47\n\tv_exp_f32 v48, v48\n\tv_exp_f32 v49, v49\n\tv_smfmac_f32_32x32x32_bf16 v[64:79], v[176:179], v[40:47], v192\n\tv_exp_f32 v50, v50\n\tv_exp_f32 v51, v51\n\tv_exp_f32 v52, v52\n\tv_exp_f32 v53, v53\n\tv_mfma_f32_32x32x16_bf16 v[32:47], v[100:103], v[184:187], 0\n\tds_read_b128 v[180:183], v195 offset:22880\n\tv_exp_f32 v54, v54\n\tv_exp_f32 v55, v55\n\tv_exp_f32 v56, v56\n\tv_exp_f32 v57, v57\n\tv_smfmac_f32_32x32x32_bf16 v[80:95], v[168:171], v[48:55], v192\n\tv_exp_f32 v58, v58\n\tv_exp_f32 v59, v59\n\tv_exp_f32 v60, v60\n\tv_exp_f32 v61, v61\n\tv_exp_f32 v62, v62\n\tv_exp_f32 v63, v63\n\tv_exp_f32 v32, v32\n\tv_exp_f32 v33, v33\n\tv_smfmac_f32_32x32x32_bf16 v[80:95], v[176:179], v[56:63], v192\n\tv_exp_f32 v34, v34\n\tv_exp_f32 v35, v35\n\tv_exp_f32 v36, v36\n\tv_exp_f32 v37, v37\n\tv_mfma_f32_32x32x16_bf16 v[48:63], v[100:103], v[188:191], 0\n\tds_read_b128 v[100:103], v193 offset:27040\n\tv_exp_f32 v38, v38\n\tv_exp_f32 v39, v39\n\tv_exp_f32 v40, v40\n\tv_exp_f32 v41, v41\n\ts_waitcnt lgkmcnt(1)\n\tv_smfmac_f32_32x32x32_bf16 v[64:79], v[172:175], v[32:39], v192\n\tds_read_b128 v[168:171], v194 offset:24960\n\tv_exp_f32 v42, v42\n\tv_exp_f32 v43, v43\n\tv_exp_f32 v44, v44\n\tv_exp_f32 v45, v45\n\tv_exp_f32 v46, v46\n\tv_exp_f32 v47, v47\n\tv_exp_f32 v48, v48\n\tv_exp_f32 v49, v49\n\tv_smfmac_f32_32x32x32_bf16 v[64:79], v[180:183], v[40:47], v192\n\tv_exp_f32 v50, v50\n\tv_exp_f32 v51, v51\n\tv_exp_f32 v52, v52\n\tv_exp_f32 v53, v53\n\tv_mfma_f32_32x32x16_bf16 v[32:47], v[96:99], v[184:187], 0\n\tds_read_b128 v[176:179], v195 offset:24960\n\tv_exp_f32 v54, v54\n\tv_exp_f32 v55, v55\n\tv_exp_f32 v56, v56\n\tv_exp_f32 v57, v57\n\tv_smfmac_f32_32x32x32_bf16 v[80:95], v[172:175], v[48:55], v192\n\tv_exp_f32 v58, v58\n\tv_exp_f32 v59, v59\n\tv_exp_f32 v60, v60\n\tv_exp_f32 v61, v61\n\tv_exp_f32 v62, v62\n\tv_exp_f32 v63, v63\n\tv_exp_f32 v32, v32\n\tv_exp_f32 v33, v33\n\tv_smfmac_f32_32x32x32_bf16 v[80:95], v[180:183], v[56:63], v192\n\tv_exp_f32 v34, v34\n\tv_exp_f32 v35, v35\n\tv_exp_f32 v36, v36\n\tv_exp_f32 v37, v37\n\tv_mfma_f32_32x32x16_bf16 v[48:63], v[96:99], v[188:191], 0\n\tds_read_b128 v[96:99], v193 offset:29120\n\tv_exp_f32 v38, v38\n\tv_exp_f32 v39, v39\n\tv_exp_f32 v40, v40\n\tv_exp_f32 v41, v41\n\ts_waitcnt lgkmcnt(1)\n\tv_smfmac_f32_32x32x32_bf16 v[64:79], v[168:171], v[32:39], v192\n\tds_read_b128 v[172:175], v194 offset:27040\n\tv_exp_f32 v42, v42\n\tv_exp_f32 v43, v43\n\tv_exp_f32 v44, v44\n\tv_exp_f32 v45, v45\n\tv_exp_f32 v46, v46\n\tv_exp_f32 v47, v47\n\tv_exp_f32 v48, v48\n\tv_exp_f32 v49, v49\n\tv_smfmac_f32_32x32x32_bf16 v[64:79], v[176:179], v[40:47], v192\n\tv_exp_f32 v50, v50\n\tv_exp_f32 v51, v51\n\tv_exp_f32 v52, v52\n\tv_exp_f32 v53, v53\n\tv_mfma_f32_32x32x16_bf16 v[32:47], v[100:103], v[184:187], 0\n\tds_read_b128 v[180:183], v195 offset:27040\n\tv_exp_f32 v54, v54\n\tv_exp_f32 v55, v55\n\tv_exp_f32 v56, v56\n\tv_exp_f32 v57, v57\n\tv_smfmac_f32_32x32x32_bf16 v[80:95], v[168:171], v[48:55], v192\n\tv_exp_f32 v58, v58\n\tv_exp_f32 v59, v59\n\tv_exp_f32 v60, v60\n\tv_exp_f32 v61, v61\n\tv_exp_f32 v62, v62\n\tv_exp_f32 v63, v63\n\tv_exp_f32 v32, v32\n\tv_exp_f32 v33, v33\n\tv_smfmac_f32_32x32x32_bf16 v[80:95], v[176:179], v[56:63], v192\n\tv_exp_f32 v34, v34\n\tv_exp_f32 v35, v35\n\tv_exp_f32 v36, v36\n\tv_exp_f32 v37, v37\n\tv_mfma_f32_32x32x16_bf16 v[48:63], v[100:103], v[188:191], 0\n\tds_read_b128 v[100:103], v193 offset:31200\n\tv_exp_f32 v38, v38\n\tv_exp_f32 v39, v39\n\tv_exp_f32 v40, v40\n\tv_exp_f32 v41, v41\n\ts_waitcnt lgkmcnt(1)\n\tv_smfmac_f32_32x32x32_bf16 v[64:79], v[172:175], v[32:39], v192\n\tds_read_b128 v[168:171], v194 offset:29120\n\tv_exp_f32 v42, v42\n\tv_exp_f32 v43, v43\n\tv_exp_f32 v44, v44\n\tv_exp_f32 v45, v45\n\tv_exp_f32 v46, v46\n\tv_exp_f32 v47, v47\n\tv_exp_f32 v48, v48\n\tv_exp_f32 v49, v49\n\tv_smfmac_f32_32x32x32_bf16 v[64:79], v[180:183], v[40:47], v192\n\tv_exp_f32 v50, v50\n\tv_exp_f32 v51, v51\n\tv_exp_f32 v52, v52\n\tv_exp_f32 v53, v53\n\tv_mfma_f32_32x32x16_bf16 v[32:47], v[96:99], v[184:187], 0\n\tds_read_b128 v[176:179], v195 offset:29120\n\tv_exp_f32 v54, v54\n\tv_exp_f32 v55, v55\n\tv_exp_f32 v56, v56\n\tv_exp_f32 v57, v57\n\tv_smfmac_f32_32x32x32_bf16 v[80:95], v[172:175], v[48:55], v192\n\tv_exp_f32 v58, v58\n\tv_exp_f32 v59, v59\n\tv_exp_f32 v60, v60\n\tv_exp_f32 v61, v61\n\tv_exp_f32 v62, v62\n\tv_exp_f32 v63, v63\n\tv_exp_f32 v32, v32\n\tv_exp_f32 v33, v33\n\tv_smfmac_f32_32x32x32_bf16 v[80:95], v[180:183], v[56:63], v192\n\tv_exp_f32 v34, v34\n\tv_exp_f32 v35, v35\n\tv_exp_f32 v36, v36\n\tv_exp_f32 v37, v37\n\tv_mfma_f32_32x32x16_bf16 v[48:63], v[96:99], v[188:191], 0\n\tds_read_b128 v[96:99], v193 offset:33280\n\tv_exp_f32 v38, v38\n\tv_exp_f32 v39, v39\n\tv_exp_f32 v40, v40\n\tv_exp_f32 v41, v41\n\ts_waitcnt lgkmcnt(1)\n\tv_smfmac_f32_32x32x32_bf16 v[64:79], v[168:171], v[32:39], v192\n\tds_read_b128 v[172:175], v194 offset:31200\n\tv_exp_f32 v42, v42\n\tv_exp_f32 v43, v43\n\tv_exp_f32 v44, v44\n\tv_exp_f32 v45, v45\n\tv_exp_f32 v46, v46\n\tv_exp_f32 v47, v47\n\tv_exp_f32 v48, v48\n\tv_exp_f32 v49, v49\n\tv_smfmac_f32_32x32x32_bf16 v[64:79], v[176:179], v[40:47], v192\n\tv_exp_f32 v50, v50\n\tv_exp_f32 v51, v51\n\tv_exp_f32 v52, v52\n\tv_exp_f32 v53, v53\n\tv_mfma_f32_32x32x16_bf16 v[32:47], v[100:103], v[184:187], 0\n\tds_read_b128 v[180:183], v195 offset:31200\n\tv_exp_f32 v54, v54\n\tv_exp_f32 v55, v55\n\tv_exp_f32 v56, v56\n\tv_exp_f32 v57, v57\n\tv_smfmac_f32_32x32x32_bf16 v[80:95], v[168:171], v[48:55], v192\n\tv_exp_f32 v58, v58\n\tv_exp_f32 v59, v59\n\tv_exp_f32 v60, v60\n\tv_exp_f32 v61, v61\n\tv_exp_f32 v62, v62\n\tv_exp_f32 v63, v63\n\tv_exp_f32 v32, v32\n\tv_exp_f32 v33, v33\n\tv_smfmac_f32_32x32x32_bf16 v[80:95], v[176:179], v[56:63], v192\n\tv_exp_f32 v34, v34\n\tv_exp_f32 v35, v35\n\tv_exp_f32 v36, v36\n\tv_exp_f32 v37, v37\n\tv_mfma_f32_32x32x16_bf16 v[48:63], v[100:103], v[188:191], 0\n\tds_read_b128 v[100:103], v193 offset:35360\n\tv_exp_f32 v38, v38\n\tv_exp_f32 v39, v39\n\tv_exp_f32 v40, v40\n\tv_exp_f32 v41, v41\n\ts_waitcnt lgkmcnt(1)\n\tv_smfmac_f32_32x32x32_bf16 v[64:79], v[172:175], v[32:39], v192\n\tds_read_b128 v[168:171], v194 offset:33280\n\tv_exp_f32 v42, v42\n\tv_exp_f32 v43, v43\n\tv_exp_f32 v44, v44\n\tv_exp_f32 v45, v45\n\tv_exp_f32 v46, v46\n\tv_exp_f32 v47, v47\n\tv_exp_f32 v48, v48\n\tv_exp_f32 v49, v49\n\tv_smfmac_f32_32x32x32_bf16 v[64:79], v[180:183], v[40:47], v192\n\tv_exp_f32 v50, v50\n\tv_exp_f32 v51, v51\n\tv_exp_f32 v52, v52\n\tv_exp_f32 v53, v53\n\tv_mfma_f32_32x32x16_bf16 v[32:47], v[96:99], v[184:187], 0\n\tds_read_b128 v[176:179], v195 offset:33280\n\tv_exp_f32 v54, v54\n\tv_exp_f32 v55, v55\n\tv_exp_f32 v56, v56\n\tv_exp_f32 v57, v57\n\tv_smfmac_f32_32x32x32_bf16 v[80:95], v[172:175], v[48:55], v192\n\tv_exp_f32 v58, v58\n\tv_exp_f32 v59, v59\n\tv_exp_f32 v60, v60\n\tv_exp_f32 v61, v61\n\tv_exp_f32 v62, v62\n\tv_exp_f32 v63, v63\n\tv_exp_f32 v32, v32\n\tv_exp_f32 v33, v33\n\tv_smfmac_f32_32x32x32_bf16 v[80:95], v[180:183], v[56:63], v192\n\tv_exp_f32 v34, v34\n\tv_exp_f32 v35, v35\n\tv_exp_f32 v36, v36\n\tv_exp_f32 v37, v37\n\tv_mfma_f32_32x32x16_bf16 v[48:63], v[96:99], v[188:191], 0\n\tds_read_b128 v[96:99], v193 offset:37440\n\tv_exp_f32 v38, v38\n\tv_exp_f32 v39, v39\n\tv_exp_f32 v40, v40\n\tv_exp_f32 v41, v41\n\ts_waitcnt lgkmcnt(1)\n\tv_smfmac_f32_32x32x32_bf16 v[64:79], v[168:171], v[32:39], v192\n\tds_read_b128 v[172:175], v194 offset:35360\n\tv_exp_f32 v42, v42\n\tv_exp_f32 v43, v43\n\tv_exp_f32 v44, v44\n\tv_exp_f32 v45, v45\n\tv_exp_f32 v46, v46\n\tv_exp_f32 v47, v47\n\tv_exp_f32 v48, v48\n\tv_exp_f32 v49, v49\n\tv_smfmac_f32_32x32x32_bf16 v[64:79], v[176:179], v[40:47], v192\n\tv_exp_f32 v50, v50\n\tv_exp_f32 v51, v51\n\tv_exp_f32 v52, v52\n\tv_exp_f32 v53, v53\n\tv_mfma_f32_32x32x16_bf16 v[32:47], v[100:103], v[184:187], 0\n\tds_read_b128 v[180:183], v195 offset:35360\n\tv_exp_f32 v54, v54\n\tv_exp_f32 v55, v55\n\tv_exp_f32 v56, v56\n\tv_exp_f32 v57, v57\n\tv_smfmac_f32_32x32x32_bf16 v[80:95], v[168:171], v[48:55], v192\n\tv_exp_f32 v58, v58\n\tv_exp_f32 v59, v59\n\tv_exp_f32 v60, v60\n\tv_exp_f32 v61, v61\n\tv_exp_f32 v62, v62\n\tv_exp_f32 v63, v63\n\tv_exp_f32 v32, v32\n\tv_exp_f32 v33, v33\n\tv_smfmac_f32_32x32x32_bf16 v[80:95], v[176:179], v[56:63], v192\n\tv_exp_f32 v34, v34\n\tv_exp_f32 v35, v35\n\tv_exp_f32 v36, v36\n\tv_exp_f32 v37, v37\n\tv_mfma_f32_32x32x16_bf16 v[48:63], v[100:103], v[188:191], 0\n\tds_read_b128 v[100:103], v193 offset:39520\n\tv_exp_f32 v38, v38\n\tv_exp_f32 v39, v39\n\tv_exp_f32 v40, v40\n\tv_exp_f32 v41, v41\n\ts_waitcnt lgkmcnt(1)\n\tv_smfmac_f32_32x32x32_bf16 v[64:79], v[172:175], v[32:39], v192\n\tds_read_b128 v[168:171], v194 offset:37440\n\tv_exp_f32 v42, v42\n\tv_exp_f32 v43, v43\n\tv_exp_f32 v44, v44\n\tv_exp_f32 v45, v45\n\tv_exp_f32 v46, v46\n\tv_exp_f32 v47, v47\n\tv_exp_f32 v48, v48\n\tv_exp_f32 v49, v49\n\tv_smfmac_f32_32x32x32_bf16 v[64:79], v[180:183], v[40:47], v192\n\tv_exp_f32 v50, v50\n\tv_exp_f32 v51, v51\n\tv_exp_f32 v52, v52\n\tv_exp_f32 v53, v53\n\tv_mfma_f32_32x32x16_bf16 v[32:47], v[96:99], v[184:187], 0\n\tds_read_b128 v[176:179], v195 offset:37440\n\tv_exp_f32 v54, v54\n\tv_exp_f32 v55, v55\n\tv_exp_f32 v56, v56\n\tv_exp_f32 v57, v57\n\tv_smfmac_f32_32x32x32_bf16 v[80:95], v[172:175], v[48:55], v192\n\tv_exp_f32 v58, v58\n\tv_exp_f32 v59, v59\n\tv_exp_f32 v60, v60\n\tv_exp_f32 v61, v61\n\tv_exp_f32 v62, v62\n\tv_exp_f32 v63, v63\n\tv_exp_f32 v32, v32\n\tv_exp_f32 v33, v33\n\tv_smfmac_f32_32x32x32_bf16 v[80:95], v[180:183], v[56:63], v192\n\tv_exp_f32 v34, v34\n\tv_exp_f32 v35, v35\n\tv_exp_f32 v36, v36\n\tv_exp_f32 v37, v37\n\tv_mfma_f32_32x32x16_bf16 v[48:63], v[96:99], v[188:191], 0\n\tds_read_b128 v[96:99], v193 offset:41600\n\tv_exp_f32 v38, v38\n\tv_exp_f32 v39, v39\n\tv_exp_f32 v40, v40\n\tv_exp_f32 v41, v41\n\ts_waitcnt lgkmcnt(1)\n\tv_smfmac_f32_32x32x32_bf16 v[64:79], v[168:171], v[32:39], v192\n\tds_read_b128 v[172:175], v194 offset:39520\n\tv_exp_f32 v42, v42\n\tv_exp_f32 v43, v43\n\tv_exp_f32 v44, v44\n\tv_exp_f32 v45, v45\n\tv_exp_f32 v46, v46\n\tv_exp_f32 v47, v47\n\tv_exp_f32 v48, v48\n\tv_exp_f32 v49, v49\n\tv_smfmac_f32_32x32x32_bf16 v[64:79], v[176:179], v[40:47], v192\n\tv_exp_f32 v50, v50\n\tv_exp_f32 v51, v51\n\tv_exp_f32 v52, v52\n\tv_exp_f32 v53, v53\n\tv_mfma_f32_32x32x16_bf16 v[32:47], v[100:103], v[184:187], 0\n\tds_read_b128 v[180:183], v195 offset:39520\n\tv_exp_f32 v54, v54\n\tv_exp_f32 v55, v55\n\tv_exp_f32 v56, v56\n\tv_exp_f32 v57, v57\n\tv_smfmac_f32_32x32x32_bf16 v[80:95], v[168:171], v[48:55], v192\n\tv_exp_f32 v58, v58\n\tv_exp_f32 v59, v59\n\tv_exp_f32 v60, v60\n\tv_exp_f32 v61, v61\n\tv_exp_f32 v62, v62\n\tv_exp_f32 v63, v63\n\tv_exp_f32 v32, v32\n\tv_exp_f32 v33, v33\n\tv_smfmac_f32_32x32x32_bf16 v[80:95], v[176:179], v[56:63], v192\n\tv_exp_f32 v34, v34\n\tv_exp_f32 v35, v35\n\tv_exp_f32 v36, v36\n\tv_exp_f32 v37, v37\n\tv_mfma_f32_32x32x16_bf16 v[48:63], v[100:103], v[188:191], 0\n\tds_read_b128 v[100:103], v193 offset:43680\n\tv_exp_f32 v38, v38\n\tv_exp_f32 v39, v39\n\tv_exp_f32 v40, v40\n\tv_exp_f32 v41, v41\n\ts_waitcnt lgkmcnt(1)\n\tv_smfmac_f32_32x32x32_bf16 v[64:79], v[172:175], v[32:39], v192\n\tds_read_b128 v[168:171], v194 offset:41600\n\tv_exp_f32 v42, v42\n\tv_exp_f32 v43, v43\n\tv_exp_f32 v44, v44\n\tv_exp_f32 v45, v45\n\tv_exp_f32 v46, v46\n\tv_exp_f32 v47, v47\n\tv_exp_f32 v48, v48\n\tv_exp_f32 v49, v49\n\tv_smfmac_f32_32x32x32_bf16 v[64:79], v[180:183], v[40:47], v192\n\tv_exp_f32 v50, v50\n\tv_exp_f32 v51, v51\n\tv_exp_f32 v52, v52\n\tv_exp_f32 v53, v53\n\tv_mfma_f32_32x32x16_bf16 v[32:47], v[96:99], v[184:187], 0\n\tds_read_b128 v[176:179], v195 offset:41600\n\tv_exp_f32 v54, v54\n\tv_exp_f32 v55, v55\n\tv_exp_f32 v56, v56\n\tv_exp_f32 v57, v57\n\tv_smfmac_f32_32x32x32_bf16 v[80:95], v[172:175], v[48:55], v192\n\tv_exp_f32 v58, v58\n\tv_exp_f32 v59, v59\n\tv_exp_f32 v60, v60\n\tv_exp_f32 v61, v61\n\tv_exp_f32 v62, v62\n\tv_exp_f32 v63, v63\n\tv_exp_f32 v32, v32\n\tv_exp_f32 v33, v33\n\tv_smfmac_f32_32x32x32_bf16 v[80:95], v[180:183], v[56:63], v192\n\tbuffer_load_dwordx4 v[104:107], v196, s[80:83], s84 offen offset:0 sc1\n\tv_exp_f32 v34, v34\n\tv_exp_f32 v35, v35\n\tv_exp_f32 v36, v36\n\tv_exp_f32 v37, v37\n\tv_mfma_f32_32x32x16_bf16 v[48:63], v[96:99], v[188:191], 0\n\tds_read_b128 v[96:99], v193 offset:45760\n\tv_exp_f32 v38, v38\n\tv_exp_f32 v39, v39\n\tv_exp_f32 v40, v40\n\tv_exp_f32 v41, v41\n\ts_waitcnt lgkmcnt(1)\n\tv_smfmac_f32_32x32x32_bf16 v[64:79], v[168:171], v[32:39], v192\n\tds_read_b128 v[172:175], v194 offset:43680\n\tv_exp_f32 v42, v42\n\tv_exp_f32 v43, v43\n\tv_exp_f32 v44, v44\n\tv_exp_f32 v45, v45\n\tv_exp_f32 v46, v46\n\tv_exp_f32 v47, v47\n\tv_exp_f32 v48, v48\n\tv_exp_f32 v49, v49\n\tv_smfmac_f32_32x32x32_bf16 v[64:79], v[176:179], v[40:47], v192\n\tv_exp_f32 v50, v50\n\tv_exp_f32 v51, v51\n\tv_exp_f32 v52, v52\n\tv_exp_f32 v53, v53\n\tv_mfma_f32_32x32x16_bf16 v[32:47], v[100:103], v[184:187], 0\n\tds_read_b128 v[180:183], v195 offset:43680\n\tbuffer_load_dwordx4 v[108:111], v196, s[80:83], s84 offen offset:1024 sc1\n\tv_exp_f32 v54, v54\n\tv_exp_f32 v55, v55\n\tv_exp_f32 v56, v56\n\tv_exp_f32 v57, v57\n\tv_smfmac_f32_32x32x32_bf16 v[80:95], v[168:171], v[48:55], v192\n\tv_exp_f32 v58, v58\n\tv_exp_f32 v59, v59\n\tv_exp_f32 v60, v60\n\tv_exp_f32 v61, v61\n\tv_exp_f32 v62, v62\n\tv_exp_f32 v63, v63\n\tv_exp_f32 v32, v32\n\tv_exp_f32 v33, v33\n\tv_smfmac_f32_32x32x32_bf16 v[80:95], v[176:179], v[56:63], v192\n\tbuffer_load_dwordx4 v[112:115], v196, s[80:83], s84 offen offset:2048 sc1\n\tv_exp_f32 v34, v34\n\tv_exp_f32 v35, v35\n\tv_exp_f32 v36, v36\n\tv_exp_f32 v37, v37\n\tv_mfma_f32_32x32x16_bf16 v[48:63], v[100:103], v[188:191], 0\n\tds_read_b128 v[100:103], v193 offset:47840\n\tv_exp_f32 v38, v38\n\tv_exp_f32 v39, v39\n\tv_exp_f32 v40, v40\n\tv_exp_f32 v41, v41\n\ts_waitcnt lgkmcnt(1)\n\tv_smfmac_f32_32x32x32_bf16 v[64:79], v[172:175], v[32:39], v192\n\tds_read_b128 v[168:171], v194 offset:45760\n\tv_exp_f32 v42, v42\n\tv_exp_f32 v43, v43\n\tv_exp_f32 v44, v44\n\tv_exp_f32 v45, v45\n\tv_exp_f32 v46, v46\n\tv_exp_f32 v47, v47\n\tv_exp_f32 v48, v48\n\tv_exp_f32 v49, v49\n\tv_smfmac_f32_32x32x32_bf16 v[64:79], v[180:183], v[40:47], v192\n\tv_exp_f32 v50, v50\n\tv_exp_f32 v51, v51\n\tv_exp_f32 v52, v52\n\tv_exp_f32 v53, v53\n\tv_mfma_f32_32x32x16_bf16 v[32:47], v[96:99], v[184:187], 0\n\tds_read_b128 v[176:179], v195 offset:45760\n\tbuffer_load_dwordx4 v[116:119], v196, s[80:83], s84 offen offset:3072 sc1\n\ts_add_u32 s84, s84, 4096\n\tv_exp_f32 v54, v54\n\tv_exp_f32 v55, v55\n\tv_exp_f32 v56, v56\n\tv_exp_f32 v57, v57\n\tv_smfmac_f32_32x32x32_bf16 v[80:95], v[172:175], v[48:55], v192\n\tv_exp_f32 v58, v58\n\tv_exp_f32 v59, v59\n\tv_exp_f32 v60, v60\n\tv_exp_f32 v61, v61\n\tv_exp_f32 v62, v62\n\tv_exp_f32 v63, v63\n\tv_exp_f32 v32, v32\n\tv_exp_f32 v33, v33\n\tv_smfmac_f32_32x32x32_bf16 v[80:95], v[180:183], v[56:63], v192\n\tbuffer_load_dwordx4 v[120:123], v196, s[80:83], s84 offen offset:0 sc1\n\tv_exp_f32 v34, v34\n\tv_exp_f32 v35, v35\n\tv_exp_f32 v36, v36\n\tv_exp_f32 v37, v37\n\tv_mfma_f32_32x32x16_bf16 v[48:63], v[96:99], v[188:191], 0\n\tds_read_b128 v[96:99], v193 offset:49920\n\tv_exp_f32 v38, v38\n\tv_exp_f32 v39, v39\n\tv_exp_f32 v40, v40\n\tv_exp_f32 v41, v41\n\ts_waitcnt lgkmcnt(1)\n\tv_smfmac_f32_32x32x32_bf16 v[64:79], v[168:171], v[32:39], v192\n\tds_read_b128 v[172:175], v194 offset:47840\n\tv_exp_f32 v42, v42\n\tv_exp_f32 v43, v43\n\tv_exp_f32 v44, v44\n\tv_exp_f32 v45, v45\n\tv_exp_f32 v46, v46\n\tv_exp_f32 v47, v47\n\tv_exp_f32 v48, v48\n\tv_exp_f32 v49, v49\n\tv_smfmac_f32_32x32x32_bf16 v[64:79], v[176:179], v[40:47], v192\n\tv_exp_f32 v50, v50\n\tv_exp_f32 v51, v51\n\tv_exp_f32 v52, v52\n\tv_exp_f32 v53, v53\n\tv_mfma_f32_32x32x16_bf16 v[32:47], v[100:103], v[184:187], 0\n\tds_read_b128 v[180:183], v195 offset:47840\n\tbuffer_load_dwordx4 v[124:127], v196, s[80:83], s84 offen offset:1024 sc1\n\tv_exp_f32 v54, v54\n\tv_exp_f32 v55, v55\n\tv_exp_f32 v56, v56\n\tv_exp_f32 v57, v57\n\tv_smfmac_f32_32x32x32_bf16 v[80:95], v[168:171], v[48:55], v192\n\tv_exp_f32 v58, v58\n\tv_exp_f32 v59, v59\n\tv_exp_f32 v60, v60\n\tv_exp_f32 v61, v61\n\tv_exp_f32 v62, v62\n\tv_exp_f32 v63, v63\n\tv_exp_f32 v32, v32\n\tv_exp_f32 v33, v33\n\tv_smfmac_f32_32x32x32_bf16 v[80:95], v[176:179], v[56:63], v192\n\tbuffer_load_dwordx4 v[128:131], v196, s[80:83], s84 offen offset:2048 sc1\n\tv_exp_f32 v34, v34\n\tv_exp_f32 v35, v35\n\tv_exp_f32 v36, v36\n\tv_exp_f32 v37, v37\n\tv_mfma_f32_32x32x16_bf16 v[48:63], v[100:103], v[188:191], 0\n\tds_read_b128 v[100:103], v193 offset:52000\n\tv_exp_f32 v38, v38\n\tv_exp_f32 v39, v39\n\tv_exp_f32 v40, v40\n\tv_exp_f32 v41, v41\n\ts_waitcnt lgkmcnt(1)\n\tv_smfmac_f32_32x32x32_bf16 v[64:79], v[172:175], v[32:39], v192\n\tds_read_b128 v[168:171], v194 offset:49920\n\tv_exp_f32 v42, v42\n\tv_exp_f32 v43, v43\n\tv_exp_f32 v44, v44\n\tv_exp_f32 v45, v45\n\tv_exp_f32 v46, v46\n\tv_exp_f32 v47, v47\n\tv_exp_f32 v48, v48\n\tv_exp_f32 v49, v49\n\tv_smfmac_f32_32x32x32_bf16 v[64:79], v[180:183], v[40:47], v192\n\tv_exp_f32 v50, v50\n\tv_exp_f32 v51, v51\n\tv_exp_f32 v52, v52\n\tv_exp_f32 v53, v53\n\tv_mfma_f32_32x32x16_bf16 v[32:47], v[96:99], v[184:187], 0\n\tds_read_b128 v[176:179], v195 offset:49920\n\tbuffer_load_dwordx4 v[132:135], v196, s[80:83], s84 offen offset:3072 sc1\n\ts_add_u32 s84, s84, 4096\n\tv_exp_f32 v54, v54\n\tv_exp_f32 v55, v55\n\tv_exp_f32 v56, v56\n\tv_exp_f32 v57, v57\n\tv_smfmac_f32_32x32x32_bf16 v[80:95], v[172:175], v[48:55], v192\n\tv_exp_f32 v58, v58\n\tv_exp_f32 v59, v59\n\tv_exp_f32 v60, v60\n\tv_exp_f32 v61, v61\n\tv_exp_f32 v62, v62\n\tv_exp_f32 v63, v63\n\tv_exp_f32 v32, v32\n\tv_exp_f32 v33, v33\n\tv_smfmac_f32_32x32x32_bf16 v[80:95], v[180:183], v[56:63], v192\n\tbuffer_load_dwordx4 v[136:139], v196, s[80:83], s84 offen offset:0 sc1\n\tv_exp_f32 v34, v34\n\tv_exp_f32 v35, v35\n\tv_exp_f32 v36, v36\n\tv_exp_f32 v37, v37\n\tv_mfma_f32_32x32x16_bf16 v[48:63], v[96:99], v[188:191], 0\n\tds_read_b128 v[96:99], v193 offset:54080\n\tv_exp_f32 v38, v38\n\tv_exp_f32 v39, v39\n\tv_exp_f32 v40, v40\n\tv_exp_f32 v41, v41\n\ts_waitcnt lgkmcnt(1)\n\tv_smfmac_f32_32x32x32_bf16 v[64:79], v[168:171], v[32:39], v192\n\tds_read_b128 v[172:175], v194 offset:52000\n\tv_exp_f32 v42, v42\n\tv_exp_f32 v43, v43\n\tv_exp_f32 v44, v44\n\tv_exp_f32 v45, v45\n\tv_exp_f32 v46, v46\n\tv_exp_f32 v47, v47\n\tv_exp_f32 v48, v48\n\tv_exp_f32 v49, v49\n\tv_smfmac_f32_32x32x32_bf16 v[64:79], v[176:179], v[40:47], v192\n\tv_exp_f32 v50, v50\n\tv_exp_f32 v51, v51\n\tv_exp_f32 v52, v52\n\tv_exp_f32 v53, v53\n\tv_mfma_f32_32x32x16_bf16 v[32:47], v[100:103], v[184:187], 0\n\tds_read_b128 v[180:183], v195 offset:52000\n\tbuffer_load_dwordx4 v[140:143], v196, s[80:83], s84 offen offset:1024 sc1\n\tv_exp_f32 v54, v54\n\tv_exp_f32 v55, v55\n\tv_exp_f32 v56, v56\n\tv_exp_f32 v57, v57\n\tv_smfmac_f32_32x32x32_bf16 v[80:95], v[168:171], v[48:55], v192\n\tv_exp_f32 v58, v58\n\tv_exp_f32 v59, v59\n\tv_exp_f32 v60, v60\n\tv_exp_f32 v61, v61\n\tv_exp_f32 v62, v62\n\tv_exp_f32 v63, v63\n\tv_exp_f32 v32, v32\n\tv_exp_f32 v33, v33\n\tv_smfmac_f32_32x32x32_bf16 v[80:95], v[176:179], v[56:63], v192\n\tbuffer_load_dwordx4 v[144:147], v196, s[80:83], s84 offen offset:2048 sc1\n\tv_exp_f32 v34, v34\n\tv_exp_f32 v35, v35\n\tv_exp_f32 v36, v36\n\tv_exp_f32 v37, v37\n\tv_mfma_f32_32x32x16_bf16 v[48:63], v[100:103], v[188:191], 0\n\tds_read_b128 v[100:103], v193 offset:56160\n\tv_exp_f32 v38, v38\n\tv_exp_f32 v39, v39\n\tv_exp_f32 v40, v40\n\tv_exp_f32 v41, v41\n\ts_waitcnt lgkmcnt(1)\n\tv_smfmac_f32_32x32x32_bf16 v[64:79], v[172:175], v[32:39], v192\n\tds_read_b128 v[168:171], v194 offset:54080\n\tv_exp_f32 v42, v42\n\tv_exp_f32 v43, v43\n\tv_exp_f32 v44, v44\n\tv_exp_f32 v45, v45\n\tv_exp_f32 v46, v46\n\tv_exp_f32 v47, v47\n\tv_exp_f32 v48, v48\n\tv_exp_f32 v49, v49\n\tv_smfmac_f32_32x32x32_bf16 v[64:79], v[180:183], v[40:47], v192\n\tv_exp_f32 v50, v50\n\tv_exp_f32 v51, v51\n\tv_exp_f32 v52, v52\n\tv_exp_f32 v53, v53\n\tv_mfma_f32_32x32x16_bf16 v[32:47], v[96:99], v[184:187], 0\n\tds_read_b128 v[176:179], v195 offset:54080\n\tbuffer_load_dwordx4 v[148:151], v196, s[80:83], s84 offen offset:3072 sc1\n\ts_add_u32 s84, s84, 4096\n\tv_exp_f32 v54, v54\n\tv_exp_f32 v55, v55\n\tv_exp_f32 v56, v56\n\tv_exp_f32 v57, v57\n\tv_smfmac_f32_32x32x32_bf16 v[80:95], v[172:175], v[48:55], v192\n\tv_exp_f32 v58, v58\n\tv_exp_f32 v59, v59\n\tv_exp_f32 v60, v60\n\tv_exp_f32 v61, v61\n\tv_exp_f32 v62, v62\n\tv_exp_f32 v63, v63\n\tv_exp_f32 v32, v32\n\tv_exp_f32 v33, v33\n\tv_smfmac_f32_32x32x32_bf16 v[80:95], v[180:183], v[56:63], v192\n\tbuffer_load_dwordx4 v[152:155], v196, s[80:83], s84 offen offset:0 sc1\n\tv_exp_f32 v34, v34\n\tv_exp_f32 v35, v35\n\tv_exp_f32 v36, v36\n\tv_exp_f32 v37, v37\n\tv_mfma_f32_32x32x16_bf16 v[48:63], v[96:99], v[188:191], 0\n\tds_read_b128 v[96:99], v193 offset:58240\n\tv_exp_f32 v38, v38\n\tv_exp_f32 v39, v39\n\tv_exp_f32 v40, v40\n\tv_exp_f32 v41, v41\n\ts_waitcnt lgkmcnt(1)\n\tv_smfmac_f32_32x32x32_bf16 v[64:79], v[168:171], v[32:39], v192\n\tds_read_b128 v[172:175], v194 offset:56160\n\tv_exp_f32 v42, v42\n\tv_exp_f32 v43, v43\n\tv_exp_f32 v44, v44\n\tv_exp_f32 v45, v45\n\tv_exp_f32 v46, v46\n\tv_exp_f32 v47, v47\n\tv_exp_f32 v48, v48\n\tv_exp_f32 v49, v49\n\tv_smfmac_f32_32x32x32_bf16 v[64:79], v[176:179], v[40:47], v192\n\tv_exp_f32 v50, v50\n\tv_exp_f32 v51, v51\n\tv_exp_f32 v52, v52\n\tv_exp_f32 v53, v53\n\tv_mfma_f32_32x32x16_bf16 v[32:47], v[100:103], v[184:187], 0\n\tds_read_b128 v[180:183], v195 offset:56160\n\tbuffer_load_dwordx4 v[156:159], v196, s[80:83], s84 offen offset:1024 sc1\n\tv_exp_f32 v54, v54\n\tv_exp_f32 v55, v55\n\tv_exp_f32 v56, v56\n\tv_exp_f32 v57, v57\n\tv_smfmac_f32_32x32x32_bf16 v[80:95], v[168:171], v[48:55], v192\n\tv_exp_f32 v58, v58\n\tv_exp_f32 v59, v59\n\tv_exp_f32 v60, v60\n\tv_exp_f32 v61, v61\n\tv_exp_f32 v62, v62\n\tv_exp_f32 v63, v63\n\tv_exp_f32 v32, v32\n\tv_exp_f32 v33, v33\n\tv_smfmac_f32_32x32x32_bf16 v[80:95], v[176:179], v[56:63], v192\n\tbuffer_load_dwordx4 v[160:163], v196, s[80:83], s84 offen offset:2048 sc1\n\tv_exp_f32 v34, v34\n\tv_exp_f32 v35, v35\n\tv_exp_f32 v36, v36\n\tv_exp_f32 v37, v37\n\tv_mfma_f32_32x32x16_bf16 v[48:63], v[100:103], v[188:191], 0\n\tds_read_b128 v[100:103], v193 offset:60320\n\tv_exp_f32 v38, v38\n\tv_exp_f32 v39, v39\n\tv_exp_f32 v40, v40\n\tv_exp_f32 v41, v41\n\ts_waitcnt lgkmcnt(1)\n\tv_smfmac_f32_32x32x32_bf16 v[64:79], v[172:175], v[32:39], v192\n\tds_read_b128 v[168:171], v194 offset:58240\n\tv_exp_f32 v42, v42\n\tv_exp_f32 v43, v43\n\tv_exp_f32 v44, v44\n\tv_exp_f32 v45, v45\n\tv_exp_f32 v46, v46\n\tv_exp_f32 v47, v47\n\tv_exp_f32 v48, v48\n\tv_exp_f32 v49, v49\n\tv_smfmac_f32_32x32x32_bf16 v[64:79], v[180:183], v[40:47], v192\n\tv_exp_f32 v50, v50\n\tv_exp_f32 v51, v51\n\tv_exp_f32 v52, v52\n\tv_exp_f32 v53, v53\n\tv_mfma_f32_32x32x16_bf16 v[32:47], v[96:99], v[184:187], 0\n\tds_read_b128 v[176:179], v195 offset:58240\n\tbuffer_load_dwordx4 v[164:167], v196, s[80:83], s84 offen offset:3072 sc1\n\tv_exp_f32 v54, v54\n\tv_exp_f32 v55, v55\n\tv_exp_f32 v56, v56\n\tv_exp_f32 v57, v57\n\tv_smfmac_f32_32x32x32_bf16 v[80:95], v[172:175], v[48:55], v192\n\tv_exp_f32 v58, v58\n\tv_exp_f32 v59, v59\n\tv_exp_f32 v60, v60\n\tv_exp_f32 v61, v61\n\tv_exp_f32 v62, v62\n\tv_exp_f32 v63, v63\n\tv_exp_f32 v32, v32\n\tv_exp_f32 v33, v33\n\tv_smfmac_f32_32x32x32_bf16 v[80:95], v[180:183], v[56:63], v192\n\tv_exp_f32 v34, v34\n\tv_exp_f32 v35, v35\n\tv_exp_f32 v36, v36\n\tv_exp_f32 v37, v37\n\tv_mfma_f32_32x32x16_bf16 v[48:63], v[96:99], v[188:191], 0\n\tds_read_b128 v[96:99], v193 offset:62400\n\tv_exp_f32 v38, v38\n\tv_exp_f32 v39, v39\n\tv_exp_f32 v40, v40\n\tv_exp_f32 v41, v41\n\ts_waitcnt lgkmcnt(1)\n\tv_smfmac_f32_32x32x32_bf16 v[64:79], v[168:171], v[32:39], v192\n\tds_read_b128 v[172:175], v194 offset:60320\n\tv_exp_f32 v42, v42\n\tv_exp_f32 v43, v43\n\tv_exp_f32 v44, v44\n\tv_exp_f32 v45, v45\n\tv_exp_f32 v46, v46\n\tv_exp_f32 v47, v47\n\tv_exp_f32 v48, v48\n\tv_exp_f32 v49, v49\n\tv_smfmac_f32_32x32x32_bf16 v[64:79], v[176:179], v[40:47], v192\n\tv_exp_f32 v50, v50\n\tv_exp_f32 v51, v51\n\tv_exp_f32 v52, v52\n\tv_exp_f32 v53, v53\n\tv_mfma_f32_32x32x16_bf16 v[32:47], v[100:103], v[184:187], 0\n\tds_read_b128 v[180:183], v195 offset:60320\n\tv_exp_f32 v54, v54\n\tv_exp_f32 v55, v55\n\tv_exp_f32 v56, v56\n\tv_exp_f32 v57, v57\n\tv_smfmac_f32_32x32x32_bf16 v[80:95], v[168:171], v[48:55], v192\n\tglobal_load_dwordx2 %[x0], %[xo], %[xb0]\n\tglobal_load_dwordx2 %[x1], %[xo], %[xb1]\n\tbuffer_load_dword %[g0], %[go], s[80:83], %[gs] offen sc1\n\tbuffer_load_dword %[g1], %[go], s[80:83], %[gs] offen offset:64 sc1\n\tv_exp_f32 v58, v58\n\tv_exp_f32 v59, v59\n\tv_exp_f32 v60, v60\n\tv_exp_f32 v61, v61\n\tv_exp_f32 v62, v62\n\tv_exp_f32 v63, v63\n\tv_exp_f32 v32, v32\n\tv_exp_f32 v33, v33\n\tv_smfmac_f32_32x32x32_bf16 v[80:95], v[176:179], v[56:63], v192\n\tv_exp_f32 v34, v34\n\tv_exp_f32 v35, v35\n\tv_exp_f32 v36, v36\n\tv_exp_f32 v37, v37\n\tv_mfma_f32_32x32x16_bf16 v[48:63], v[100:103], v[188:191], 0\n\tds_read_b128 v[100:103], v193 offset:64480\n\tv_exp_f32 v38, v38\n\tv_exp_f32 v39, v39\n\tv_exp_f32 v40, v40\n\tv_exp_f32 v41, v41\n\ts_waitcnt lgkmcnt(1)\n\tv_smfmac_f32_32x32x32_bf16 v[64:79], v[172:175], v[32:39], v192\n\tds_read_b128 v[168:171], v194 offset:62400\n\tv_exp_f32 v42, v42\n\tv_exp_f32 v43, v43\n\tv_exp_f32 v44, v44\n\tv_exp_f32 v45, v45\n\tv_exp_f32 v46, v46\n\tv_exp_f32 v47, v47\n\tv_exp_f32 v48, v48\n\tv_exp_f32 v49, v49\n\tv_smfmac_f32_32x32x32_bf16 v[64:79], v[180:183], v[40:47], v192\n\tv_exp_f32 v50, v50\n\tv_exp_f32 v51, v51\n\tv_exp_f32 v52, v52\n\tv_exp_f32 v53, v53\n\tv_mfma_f32_32x32x16_bf16 v[32:47], v[96:99], v[184:187], 0\n\tds_read_b128 v[176:179], v195 offset:62400\n\tv_exp_f32 v54, v54\n\tv_exp_f32 v55, v55\n\tv_exp_f32 v56, v56\n\tv_exp_f32 v57, v57\n\tv_smfmac_f32_32x32x32_bf16 v[80:95], v[172:175], v[48:55], v192\n\tv_exp_f32 v58, v58\n\tv_exp_f32 v59, v59\n\tv_exp_f32 v60, v60\n\tv_exp_f32 v61, v61\n\tv_exp_f32 v62, v62\n\tv_exp_f32 v63, v63\n\tv_exp_f32 v32, v32\n\tv_exp_f32 v33, v33\n\tv_smfmac_f32_32x32x32_bf16 v[80:95], v[180:183], v[56:63], v192\n\tv_exp_f32 v34, v34\n\tv_exp_f32 v35, v35\n\tv_exp_f32 v36, v36\n\tv_exp_f32 v37, v37\n\tv_mfma_f32_32x32x16_bf16 v[48:63], v[96:99], v[188:191], 0\n\tv_exp_f32 v38, v38\n\tv_exp_f32 v39, v39\n\tv_exp_f32 v40, v40\n\tv_exp_f32 v41, v41\n\ts_waitcnt lgkmcnt(0)\n\tv_smfmac_f32_32x32x32_bf16 v[64:79], v[168:171], v[32:39], v192\n\tds_read_b128 v[172:175], v194 offset:64480\n\tv_exp_f32 v42, v42\n\tv_exp_f32 v43, v43\n\tv_exp_f32 v44, v44\n\tv_exp_f32 v45, v45\n\tv_exp_f32 v46, v46\n\tv_exp_f32 v47, v47\n\tv_exp_f32 v48, v48\n\tv_exp_f32 v49, v49\n\tv_smfmac_f32_32x32x32_bf16 v[64:79], v[176:179], v[40:47], v192\n\tv_exp_f32 v50, v50\n\tv_exp_f32 v51, v51\n\tv_exp_f32 v52, v52\n\tv_exp_f32 v53, v53\n\tv_mfma_f32_32x32x16_bf16 v[32:47], v[100:103], v[184:187], 0\n\tds_read_b128 v[180:183], v195 offset:64480\n\tv_exp_f32 v54, v54\n\tv_exp_f32 v55, v55\n\tv_exp_f32 v56, v56\n\tv_exp_f32 v57, v57\n\tv_smfmac_f32_32x32x32_bf16 v[80:95], v[168:171], v[48:55], v192\n\tv_exp_f32 v58, v58\n\tv_exp_f32 v59, v59\n\tv_exp_f32 v60, v60\n\tv_exp_f32 v61, v61\n\tv_exp_f32 v62, v62\n\tv_exp_f32 v63, v63\n\tv_exp_f32 v32, v32\n\tv_exp_f32 v33, v33\n\tv_smfmac_f32_32x32x32_bf16 v[80:95], v[176:179], v[56:63], v192\n\tv_exp_f32 v34, v34\n\tv_exp_f32 v35, v35\n\tv_exp_f32 v36, v36\n\tv_exp_f32 v37, v37\n\tv_mfma_f32_32x32x16_bf16 v[48:63], v[100:103], v[188:191], 0\n\tv_exp_f32 v38, v38\n\tv_exp_f32 v39, v39\n\tv_exp_f32 v40, v40\n\tv_exp_f32 v41, v41\n\ts_waitcnt lgkmcnt(0)\n\tv_smfmac_f32_32x32x32_bf16 v[64:79], v[172:175], v[32:39], v192\n\tv_exp_f32 v42, v42\n\tv_exp_f32 v43, v43\n\tv_exp_f32 v44, v44\n\tv_exp_f32 v45, v45\n\tv_exp_f32 v46, v46\n\tv_exp_f32 v47, v47\n\tv_exp_f32 v48, v48\n\tv_exp_f32 v49, v49\n\tv_smfmac_f32_32x32x32_bf16 v[64:79], v[180:183], v[40:47], v192\n\tv_exp_f32 v50, v50\n\tv_exp_f32 v51, v51\n\tv_exp_f32 v52, v52\n\tv_exp_f32 v53, v53\n\tv_exp_f32 v54, v54\n\tv_exp_f32 v55, v55\n\tv_exp_f32 v56, v56\n\tv_exp_f32 v57, v57\n\tv_smfmac_f32_32x32x32_bf16 v[80:95], v[172:175], v[48:55], v192\n\tv_exp_f32 v58, v58\n\tv_exp_f32 v59, v59\n\tv_exp_f32 v60, v60\n\tv_exp_f32 v61, v61\n\tv_exp_f32 v62, v62\n\tv_exp_f32 v63, v63\n\ts_nop 1\n\tv_smfmac_f32_32x32x32_bf16 v[80:95], v[180:183], v[56:63], v192\n\ts_waitcnt vmcnt(0) lgkmcnt(0)"
                     : "=&{v[64:79]}"(o0), "=&{v[80:95]}"(o1), "=&{v[104:119]}"(W0), "=&{v[120:135]}"(W1), "=&{v[136:151]}"(W2), "=&{v[152:167]}"(W3),
                       [x0] "=&v"(xres[0]), [x1] "=&v"(xres[1]), [g0] "=&v"(gres[0]), [g1] "=&v"(gres[1]),
                       "+{v193}"(kp), "+{v194}"(vp), "+{v195}"(vp1), "+{s84}"(wso) ASTV
                     : "{v[184:187]}"(qa), "{v[188:191]}"(qb), "{v196}"(l * 16), "{v198}"(LB + L_MISC), "{s[80:83]}"(srd),
                       [xo] "v"(xo), [go] "v"(go), [xb0] "s"(x), [xb1] "s"(x + 16 * 512), [gs] "s"(WS_GATE)
                     : "v32", "v33", "v34", "v35", "v36", "v37", "v38", "v39", "v40", "v41", "v42", "v43", "v44", "v45", "v46", "v47", "v48", "v49", "v50", "v51", "v52", "v53", "v54", "v55", "v56", "v57", "v58", "v59", "v60", "v61", "v62", "v63", "v96", "v97", "v98", "v99", "v100", "v101", "v102", "v103", "v168", "v169", "v170", "v171", "v172", "v173", "v174", "v175", "v176", "v177", "v178", "v179", "v180", "v181", "v182", "v183", "v192", "v197", "v199", ASTC "s78", "s79", "s85", "vcc", "scc", "memory");
        __builtin_amdgcn_s_setprio(0);
        ST(8); STC(31); AP(8);
        const int tha = (w >> 1) & 1;
        #pragma unroll
        for (int g = 0; g < 2; ++g) {
            if (g == 1) asm volatile("" : "+{v[80:95]}"(o1) :: "memory");
            f32x16& oa = g ? o1 : o0;
            const float ls = l < 32 ? oa[8] : oa[12];
            bool ok = (ls >= 8.6736174e-19f) & (ls <= 1.2676506e30f) &
                      (__builtin_fabsf(((oa[0] + oa[2]) + (oa[4] + oa[6])) + ((oa[1] + oa[3]) + (oa[5] + oa[7]))) < __builtin_inff());
            if (__builtin_amdgcn_ballot_w64(!ok)) oa = attn_true_max(L + l * 16, L + vrel0, L + vrel1, g ? qb : qa);
            const float inv = __builtin_amdgcn_rcpf(l < 32 ? oa[8] : oa[12]);
            u32x4 o;
            #pragma unroll
            for (int i = 0; i < 4; ++i) { unsigned uu = pk2(oa[2 * i] * inv, oa[2 * i + 1] * inv); o[i] = uu < 0xFFFFFFFEu ? uu : 0xFFFFFFFEu; }
            STORE128(o, rs, l * 16, WS_OF + ((tb * 2 + tha) * 32 + 4 * hg + 2 * (w & 1) + g) * 1024);
        }
        ST(19);
    } else {
        const int u = w - 4;
#define PRET 0
        if ((PRET & 1) && u == 0) touch(l * 8192 + hg * 128, wq, LB + L_FW1 + 1024);
        if ((PRET & 2) && u == 1) touch((64 * (b & 7) + l) * 1024 + 32 * (b >> 3), wo, LB + L_FW1 + 1280);
        if ((PRET & 4) && u == 2) touch(l < 16 ? WS_OF + ((tb * 2 + (l >> 3)) * 32 + 4 * hg) * 1024 + (l & 7) * 512 : l < 32 ? WS_F1 + (l & 7) * (256 * FSTR) : l < 48 ? WS_KF + tb * 2048 : WS_VF + tb * 2048, ws, LB + L_FW1 + 1536);
        if ((PRET & 8) && u == 3) touch((tb * 64 + l) * 1024, out, LB + L_FW1 + 1792);
        const int pc0 = (16 * (j & 3) * QROT + u) & 63;
        unsigned qvo = pc0 * 8192 + (l >> 3) * 1024 + (((l & 7) ^ (((l >> 4) & 1) << 2)) * 16), qd = pc0 * 1024;
        const char* src = (const char*)wq + hg * 128;
        const unsigned xvo = (l >> 4) * 1024 + ((l15 ^ (4 * u + g4)) * 16);
        const int r1 = ((tg + 1) & 3) * 16 + 4 * u, r2 = ((tg + 2) & 3) * 16 + 4 * u, r3 = ((tg + 3) & 3) * 16 + 4 * u;
        const unsigned xv1 = xvo + r1 * 1024, xv2 = xvo + r2 * 1024, xv3 = xvo + r3 * 1024;
#define QISSUE(i) do { const unsigned kq = ((j & 3) * QROT + (i)) & 3; const char* xq = xt + kq * 256; const unsigned mq = LB + L_XT + kq * 16384; \
            dma16n(xv1, xq, mq + r1 * 256); dma16n(xv2, xq, mq + r2 * 256); dma16n(xv3, xq, mq + r3 * 256); wq_issue<4>(qvo, qd, src, LB + L_WQ); } while (0)
        const unsigned tsa = WS_TG + u * 8192, tsb = tsa + 4096;
#define TISSUE() asm volatile("buffer_load_dwordx4 v[104:107], %[vo], s[80:83], %[sa] offen offset:0 sc1\n\tbuffer_load_dwordx4 v[108:111], %[vo], s[80:83], %[sa] offen offset:1024 sc1\n\tbuffer_load_dwordx4 v[112:115], %[vo], s[80:83], %[sa] offen offset:2048 sc1\n\tbuffer_load_dwordx4 v[116:119], %[vo], s[80:83], %[sa] offen offset:3072 sc1\n\tbuffer_load_dwordx4 v[120:123], %[vo], s[80:83], %[sb] offen offset:0 sc1\n\tbuffer_load_dwordx4 v[124:127], %[vo], s[80:83], %[sb] offen offset:1024 sc1\n\tbuffer_load_dwordx4 v[128:131], %[vo], s[80:83], %[sb] offen offset:2048 sc1\n\tbuffer_load_dwordx4 v[132:135], %[vo], s[80:83], %[sb] offen offset:3072 sc1" :: [vo] "v"(l * 16), [sa] "s"(tsa), [sb] "s"(tsb), "{s[80:83]}"(srd) : "v104", "v105", "v106", "v107", "v108", "v109", "v110", "v111", "v112", "v113", "v114", "v115", "v116", "v117", "v118", "v119", "v120", "v121", "v122", "v123", "v124", "v125", "v126", "v127", "v128", "v129", "v130", "v131", "v132", "v133", "v134", "v135", "memory")
        if (u == RW && type == 0) { dma16n(l * 16, w_r, LB + L_GW); dma16n(l * 16, gate_w, LB + L_GW + 1024); }
        QISSUE(0); QISSUE(1);
        GAS unsigned* fp = fp0;
        if (HOIST) asm volatile("" : "+v"(fp));
        ST(54);
        if (QWIN == 3) QISSUE(2);
        wait_vmn<7 * (QWIN - 1)>(); lds_signal(L, L_CQ, l); ST(56);
        if (QWIN == 3) QISSUE(3); else QISSUE(2);
        wait_vmn<7 * (QWIN - 1)>(); lds_signal(L, L_CQ + 4, l); ST(57);
        asm volatile("v_readfirstlane_b32 %0, %1" : "=s"(tag) : "v"(E) : "memory");
        tag += 1u;
        if (QWIN == 2) QISSUE(3);
        if (u == RW && type == 0) { ROUTER(); ST(2); }
        if (TPOS == 1) TISSUE();
        const unsigned ak = LB + 2 * u * REC + (l15 + 32 * (g4 & 1)) * 16 + (g4 >> 1) * 8;
        const unsigned av = LB + 2 * u * REC + 1024 + (g4 >> 1) * 256 + l15 * 16 + (g4 & 1) * 8;
        asm volatile("" :: "v"(ak), "v"(av), "v"(ak + REC), "v"(av + REC), "s"(tag));
        wait_vmn<7 + (TPOS < 2 ? 8 : 0)>(); lds_signal(L, L_CQ + 8, l); ST(58);
        if (TPOS == 2) TISSUE();
        lds_wait_ge(L, L_C5, 1u);
        const u32x4 oz = {(l & 1) ? 0u : ones[0], (l & 1) ? 0u : ones[0], (l & 1) ? 0u : ones[0], (l & 1) ? 0u : ones[0]};
        if (l < 4) *(LAS u32x4*)(L + (2 * u + (l >> 1)) * REC + 2048 + (l & 1) * 16) = oz;
        wait_vmn<(TPOS < 3 ? 8 : 0)>(); lds_signal(L, L_CQ + 12, l);
        ST(1); AP(1);
        unsigned fsm;
        asm volatile("buffer_load_dwordx4 v[104:107], %[vo], s[80:83], %[sa] offen offset:0 sc1\n\tbuffer_load_dwordx4 v[108:111], %[vo], s[80:83], %[sa] offen offset:1024 sc1\n\tbuffer_load_dwordx4 v[112:115], %[vo], s[80:83], %[sa] offen offset:2048 sc1\n\tbuffer_load_dwordx4 v[116:119], %[vo], s[80:83], %[sa] offen offset:3072 sc1\n\tbuffer_load_dwordx4 v[120:123], %[vo], s[80:83], %[sb] offen offset:0 sc1\n\tbuffer_load_dwordx4 v[124:127], %[vo], s[80:83], %[sb] offen offset:1024 sc1\n\tbuffer_load_dwordx4 v[128:131], %[vo], s[80:83], %[sb] offen offset:2048 sc1\n\tbuffer_load_dwordx4 v[132:135], %[vo], s[80:83], %[sb] offen offset:3072 sc1\n\ts_mov_b32 s78, 0x40000\n\tMQA_TCHK:\n\ts_mov_b64 s[76:77], 0\n\ts_waitcnt vmcnt(7)\n\tv_cmp_ne_u32 vcc, %[tag], v105\n\tds_write2_b32 %[ak0], v104, v106 offset0:0 offset1:1\n\ts_or_b64 s[76:77], s[76:77], vcc\n\tv_cmp_ne_u32 vcc, %[tag], v107\n\ts_or_b64 s[76:77], s[76:77], vcc\n\ts_waitcnt vmcnt(6)\n\tv_cmp_ne_u32 vcc, %[tag], v109\n\tds_write2_b32 %[ak0], v108, v110 offset0:64 offset1:65\n\ts_or_b64 s[76:77], s[76:77], vcc\n\tv_cmp_ne_u32 vcc, %[tag], v111\n\ts_or_b64 s[76:77], s[76:77], vcc\n\ts_waitcnt vmcnt(5)\n\tv_cmp_ne_u32 vcc, %[tag], v113\n\tds_write2_b32 %[av0], v112, v114 offset0:0 offset1:1\n\ts_or_b64 s[76:77], s[76:77], vcc\n\tv_cmp_ne_u32 vcc, %[tag], v115\n\ts_or_b64 s[76:77], s[76:77], vcc\n\ts_waitcnt vmcnt(4)\n\tv_cmp_ne_u32 vcc, %[tag], v117\n\tds_write2_b32 %[av0], v116, v118 offset0:128 offset1:129\n\ts_or_b64 s[76:77], s[76:77], vcc\n\tv_cmp_ne_u32 vcc, %[tag], v119\n\ts_or_b64 s[76:77], s[76:77], vcc\n\ts_waitcnt vmcnt(3)\n\tv_cmp_ne_u32 vcc, %[tag], v121\n\tds_write2_b32 %[ak1], v120, v122 offset0:0 offset1:1\n\ts_or_b64 s[76:77], s[76:77], vcc\n\tv_cmp_ne_u32 vcc, %[tag], v123\n\ts_or_b64 s[76:77], s[76:77], vcc\n\ts_waitcnt vmcnt(2)\n\tv_cmp_ne_u32 vcc, %[tag], v125\n\tds_write2_b32 %[ak1], v124, v126 offset0:64 offset1:65\n\ts_or_b64 s[76:77], s[76:77], vcc\n\tv_cmp_ne_u32 vcc, %[tag], v127\n\ts_or_b64 s[76:77], s[76:77], vcc\n\ts_waitcnt vmcnt(1)\n\tv_cmp_ne_u32 vcc, %[tag], v129\n\tds_write2_b32 %[av1], v128, v130 offset0:0 offset1:1\n\ts_or_b64 s[76:77], s[76:77], vcc\n\tv_cmp_ne_u32 vcc, %[tag], v131\n\ts_or_b64 s[76:77], s[76:77], vcc\n\ts_waitcnt vmcnt(0)\n\tv_cmp_ne_u32 vcc, %[tag], v133\n\tds_write2_b32 %[av1], v132, v134 offset0:128 offset1:129\n\ts_or_b64 s[76:77], s[76:77], vcc\n\tv_cmp_ne_u32 vcc, %[tag], v135\n\ts_or_b64 s[76:77], s[76:77], vcc\n\ts_cmp_eq_u64 s[76:77], 0\n\ts_cbranch_scc1 MQA_TOK\n\ts_sub_u32 s78, s78, 1\n\ts_cmp_eq_u32 s78, 0\n\ts_cbranch_scc1 MQA_TOK\n\ts_waitcnt lgkmcnt(0)\n\tbuffer_load_dwordx4 v[104:107], %[vo], s[80:83], %[sa] offen offset:0 sc1\n\tbuffer_load_dwordx4 v[108:111], %[vo], s[80:83], %[sa] offen offset:1024 sc1\n\tbuffer_load_dwordx4 v[112:115], %[vo], s[80:83], %[sa] offen offset:2048 sc1\n\tbuffer_load_dwordx4 v[116:119], %[vo], s[80:83], %[sa] offen offset:3072 sc1\n\tbuffer_load_dwordx4 v[120:123], %[vo], s[80:83], %[sb] offen offset:0 sc1\n\tbuffer_load_dwordx4 v[124:127], %[vo], s[80:83], %[sb] offen offset:1024 sc1\n\tbuffer_load_dwordx4 v[128:131], %[vo], s[80:83], %[sb] offen offset:2048 sc1\n\tbuffer_load_dwordx4 v[132:135], %[vo], s[80:83], %[sb] offen offset:3072 sc1\n\ts_branch MQA_TCHK\n\tMQA_TOK:\n\tglobal_load_dword v101, %[fp], off sc1\n\ts_waitcnt lgkmcnt(0)\n\ts_barrier\n\ts_mov_b32 s78, 0x40000\n\tMQA_FSP:\n\ts_waitcnt vmcnt(0)\n\tv_cmp_ne_u32 vcc, %[tag], v101\n\ts_and_b32 s76, vcc_lo, 0x3ff00\n\ts_cmp_eq_u32 s76, 0\n\ts_cbranch_scc1 MQA_FDONE\n\ts_sub_u32 s78, s78, 1\n\ts_cmp_eq_u32 s78, 0\n\ts_cbranch_scc1 MQA_FDONE\n\ts_sleep 2\n\tglobal_load_dword v101, %[fp], off sc1\n\ts_branch MQA_FSP\n\tMQA_FDONE:" : "=&{v101}"(fsm) : [vo] "v"(l * 16), [sa] "s"(tsa), [sb] "s"(tsb), "{s[80:83]}"(srd), [tag] "s"(tag), [fp] "v"(fp),
                                    [ak0] "v"(ak), [av0] "v"(av), [ak1] "v"(ak + REC), [av1] "v"(av + REC)
                     : "v104", "v105", "v106", "v107", "v108", "v109", "v110", "v111", "v112", "v113", "v114", "v115", "v116", "v117", "v118", "v119", "v120", "v121", "v122", "v123", "v124", "v125", "v126", "v127", "v128", "v129", "v130", "v131", "v132", "v133", "v134", "v135", "s76", "s77", "s78", "vcc", "scc", "memory");
        ST(6); AP(6);
        __builtin_amdgcn_s_setprio(BPR);
        kv_issue<FSPL>(l * 16, srd, kso0 + 4 * 2048, kdst0 + 4 * 2 * REC);
        ST(23);
        if (l < 28) *(LAS u32x4*)(L + (8 + 14 * u + (l >> 1)) * REC + 2048 + (l & 1) * 16) = oz;
        {
            unsigned so = kso0 + (4 + FSPL) * 2048, dst = kdst0 + (4 + FSPL) * 2 * REC;
            asm volatile("v_cmp_ne_u32 vcc, %[tag], v101\n\ts_andn2_b64 s[76:77], vcc, 0xff\n\ts_cmp_eq_u64 s[76:77], 0\n\ts_cbranch_scc1 MQA_F2F\n\tglobal_load_dword v101, %[fp], off sc1\n\ts_mov_b32 s78, 0x40000\n\ts_waitcnt vmcnt(1)\n\ts_waitcnt lgkmcnt(0)\n\ts_mov_b64 exec, 1\n\tds_add_u32 %[mb], %[one] offset:4\n\ts_mov_b64 exec, -1\n\tMQA_F2L:\n\ts_waitcnt vmcnt(0)\n\tv_cmp_ne_u32 vcc, %[tag], v101\n\ts_andn2_b64 s[76:77], vcc, 0xff\n\ts_cmp_eq_u64 s[76:77], 0\n\ts_cbranch_scc1 MQA_F2S\n\ts_sub_u32 s78, s78, 1\n\ts_cmp_eq_u32 s78, 0\n\ts_cbranch_scc1 MQA_F2S\n\ts_sleep 2\n\tglobal_load_dword v101, %[fp], off sc1\n\ts_branch MQA_F2L\n\tMQA_F2S:\n\t.rept 3\n\ts_sleep 4\n\ts_mov_b32 m0, %[dst]\n\ts_add_u32 %[dst], %[dst], 4160\n\tbuffer_load_dwordx4 %[vo], s[80:83], %[so] offen sc1 lds\n\ts_add_u32 %[so], %[so], 2048\n\t.endr\n\ts_branch MQA_F2E\n\tMQA_F2F:\n\t.rept 3\n\ts_sleep 4\n\ts_mov_b32 m0, %[dst]\n\ts_add_u32 %[dst], %[dst], 4160\n\tbuffer_load_dwordx4 %[vo], s[80:83], %[so] offen sc1 lds\n\ts_add_u32 %[so], %[so], 2048\n\t.endr\n\ts_waitcnt vmcnt(3)\n\ts_waitcnt lgkmcnt(0)\n\ts_mov_b64 exec, 1\n\tds_add_u32 %[mb], %[one] offset:4\n\ts_mov_b64 exec, -1\n\tMQA_F2E:" : "+{v101}"(fsm), [so] "+s"(so), [dst] "+s"(dst)
                         : [vo] "v"(l * 16), "{s[80:83]}"(srd), [tag] "s"(tag), [fp] "v"(fp), [mb] "v"(LB + L_MISC), [one] "v"(1u)
                         : "s76", "s77", "s78", "m0", "vcc", "scc", "memory");
        }
        ST(24);
        unsigned akey = 0;
        if (u == 0) {
            const u32x4 sent = {0xFFFFFFFFu, 0xFFFFFFFFu, 0xFFFFFFFFu, 0xFFFFFFFFu};
            SENTINELS();
        } else if (u == 1) {
            akey = *(LAS unsigned*)(L + L_AKEY);
            if (type == 0 && l < 16) __hip_atomic_store((GAS unsigned*)(ws + WS_GATE) + tb * 64 + tg * 16 + l15, *(LAS unsigned*)(L + L_GT + 4 * l), RLX_AGENT);
        }
        const char* fga = (const char*)(((l & 1) ? w_up : w_gate) + (size_t)(128 * u + krow) * 1792 + 8 * (xcd * 28 + (j < 28 ? j : 27)));
        const char* fda = (const char*)(w_down + (size_t)(224 * xcd + 64 * u + krow) * 512 + 16 * j) + (l & 1) * 16;
#define FFNISSUE() do { _Pragma("unroll") for (int p = 0; p < 4; ++p) { if (KSF) __builtin_amdgcn_s_sleep(KSF); dma16p(fga + p * (32 * 1792 * 2), LB + L_FW1 + u * 4096 + p * 1024); } \
            if (KSF) __builtin_amdgcn_s_sleep(KSF); \
            dma16p(fda, LB + L_FW2 + 2 * u * 1024); \
            if (KSF) __builtin_amdgcn_s_sleep(KSF); \
            dma16p(fda + (u < 3 ? 32 * 1024 : 0), LB + L_FW2 + 2 * u * 1024 + (u < 3 ? 1024 : 0)); } while (0)
        if (FPN == 0) FFNISSUE();
        kv_paced<16 + X2 - P1, KS2>(l * 16, srd, kso0 + (4 + P1) * 2048, kdst0 + (4 + P1) * 2 * REC);
        wait_vmn<X2>(); lds_signal(L, L_C9, l);
        if (u == 0) lds_signal(L, L_C8, l);
        if (u == 1) {
            lds_wait_slow<L_C8, 5u>(LB + L_MISC);
            if (l < 8) __hip_atomic_store((GAS unsigned long long*)(ws + WS_F2 + l * 2048 + b * 8), ((unsigned long long)tag << 32) | akey, RLX_AGENT);
        }
        if (b == 0 && u == 0) {
            lds_wait_ge(L, L_C9, 4u);
            if (l == 0) __hip_atomic_store((GAS unsigned*)(ws + WS_EPOCH), tag, RLX_AGENT);
        }
        if (FPN == 1) FFNISSUE();
        kv_paced<12 - X2, KS3>(l * 16, srd, kso0 + (20 + X2) * 2048, kdst0 + (20 + X2) * 2 * REC);
        if (FPN == 2) FFNISSUE();
        wait_vmn<FPN == 2 ? 6 : 0>(); lds_signal(L, L_CA, l);
        WAIT_VM0();
        lds_signal(L, L_C7, l);
#define BPR2 BPR
#define BPR3 BPR2
        if (BPR2 != BPR) __builtin_amdgcn_s_setprio(BPR2);
        ST(34);
        if (u == 0) {
            u32x4 fl0, fl1;
            _Pragma("nounroll") for (int i = 0; i < SPIN_CAP; ++i) {
                fl0 = __builtin_amdgcn_raw_buffer_load_b128(rs, l * 16, WS_F2 + (b & 7) * 2048, 16);
                fl1 = __builtin_amdgcn_raw_buffer_load_b128(rs, l * 16, WS_F2 + (b & 7) * 2048 + 1024, 16);
                if (__all(fl0[1] == tag && fl0[3] == tag && fl1[1] == tag && fl1[3] == tag)) break;
                __builtin_amdgcn_s_sleep(JSLEEP);
                FENCE();
            }
            const unsigned key = __builtin_amdgcn_readfirstlane(wave_max(max(max(fl0[0], fl0[2]), max(fl1[0], fl1[2]))));
            dma16(l * 16, (const char*)x + (size_t)(2047 - (int)(key & 0x7ffu)) * 1024, LB + L_XSEL);
            if (l == 0) *(LAS unsigned*)(L + L_KEY) = key;
            lds_wait_ge(L, L_CA, 4u);
            WAIT_VM0();
            lds_signal(L, L_C6, l);
            lds_sig<L_CA>(LB + L_MISC, 16u);
            ST(14);
        }
        lds_wait_slow<L_C6, 1u>(LB + L_MISC);
        if (BPR3 != BPR2) __builtin_amdgcn_s_setprio(BPR3);
        if (u == 2 && j < 28) {
            lds_wait_slow<L_C7, 4u>(LB + L_MISC);
            f32x4 a;
            asm volatile("ds_read_b64_tr_b16 v[104:105], %[wp] offset:0\n\tds_read_b64_tr_b16 v[106:107], %[wp] offset:512\n\tds_read_b128 v[108:111], %[xp] offset:0\n\tds_read_b64_tr_b16 v[112:113], %[wp] offset:1024\n\tds_read_b64_tr_b16 v[114:115], %[wp] offset:1536\n\tds_read_b128 v[116:119], %[xp] offset:64\n\tds_read_b64_tr_b16 v[120:121], %[wp] offset:2048\n\tds_read_b64_tr_b16 v[122:123], %[wp] offset:2560\n\tds_read_b128 v[124:127], %[xp] offset:128\n\tds_read_b64_tr_b16 v[128:129], %[wp] offset:3072\n\tds_read_b64_tr_b16 v[130:131], %[wp] offset:3584\n\tds_read_b128 v[132:135], %[xp] offset:192\n\tds_read_b64_tr_b16 v[136:137], %[wp] offset:4096\n\tds_read_b64_tr_b16 v[138:139], %[wp] offset:4608\n\tds_read_b128 v[140:143], %[xp] offset:256\n\ts_waitcnt lgkmcnt(12)\n\tv_mfma_f32_16x16x32_bf16 v[144:147], v[104:107], v[108:111], 0\n\tds_read_b64_tr_b16 v[104:105], %[wp] offset:5120\n\tds_read_b64_tr_b16 v[106:107], %[wp] offset:5632\n\tds_read_b128 v[108:111], %[xp] offset:320\n\ts_waitcnt lgkmcnt(12)\n\tv_mfma_f32_16x16x32_bf16 v[144:147], v[112:115], v[116:119], v[144:147]\n\tds_read_b64_tr_b16 v[112:113], %[wp] offset:6144\n\tds_read_b64_tr_b16 v[114:115], %[wp] offset:6656\n\tds_read_b128 v[116:119], %[xp] offset:384\n\ts_waitcnt lgkmcnt(12)\n\tv_mfma_f32_16x16x32_bf16 v[144:147], v[120:123], v[124:127], v[144:147]\n\tds_read_b64_tr_b16 v[120:121], %[wp] offset:7168\n\tds_read_b64_tr_b16 v[122:123], %[wp] offset:7680\n\tds_read_b128 v[124:127], %[xp] offset:448\n\ts_waitcnt lgkmcnt(12)\n\tv_mfma_f32_16x16x32_bf16 v[144:147], v[128:131], v[132:135], v[144:147]\n\tds_read_b64_tr_b16 v[128:129], %[wp] offset:8192\n\tds_read_b64_tr_b16 v[130:131], %[wp] offset:8704\n\tds_read_b128 v[132:135], %[xp] offset:512\n\ts_waitcnt lgkmcnt(12)\n\tv_mfma_f32_16x16x32_bf16 v[144:147], v[136:139], v[140:143], v[144:147]\n\tds_read_b64_tr_b16 v[136:137], %[wp] offset:9216\n\tds_read_b64_tr_b16 v[138:139], %[wp] offset:9728\n\tds_read_b128 v[140:143], %[xp] offset:576\n\ts_waitcnt lgkmcnt(12)\n\tv_mfma_f32_16x16x32_bf16 v[144:147], v[104:107], v[108:111], v[144:147]\n\tds_read_b64_tr_b16 v[104:105], %[wp] offset:10240\n\tds_read_b64_tr_b16 v[106:107], %[wp] offset:10752\n\tds_read_b128 v[108:111], %[xp] offset:640\n\ts_waitcnt lgkmcnt(12)\n\tv_mfma_f32_16x16x32_bf16 v[144:147], v[112:115], v[116:119], v[144:147]\n\tds_read_b64_tr_b16 v[112:113], %[wp] offset:11264\n\tds_read_b64_tr_b16 v[114:115], %[wp] offset:11776\n\tds_read_b128 v[116:119], %[xp] offset:704\n\ts_waitcnt lgkmcnt(12)\n\tv_mfma_f32_16x16x32_bf16 v[144:147], v[120:123], v[124:127], v[144:147]\n\tds_read_b64_tr_b16 v[120:121], %[wp] offset:12288\n\tds_read_b64_tr_b16 v[122:123], %[wp] offset:12800\n\tds_read_b128 v[124:127], %[xp] offset:768\n\ts_waitcnt lgkmcnt(12)\n\tv_mfma_f32_16x16x32_bf16 v[144:147], v[128:131], v[132:135], v[144:147]\n\tds_read_b64_tr_b16 v[128:129], %[wp] offset:13312\n\tds_read_b64_tr_b16 v[130:131], %[wp] offset:13824\n\tds_read_b128 v[132:135], %[xp] offset:832\n\ts_waitcnt lgkmcnt(12)\n\tv_mfma_f32_16x16x32_bf16 v[144:147], v[136:139], v[140:143], v[144:147]\n\tds_read_b64_tr_b16 v[136:137], %[wp] offset:14336\n\tds_read_b64_tr_b16 v[138:139], %[wp] offset:14848\n\tds_read_b128 v[140:143], %[xp] offset:896\n\ts_waitcnt lgkmcnt(12)\n\tv_mfma_f32_16x16x32_bf16 v[144:147], v[104:107], v[108:111], v[144:147]\n\tds_read_b64_tr_b16 v[104:105], %[wp] offset:15360\n\tds_read_b64_tr_b16 v[106:107], %[wp] offset:15872\n\tds_read_b128 v[108:111], %[xp] offset:960\n\ts_waitcnt lgkmcnt(12)\n\tv_mfma_f32_16x16x32_bf16 v[144:147], v[112:115], v[116:119], v[144:147]\n\ts_waitcnt lgkmcnt(9)\n\tv_mfma_f32_16x16x32_bf16 v[144:147], v[120:123], v[124:127], v[144:147]\n\ts_waitcnt lgkmcnt(6)\n\tv_mfma_f32_16x16x32_bf16 v[144:147], v[128:131], v[132:135], v[144:147]\n\ts_waitcnt lgkmcnt(3)\n\tv_mfma_f32_16x16x32_bf16 v[144:147], v[136:139], v[140:143], v[144:147]\n\ts_waitcnt lgkmcnt(0)\n\tv_mfma_f32_16x16x32_bf16 v[144:147], v[104:107], v[108:111], v[144:147]\n\ts_nop 7\n\ts_nop 1" : "=&{v[144:147]}"(a) : [wp] "v"(LB + L_FW1 + 8 * l), [xp] "v"(LB + L_XSEL + g4 * 16) : "v104", "v105", "v106", "v107", "v108", "v109", "v110", "v111", "v112", "v113", "v114", "v115", "v116", "v117", "v118", "v119", "v120", "v121", "v122", "v123", "v124", "v125", "v126", "v127", "v128", "v129", "v130", "v131", "v132", "v133", "v134", "v135", "v136", "v137", "v138", "v139", "v140", "v141", "v142", "v143", "memory");
            const float a01 = (l & 1) ? a[1] : a[0], a23 = (l & 1) ? a[3] : a[2], sv = (l & 2) ? a23 : a01;
            const float uq = bfr(u2f(__builtin_amdgcn_permlane32_swap(f2u(sv), f2u(sv), false, false)[1]));
            const float gq = bfr(sv);
            const float hv = bfr(gq * sigmoidf_(gq)) * uq;
            const float hn = u2f(__builtin_amdgcn_mov_dpp(f2u(hv), 0xB1, 0xf, 0xf, true));
            const unsigned p01 = pk2(hv, hn), p23 = __builtin_amdgcn_mov_dpp(p01, 0x4E, 0xf, 0xf, true);
            const u32x4 gr = {p01, tag, p23, tag};
            if (l == 0 || l == 16) STORE128(gr, rs, g4 * 16, WS_HG + (xcd * 28 + j) * 32);
            ST(15);
        } else if (u == 3) {
            lds_wait_slow<L_C7, 4u>(LB + L_MISC);
            u32x4 fa[7];
            #pragma unroll
            for (int s = 0; s < 7; ++s) fa[s] = tr_frag(L + L_FW2 + s * 1024 + 8 * l);
            u32x4 hgp;
            _Pragma("nounroll") for (int i = 0; i < SPIN_CAP; ++i) {
                hgp = __builtin_amdgcn_raw_buffer_load_b128(rs, (l < 56 ? l : 55) * 16, WS_HG + xcd * 896, 16);
                if (__all(hgp[1] == tag && hgp[3] == tag)) break;
                __builtin_amdgcn_s_sleep(JSLEEP);
                FENCE();
            }
            u32x2 hv = {hgp[0], hgp[2]};
            if (l < 56) *(LAS u32x2*)(L + L_HBUF + l * 8) = hv;
            WAIT_LGKM0();
            f32x4 a = {0.f, 0.f, 0.f, 0.f};
            u32x4 fb[7];
            #pragma unroll
            for (int s = 0; s < 7; ++s) fb[s] = *(LAS u32x4*)(L + L_HBUF + s * 64 + g4 * 16);
            __builtin_amdgcn_sched_barrier(0);
            #pragma unroll
            for (int s = 0; s < 7; ++s) a = mfma16(fa[s], fb[s], a);
            u32x4 g0 = {f2u(a[0]), tag, f2u(a[1]), tag};
            u32x4 g1 = {f2u(a[2]), tag, f2u(a[3]), tag};
            if (l15 == 0) {
                STORE128(g0, rs, g4 * 32, WS_YG + (xcd * 512 + 16 * j) * 8);
                STORE128(g1, rs, g4 * 32 + 16, WS_YG + (xcd * 512 + 16 * j) * 8);
            }
            ST(16);
        }
        const int cg = hg & 3, n0 = cg * 128 + 16 * w;
        #pragma unroll
        for (int t = 0; t < 2; ++t) {
            const int tok = tb * 64 + (hg >> 2) * 32 + 16 * t + l15;
            xres[t] = *(const GAS u32x2*)(x + (size_t)tok * 512 + n0 + 4 * g4);
            gres[t] = u2f(__hip_atomic_load((GAS unsigned*)(ws + WS_GATE) + tok, RLX_AGENT));
        }
        unsigned wso = WS_WOF + (cg * 8 + w) * 16 * 1024;
        asm volatile("buffer_load_dwordx4 v[104:107], %[vo], s[80:83], %[so] offen offset:0 sc1\n\tbuffer_load_dwordx4 v[108:111], %[vo], s[80:83], %[so] offen offset:1024 sc1\n\tbuffer_load_dwordx4 v[112:115], %[vo], s[80:83], %[so] offen offset:2048 sc1\n\tbuffer_load_dwordx4 v[116:119], %[vo], s[80:83], %[so] offen offset:3072 sc1\n\ts_add_u32 %[so], %[so], 4096\n\tbuffer_load_dwordx4 v[120:123], %[vo], s[80:83], %[so] offen offset:0 sc1\n\tbuffer_load_dwordx4 v[124:127], %[vo], s[80:83], %[so] offen offset:1024 sc1\n\tbuffer_load_dwordx4 v[128:131], %[vo], s[80:83], %[so] offen offset:2048 sc1\n\tbuffer_load_dwordx4 v[132:135], %[vo], s[80:83], %[so] offen offset:3072 sc1\n\ts_add_u32 %[so], %[so], 4096\n\tbuffer_load_dwordx4 v[136:139], %[vo], s[80:83], %[so] offen offset:0 sc1\n\tbuffer_load_dwordx4 v[140:143], %[vo], s[80:83], %[so] offen offset:1024 sc1\n\tbuffer_load_dwordx4 v[144:147], %[vo], s[80:83], %[so] offen offset:2048 sc1\n\tbuffer_load_dwordx4 v[148:151], %[vo], s[80:83], %[so] offen offset:3072 sc1\n\ts_add_u32 %[so], %[so], 4096\n\tbuffer_load_dwordx4 v[152:155], %[vo], s[80:83], %[so] offen offset:0 sc1\n\tbuffer_load_dwordx4 v[156:159], %[vo], s[80:83], %[so] offen offset:1024 sc1\n\tbuffer_load_dwordx4 v[160:163], %[vo], s[80:83], %[so] offen offset:2048 sc1\n\tbuffer_load_dwordx4 v[164:167], %[vo], s[80:83], %[so] offen offset:3072 sc1\n\ts_waitcnt vmcnt(0)" : "=&{v[104:119]}"(W0), "=&{v[120:135]}"(W1), "=&{v[136:151]}"(W2), "=&{v[152:167]}"(W3), [so] "+s"(wso)
                     : [vo] "v"(l * 16), "{s[80:83]}"(srd) : "scc", "memory");
        asm volatile("" : "+v"(xres[0]), "+v"(xres[1]), "+v"(gres[0]), "+v"(gres[1]));
        __builtin_amdgcn_s_setprio(0);
        ST(8);
    }
    const int tho = hg >> 2, cg = hg & 3;
    const int n0 = cg * 128 + 16 * w;
    const unsigned rkey = __builtin_amdgcn_readfirstlane(*(LAS unsigned*)(L + L_KEY));
    const int idx = 2047 - (int)(rkey & 0x7ffu);
    const bool has_idx = (idx >> 5) == tb * 2 + tho;
    u32x4 yg = {0u, tag, 0u, tag};
    const unsigned yoff = ((l >> 3) * 512 + n0 + 2 * (l & 7)) * 8;
#define YGB 0
    if (has_idx) yg = __builtin_amdgcn_raw_buffer_load_b128(rs, yoff, WS_YG, 16);
    ST(21);
    BARRIER();
    ST(9); AP(9);
    float t2[2][4];
    unsigned oofs;
    unsigned opj;
#define TQSV
#define TQSC
    {
        u32x4 f[4];
        const unsigned src = WS_OF + ((tb * 2 + tho) * 32 + 4 * w) * 1024;
        if (FPOS == 0) {
            if (FSLEEP) __builtin_amdgcn_s_sleep(FSLEEP);
            #pragma unroll
            for (int k = 0; k < 4; ++k) f[k] = __builtin_amdgcn_raw_buffer_load_b128(rs, l * 16, src + k * 1024, 16);
        }
        __builtin_amdgcn_sched_barrier(0);
        float yv[4] = {0.f, 0.f, 0.f, 0.f};
        if (has_idx) {
            _Pragma("nounroll") for (int i = 0; i < SPIN_CAP; ++i) {
                if (__all(yg[1] == tag && yg[3] == tag)) break;
                FENCE();
                yg = __builtin_amdgcn_raw_buffer_load_b128(rs, yoff, WS_YG, 16);
            }
            float y0 = u2f(yg[0]), y1 = u2f(yg[2]);
            #pragma unroll
            for (int o = 8; o < 64; o <<= 1) { y0 += __shfl_xor(y0, o); y1 += __shfl_xor(y1, o); }
            unsigned ord = rkey >> 16;
            unsigned rb = (ord & 0x8000u) ? (ord & 0x7fffu) : (~ord & 0xffffu);
            const float rsel = bfr(sigmoidf_(bflo(rb)));
            yv[0] = bfr(bfr(__shfl(y0, 2 * g4)) * rsel);     yv[1] = bfr(bfr(__shfl(y1, 2 * g4)) * rsel);
            yv[2] = bfr(bfr(__shfl(y0, 2 * g4 + 1)) * rsel); yv[3] = bfr(bfr(__shfl(y1, 2 * g4 + 1)) * rsel);
        }
        #pragma unroll
        for (int t = 0; t < 2; ++t) {
            const int tok = tb * 64 + tho * 32 + 16 * t + l15;
            const float omg = bfr(1.f - gres[t]);
            const float bx[4] = {bflo(xres[t][0]), bfhi(xres[t][0]), bflo(xres[t][1]), bfhi(xres[t][1])};
            #pragma unroll
            for (int i = 0; i < 4; ++i) t2[t][i] = bfr(omg * ((has_idx && tok == idx) ? bfr(bx[i] + yv[i]) : bx[i]));
        }
        asm volatile("" : "+v"(t2[0][0]), "+v"(t2[0][1]), "+v"(t2[0][2]), "+v"(t2[0][3]), "+v"(t2[1][0]), "+v"(t2[1][1]), "+v"(t2[1][2]), "+v"(t2[1][3]));
        oofs = (tb * 64 + tho * 32 + l15) * 1024 + (n0 + 4 * g4) * 2;
        asm volatile("" : "+v"(oofs));
        __builtin_amdgcn_sched_barrier(0);
        opj = LB + (g4 >> 1) * 1024 + (g4 & 1) * 512 + l15 * 16;
        unsigned left;
        asm volatile("" : "+v"(opj));
        ST(43);
        asm volatile("s_sleep 4\n\tbuffer_load_dwordx4 v[168:171], %[vo], s[80:83], %[so] offen offset:0 sc1\n\tbuffer_load_dwordx4 v[172:175], %[vo], s[80:83], %[so] offen offset:1024 sc1\n\tbuffer_load_dwordx4 v[176:179], %[vo], s[80:83], %[so] offen offset:2048 sc1\n\tbuffer_load_dwordx4 v[180:183], %[vo], s[80:83], %[so] offen offset:3072 sc1\n\ts_mov_b32 s78, 0x40000\n\tMQA_PSL:\n\ts_waitcnt vmcnt(3)\n\tds_write_b128 %[wa], v[168:171] offset:0\n\tv_max3_u32 v184, v168, v169, v170\n\ts_waitcnt vmcnt(2)\n\tds_write_b128 %[wa], v[172:175] offset:1024\n\tv_max3_u32 v184, v184, v171, v172\n\tv_max3_u32 v184, v184, v173, v174\n\ts_waitcnt vmcnt(1)\n\tds_write_b128 %[wa], v[176:179] offset:2048\n\tv_max3_u32 v184, v184, v175, v176\n\tv_max3_u32 v184, v184, v177, v178\n\ts_waitcnt vmcnt(0)\n\tds_write_b128 %[wa], v[180:183] offset:3072\n\tv_max3_u32 v184, v184, v179, v180\n\tv_max3_u32 v184, v184, v181, v182\n\tv_max_u32 v184, v184, v183\n\tv_cmp_eq_u32 vcc, -1, v184\n\ts_cmp_eq_u64 vcc, 0\n\ts_cbranch_scc1 MQA_PSD\n\ts_sub_u32 s78, s78, 1\n\ts_cmp_eq_u32 s78, 0\n\ts_cbranch_scc1 MQA_PSD\n\ts_waitcnt lgkmcnt(0)\n\tv_max3_u32 v184, v168, v169, v170\n\tv_max_u32 v184, v184, v171\n\tv_cmp_eq_u32 vcc, -1, v184\n\ts_cmp_eq_u64 vcc, 0\n\ts_cbranch_scc1 MQA_PSK0\n\tbuffer_load_dwordx4 v[168:171], %[vo], s[80:83], %[so] offen offset:0 sc1\n\tMQA_PSK0:\n\tv_max3_u32 v184, v172, v173, v174\n\tv_max_u32 v184, v184, v175\n\tv_cmp_eq_u32 vcc, -1, v184\n\ts_cmp_eq_u64 vcc, 0\n\ts_cbranch_scc1 MQA_PSK1\n\tbuffer_load_dwordx4 v[172:175], %[vo], s[80:83], %[so] offen offset:1024 sc1\n\tMQA_PSK1:\n\tv_max3_u32 v184, v176, v177, v178\n\tv_max_u32 v184, v184, v179\n\tv_cmp_eq_u32 vcc, -1, v184\n\ts_cmp_eq_u64 vcc, 0\n\ts_cbranch_scc1 MQA_PSK2\n\tbuffer_load_dwordx4 v[176:179], %[vo], s[80:83], %[so] offen offset:2048 sc1\n\tMQA_PSK2:\n\tv_max3_u32 v184, v180, v181, v182\n\tv_max_u32 v184, v184, v183\n\tv_cmp_eq_u32 vcc, -1, v184\n\ts_cmp_eq_u64 vcc, 0\n\ts_cbranch_scc1 MQA_PSK3\n\tbuffer_load_dwordx4 v[180:183], %[vo], s[80:83], %[so] offen offset:3072 sc1\n\tMQA_PSK3:\n\ts_waitcnt vmcnt(0)\n\ts_branch MQA_PSL\n\tMQA_PSD:\n\ts_waitcnt lgkmcnt(0)\n\ts_barrier" : "=&{s78}"(left) : [vo] "v"(l * 16), [so] "s"(src), [wa] "v"(LB + 4096 * w + l * 16), "{s[80:83]}"(srd)
                     : "v168", "v169", "v170", "v171", "v172", "v173", "v174", "v175", "v176", "v177", "v178", "v179", "v180", "v181", "v182", "v183", "v184", "vcc", "scc", "memory");
        ST(10); AP(10);
    }
    ST(26);
    f32x4 oa[2];
    asm volatile("ds_read_b128 v[40:43], %[op] offset:0\n\tds_read_b128 v[44:47], %[op] offset:256\n\tds_read_b128 v[48:51], %[op] offset:2048\n\tds_read_b128 v[52:55], %[op] offset:2304\n\tds_read_b128 v[56:59], %[op] offset:4096\n\tds_read_b128 v[60:63], %[op] offset:4352\n\tds_read_b128 v[64:67], %[op] offset:6144\n\tds_read_b128 v[68:71], %[op] offset:6400\n\tds_read_b128 v[72:75], %[op] offset:8192\n\tds_read_b128 v[76:79], %[op] offset:8448\n\tds_read_b128 v[80:83], %[op] offset:10240\n\tds_read_b128 v[84:87], %[op] offset:10496\n\ts_waitcnt lgkmcnt(11)\n\tv_mfma_f32_16x16x32_bf16 v[32:35], v[104:107], v[40:43], 0\n\ts_waitcnt lgkmcnt(10)\n\tv_mfma_f32_16x16x32_bf16 v[36:39], v[104:107], v[44:47], 0\n\tds_read_b128 v[40:43], %[op] offset:12288\n\tds_read_b128 v[44:47], %[op] offset:12544\n\ts_waitcnt lgkmcnt(11)\n\tv_mfma_f32_16x16x32_bf16 v[32:35], v[108:111], v[48:51], v[32:35]\n\ts_waitcnt lgkmcnt(10)\n\tv_mfma_f32_16x16x32_bf16 v[36:39], v[108:111], v[52:55], v[36:39]\n\tds_read_b128 v[48:51], %[op] offset:14336\n\tds_read_b128 v[52:55], %[op] offset:14592\n\ts_waitcnt lgkmcnt(11)\n\tv_mfma_f32_16x16x32_bf16 v[32:35], v[112:115], v[56:59], v[32:35]\n\ts_waitcnt lgkmcnt(10)\n\tv_mfma_f32_16x16x32_bf16 v[36:39], v[112:115], v[60:63], v[36:39]\n\tds_read_b128 v[56:59], %[op] offset:16384\n\tds_read_b128 v[60:63], %[op] offset:16640\n\ts_waitcnt lgkmcnt(11)\n\tv_mfma_f32_16x16x32_bf16 v[32:35], v[116:119], v[64:67], v[32:35]\n\ts_waitcnt lgkmcnt(10)\n\tv_mfma_f32_16x16x32_bf16 v[36:39], v[116:119], v[68:71], v[36:39]\n\tds_read_b128 v[64:67], %[op] offset:18432\n\tds_read_b128 v[68:71], %[op] offset:18688\n\ts_waitcnt lgkmcnt(11)\n\tv_mfma_f32_16x16x32_bf16 v[32:35], v[120:123], v[72:75], v[32:35]\n\ts_waitcnt lgkmcnt(10)\n\tv_mfma_f32_16x16x32_bf16 v[36:39], v[120:123], v[76:79], v[36:39]\n\tds_read_b128 v[72:75], %[op] offset:20480\n\tds_read_b128 v[76:79], %[op] offset:20736\n\ts_waitcnt lgkmcnt(11)\n\tv_mfma_f32_16x16x32_bf16 v[32:35], v[124:127], v[80:83], v[32:35]\n\ts_waitcnt lgkmcnt(10)\n\tv_mfma_f32_16x16x32_bf16 v[36:39], v[124:127], v[84:87], v[36:39]\n\tds_read_b128 v[80:83], %[op] offset:22528\n\tds_read_b128 v[84:87], %[op] offset:22784\n\ts_waitcnt lgkmcnt(11)\n\tv_mfma_f32_16x16x32_bf16 v[32:35], v[128:131], v[40:43], v[32:35]\n\ts_waitcnt lgkmcnt(10)\n\tv_mfma_f32_16x16x32_bf16 v[36:39], v[128:131], v[44:47], v[36:39]\n\tds_read_b128 v[40:43], %[op] offset:24576\n\tds_read_b128 v[44:47], %[op] offset:24832\n\ts_waitcnt lgkmcnt(11)\n\tv_mfma_f32_16x16x32_bf16 v[32:35], v[132:135], v[48:51], v[32:35]\n\ts_waitcnt lgkmcnt(10)\n\tv_mfma_f32_16x16x32_bf16 v[36:39], v[132:135], v[52:55], v[36:39]\n\tds_read_b128 v[48:51], %[op] offset:26624\n\tds_read_b128 v[52:55], %[op] offset:26880\n\ts_waitcnt lgkmcnt(11)\n\tv_mfma_f32_16x16x32_bf16 v[32:35], v[136:139], v[56:59], v[32:35]\n\ts_waitcnt lgkmcnt(10)\n\tv_mfma_f32_16x16x32_bf16 v[36:39], v[136:139], v[60:63], v[36:39]\n\tds_read_b128 v[56:59], %[op] offset:28672\n\tds_read_b128 v[60:63], %[op] offset:28928\n\ts_waitcnt lgkmcnt(11)\n\tv_mfma_f32_16x16x32_bf16 v[32:35], v[140:143], v[64:67], v[32:35]\n\ts_waitcnt lgkmcnt(10)\n\tv_mfma_f32_16x16x32_bf16 v[36:39], v[140:143], v[68:71], v[36:39]\n\tds_read_b128 v[64:67], %[op] offset:30720\n\tds_read_b128 v[68:71], %[op] offset:30976\n\ts_waitcnt lgkmcnt(11)\n\tv_mfma_f32_16x16x32_bf16 v[32:35], v[144:147], v[72:75], v[32:35]\n\ts_waitcnt lgkmcnt(10)\n\tv_mfma_f32_16x16x32_bf16 v[36:39], v[144:147], v[76:79], v[36:39]\n\ts_waitcnt lgkmcnt(9)\n\tv_mfma_f32_16x16x32_bf16 v[32:35], v[148:151], v[80:83], v[32:35]\n\ts_waitcnt lgkmcnt(8)\n\tv_mfma_f32_16x16x32_bf16 v[36:39], v[148:151], v[84:87], v[36:39]\n\ts_waitcnt lgkmcnt(7)\n\tv_mfma_f32_16x16x32_bf16 v[32:35], v[152:155], v[40:43], v[32:35]\n\ts_waitcnt lgkmcnt(6)\n\tv_mfma_f32_16x16x32_bf16 v[36:39], v[152:155], v[44:47], v[36:39]\n\ts_waitcnt lgkmcnt(5)\n\tv_mfma_f32_16x16x32_bf16 v[32:35], v[156:159], v[48:51], v[32:35]\n\ts_waitcnt lgkmcnt(4)\n\tv_mfma_f32_16x16x32_bf16 v[36:39], v[156:159], v[52:55], v[36:39]\n\ts_waitcnt lgkmcnt(3)\n\tv_mfma_f32_16x16x32_bf16 v[32:35], v[160:163], v[56:59], v[32:35]\n\ts_waitcnt lgkmcnt(2)\n\tv_mfma_f32_16x16x32_bf16 v[36:39], v[160:163], v[60:63], v[36:39]\n\ts_waitcnt lgkmcnt(1)\n\tv_mfma_f32_16x16x32_bf16 v[32:35], v[164:167], v[64:67], v[32:35]\n\ts_waitcnt lgkmcnt(0)\n\tv_mfma_f32_16x16x32_bf16 v[36:39], v[164:167], v[68:71], v[36:39]\n\ts_nop 7\n\ts_nop 1" : "=&{v[32:35]}"(oa[0]), "=&{v[36:39]}"(oa[1]) : [op] "v"(opj), "{v[104:119]}"(W0), "{v[120:135]}"(W1), "{v[136:151]}"(W2), "{v[152:167]}"(W3) : "v40", "v41", "v42", "v43", "v44", "v45", "v46", "v47", "v48", "v49", "v50", "v51", "v52", "v53", "v54", "v55", "v56", "v57", "v58", "v59", "v60", "v61", "v62", "v63", "v64", "v65", "v66", "v67", "v68", "v69", "v70", "v71", "v72", "v73", "v74", "v75", "v76", "v77", "v78", "v79", "v80", "v81", "v82", "v83", "v84", "v85", "v86", "v87", "memory");
#if !(TAILV & 8) && !PSV
    ST(10); AP(10);
    BARRIER();
    ST(26);
    f32x4 oa[2];
    {
        const unsigned op = LB + (g4 >> 1) * 1024 + (g4 & 1) * 512 + l15 * 16;
        asm volatile("ds_read_b128 v[40:43], %[op] offset:0\n\tds_read_b128 v[44:47], %[op] offset:256\n\tds_read_b128 v[48:51], %[op] offset:2048\n\tds_read_b128 v[52:55], %[op] offset:2304\n\tds_read_b128 v[56:59], %[op] offset:4096\n\tds_read_b128 v[60:63], %[op] offset:4352\n\tds_read_b128 v[64:67], %[op] offset:6144\n\tds_read_b128 v[68:71], %[op] offset:6400\n\tds_read_b128 v[72:75], %[op] offset:8192\n\tds_read_b128 v[76:79], %[op] offset:8448\n\tds_read_b128 v[80:83], %[op] offset:10240\n\tds_read_b128 v[84:87], %[op] offset:10496\n\ts_waitcnt lgkmcnt(11)\n\tv_mfma_f32_16x16x32_bf16 v[32:35], v[104:107], v[40:43], 0\n\ts_waitcnt lgkmcnt(10)\n\tv_mfma_f32_16x16x32_bf16 v[36:39], v[104:107], v[44:47], 0\n\tds_read_b128 v[40:43], %[op] offset:12288\n\tds_read_b128 v[44:47], %[op] offset:12544\n\ts_waitcnt lgkmcnt(11)\n\tv_mfma_f32_16x16x32_bf16 v[32:35], v[108:111], v[48:51], v[32:35]\n\ts_waitcnt lgkmcnt(10)\n\tv_mfma_f32_16x16x32_bf16 v[36:39], v[108:111], v[52:55], v[36:39]\n\tds_read_b128 v[48:51], %[op] offset:14336\n\tds_read_b128 v[52:55], %[op] offset:14592\n\ts_waitcnt lgkmcnt(11)\n\tv_mfma_f32_16x16x32_bf16 v[32:35], v[112:115], v[56:59], v[32:35]\n\ts_waitcnt lgkmcnt(10)\n\tv_mfma_f32_16x16x32_bf16 v[36:39], v[112:115], v[60:63], v[36:39]\n\tds_read_b128 v[56:59], %[op] offset:16384\n\tds_read_b128 v[60:63], %[op] offset:16640\n\ts_waitcnt lgkmcnt(11)\n\tv_mfma_f32_16x16x32_bf16 v[32:35], v[116:119], v[64:67], v[32:35]\n\ts_waitcnt lgkmcnt(10)\n\tv_mfma_f32_16x16x32_bf16 v[36:39], v[116:119], v[68:71], v[36:39]\n\tds_read_b128 v[64:67], %[op] offset:18432\n\tds_read_b128 v[68:71], %[op] offset:18688\n\ts_waitcnt lgkmcnt(11)\n\tv_mfma_f32_16x16x32_bf16 v[32:35], v[120:123], v[72:75], v[32:35]\n\ts_waitcnt lgkmcnt(10)\n\tv_mfma_f32_16x16x32_bf16 v[36:39], v[120:123], v[76:79], v[36:39]\n\tds_read_b128 v[72:75], %[op] offset:20480\n\tds_read_b128 v[76:79], %[op] offset:20736\n\ts_waitcnt lgkmcnt(11)\n\tv_mfma_f32_16x16x32_bf16 v[32:35], v[124:127], v[80:83], v[32:35]\n\ts_waitcnt lgkmcnt(10)\n\tv_mfma_f32_16x16x32_bf16 v[36:39], v[124:127], v[84:87], v[36:39]\n\tds_read_b128 v[80:83], %[op] offset:22528\n\tds_read_b128 v[84:87], %[op] offset:22784\n\ts_waitcnt lgkmcnt(11)\n\tv_mfma_f32_16x16x32_bf16 v[32:35], v[128:131], v[40:43], v[32:35]\n\ts_waitcnt lgkmcnt(10)\n\tv_mfma_f32_16x16x32_bf16 v[36:39], v[128:131], v[44:47], v[36:39]\n\tds_read_b128 v[40:43], %[op] offset:24576\n\tds_read_b128 v[44:47], %[op] offset:24832\n\ts_waitcnt lgkmcnt(11)\n\tv_mfma_f32_16x16x32_bf16 v[32:35], v[132:135], v[48:51], v[32:35]\n\ts_waitcnt lgkmcnt(10)\n\tv_mfma_f32_16x16x32_bf16 v[36:39], v[132:135], v[52:55], v[36:39]\n\tds_read_b128 v[48:51], %[op] offset:26624\n\tds_read_b128 v[52:55], %[op] offset:26880\n\ts_waitcnt lgkmcnt(11)\n\tv_mfma_f32_16x16x32_bf16 v[32:35], v[136:139], v[56:59], v[32:35]\n\ts_waitcnt lgkmcnt(10)\n\tv_mfma_f32_16x16x32_bf16 v[36:39], v[136:139], v[60:63], v[36:39]\n\tds_read_b128 v[56:59], %[op] offset:28672\n\tds_read_b128 v[60:63], %[op] offset:28928\n\ts_waitcnt lgkmcnt(11)\n\tv_mfma_f32_16x16x32_bf16 v[32:35], v[140:143], v[64:67], v[32:35]\n\ts_waitcnt lgkmcnt(10)\n\tv_mfma_f32_16x16x32_bf16 v[36:39], v[140:143], v[68:71], v[36:39]\n\tds_read_b128 v[64:67], %[op] offset:30720\n\tds_read_b128 v[68:71], %[op] offset:30976\n\ts_waitcnt lgkmcnt(11)\n\tv_mfma_f32_16x16x32_bf16 v[32:35], v[144:147], v[72:75], v[32:35]\n\ts_waitcnt lgkmcnt(10)\n\tv_mfma_f32_16x16x32_bf16 v[36:39], v[144:147], v[76:79], v[36:39]\n\ts_waitcnt lgkmcnt(9)\n\tv_mfma_f32_16x16x32_bf16 v[32:35], v[148:151], v[80:83], v[32:35]\n\ts_waitcnt lgkmcnt(8)\n\tv_mfma_f32_16x16x32_bf16 v[36:39], v[148:151], v[84:87], v[36:39]\n\ts_waitcnt lgkmcnt(7)\n\tv_mfma_f32_16x16x32_bf16 v[32:35], v[152:155], v[40:43], v[32:35]\n\ts_waitcnt lgkmcnt(6)\n\tv_mfma_f32_16x16x32_bf16 v[36:39], v[152:155], v[44:47], v[36:39]\n\ts_waitcnt lgkmcnt(5)\n\tv_mfma_f32_16x16x32_bf16 v[32:35], v[156:159], v[48:51], v[32:35]\n\ts_waitcnt lgkmcnt(4)\n\tv_mfma_f32_16x16x32_bf16 v[36:39], v[156:159], v[52:55], v[36:39]\n\ts_waitcnt lgkmcnt(3)\n\tv_mfma_f32_16x16x32_bf16 v[32:35], v[160:163], v[56:59], v[32:35]\n\ts_waitcnt lgkmcnt(2)\n\tv_mfma_f32_16x16x32_bf16 v[36:39], v[160:163], v[60:63], v[36:39]\n\ts_waitcnt lgkmcnt(1)\n\tv_mfma_f32_16x16x32_bf16 v[32:35], v[164:167], v[64:67], v[32:35]\n\ts_waitcnt lgkmcnt(0)\n\tv_mfma_f32_16x16x32_bf16 v[36:39], v[164:167], v[68:71], v[36:39]\n\ts_nop 7\n\ts_nop 1" : "=&{v[32:35]}"(oa[0]), "=&{v[36:39]}"(oa[1]) : [op] "v"(op), "{v[104:119]}"(W0), "{v[120:135]}"(W1), "{v[136:151]}"(W2), "{v[152:167]}"(W3) : "v40", "v41", "v42", "v43", "v44", "v45", "v46", "v47", "v48", "v49", "v50", "v51", "v52", "v53", "v54", "v55", "v56", "v57", "v58", "v59", "v60", "v61", "v62", "v63", "v64", "v65", "v66", "v67", "v68", "v69", "v70", "v71", "v72", "v73", "v74", "v75", "v76", "v77", "v78", "v79", "v80", "v81", "v82", "v83", "v84", "v85", "v86", "v87", "memory");
    }
#endif
    ST(27);
    #pragma unroll
    for (int t = 0; t < 2; ++t) {
        u32x2 o2;
        #pragma unroll
        for (int i = 0; i < 2; ++i) {
            const unsigned a = pk2(oa[t][2 * i], oa[t][2 * i + 1]);
            const f32x2 m = f32x2{bflo(a), bfhi(a)} * gres[t];
            const unsigned c = pk2(m[0], m[1]);
            const f32x2 r = f32x2{bflo(c), bfhi(c)} + f32x2{t2[t][2 * i], t2[t][2 * i + 1]};
            o2[i] = pk2(r[0], r[1]);
        }
        __builtin_amdgcn_raw_buffer_store_b64(o2, ro, oofs + t * 16384, 0, OUTAUX);
    }
    ST(11); SC(53); AP(11);
    asm volatile("s_endpgm\n\tMQA_SP1:\n\ts_mov_b32 s79, 0x40000\n\tMQA_SL1:\n\ts_sleep 1\n\tds_read_b32 v197, v198 offset:4\n\ts_sub_u32 s79, s79, 1\n\ts_waitcnt lgkmcnt(0)\n\tv_readfirstlane_b32 s78, v197\n\ts_cmp_eq_u32 s79, 0\n\ts_cbranch_scc1 MQA_BK1\n\ts_cmp_ge_u32 s78, 4\n\ts_cbranch_scc0 MQA_SL1\n\ts_branch MQA_BK1\n\tMQA_SP2:\n\ts_mov_b32 s79, 0x40000\n\tMQA_SL2:\n\ts_sleep 1\n\tds_read_b32 v197, v198 offset:24\n\ts_sub_u32 s79, s79, 1\n\ts_waitcnt lgkmcnt(0)\n\tv_readfirstlane_b32 s78, v197\n\ts_cmp_eq_u32 s79, 0\n\ts_cbranch_scc1 MQA_BK2\n\ts_cmp_ge_u32 s78, 4\n\ts_cbranch_scc0 MQA_SL2\n\ts_branch MQA_BK2\n\tMQA_SP3:\n\ts_mov_b32 s79, 0x40000\n\tMQA_SL3:\n\ts_sleep 1\n\tds_read_b32 v197, v198 offset:40\n\ts_sub_u32 s79, s79, 1\n\ts_waitcnt lgkmcnt(0)\n\tv_readfirstlane_b32 s78, v197\n\ts_cmp_eq_u32 s79, 0\n\ts_cbranch_scc1 MQA_BK3\n\ts_cmp_ge_u32 s78, 20\n\ts_cbranch_scc0 MQA_SL3\n\ts_branch MQA_BK3\n\tMQA_END:" ::: "memory");
}

extern "C" void kernel_launch(void* const* d_in, const int* in_sizes, int n_in, void* d_out, int out_size,
                              void* d_ws, size_t ws_size, hipStream_t stream) {
    (void)in_sizes; (void)n_in; (void)out_size; (void)ws_size;
    (void)hipFuncSetAttribute((const void*)mqa_moe_fused, hipFuncAttributeMaxDynamicSharedMemorySize, LDS_BYTES);
    mqa_moe_fused<<<256, 512, LDS_BYTES, stream>>>(
        (const u16*)d_in[0], (const u16*)d_in[1], (const u16*)d_in[2], (const u16*)d_in[3], (const u16*)d_in[4],
        (const u16*)d_in[6], (const u16*)d_in[7], (const u16*)d_in[8], (const u16*)d_in[9], (const u16*)d_in[10],
        (u16*)d_out, (char*)d_ws);
}
```
